```python
import torch

def _softmax(x, axis=-1):
    return torch.softmax(x, dim=axis)
def _gelu(x):
    return torch.nn.functional.gelu(x, approximate="tanh")
def _silu(x):
    return torch.nn.functional.silu(x)
def _sigmoid(x):
    return torch.sigmoid(x)
def _layernorm(x, axis=-1, eps=1e-5):
    m = x.mean(dim=axis, keepdim=True)
    v = x.var(dim=axis, keepdim=True, unbiased=False)
    return (x - m) / torch.sqrt(v + eps)
def _scatter_add(tgt, idx, src, axis=0):
    return tgt.clone().index_add_(axis, idx, src)
def _segment_sum(src, seg, n):
    out = torch.zeros((n,) + src.shape[1:], dtype=src.dtype)
    return out.index_add_(0, seg, src)
def _pad(x, pw):
    flat = tuple(v for pair in reversed(pw) for v in pair)
    return torch.nn.functional.pad(x, flat)

kernel_name = 'interleave_layers__linear_kernel_hedgehog__dense_activation_silu__prompt'

def setup_inputs():

    _w = (lambda a: (lambda t: t.to(torch.bfloat16) if t.is_floating_point() else t)(torch.as_tensor(a)))
    def _A_setup():
        B, S, D, H = 1, 2048, 512, 32
        Dh = D // H
        return {
            "x": _w((lambda *s: torch.randn(*s, dtype=torch.bfloat16) * 0.05)(B, S, D)),
            "wqkv": _w((lambda *s: torch.randn(*s, dtype=torch.bfloat16) * 0.4)(D, 3 * D)),
            "w_phi": _w((lambda *s: torch.randn(*s, dtype=torch.bfloat16) * 0.4)(H, Dh, Dh) * (Dh ** -0.5)),
            "wo": _w((lambda *s: torch.randn(*s, dtype=torch.bfloat16) * 0.05)(D, D)),
        }
    _A_in = _A_setup()

    def _B_setup():
        B, S, D, F = 1, 2048, 512, 1792
        return {
            "x": _w((lambda *s: torch.randn(*s, dtype=torch.bfloat16) * 0.05)(B, S, D)),
            "w1": _w((lambda *s: torch.randn(*s, dtype=torch.bfloat16) * 1.6)(D, F) * (D ** -0.5)),
            "w2": _w((lambda *s: torch.randn(*s, dtype=torch.bfloat16) * 1.6)(F, D) * (F ** -0.5)),
        }
    _B_in = _B_setup()

    return {
        "A_x": _A_in["x"],
        "A_wqkv": _A_in["wqkv"],
        "A_w_phi": _A_in["w_phi"],
        "A_wo": _A_in["wo"],
        "B_x": _B_in["x"],
        "B_w1": _B_in["w1"],
        "B_w2": _B_in["w2"],
    }

def reference(A_x, A_wqkv, A_w_phi, A_wo, B_x, B_w1, B_w2):
    def _A_ref(x, wqkv, w_phi, wo):
        B, S, D = x.shape
        H = 32; Dh = D // H
        qkv = (lambda x, *d: x.permute(*d))((x @ wqkv).reshape(B, S, 3, H, Dh), 2, 0, 3, 1, 4)
        q, k, v = qkv[0], qkv[1], qkv[2]
        pq = torch.einsum("bhsd,hdf->bhsf", q, w_phi)
        pk = torch.einsum("bhsd,hdf->bhsf", k, w_phi)
        phi_q = torch.concatenate([_softmax(pq, axis=-1), _softmax(-pq, axis=-1)], axis=-1)
        phi_k = torch.concatenate([_softmax(pk, axis=-1), _softmax(-pk, axis=-1)], axis=-1)
        kv = torch.einsum("bhsf,bhsd->bhfd", phi_k, v)
        z  = torch.einsum("bhsf->bhf", phi_k)
        num = torch.einsum("bhqf,bhfd->bhqd", phi_q, kv)
        den = torch.einsum("bhqf,bhf->bhq", phi_q, z)[..., None] + 1e-6
        o = num / den
        return (lambda x, *d: x.permute(*d))(o, 0, 2, 1, 3).reshape(B, S, D) @ wo

    def _B_ref(x, w1, w2):
        h = torch.einsum("bsd,df->bsf", x, w1)
        h = _silu(h)
        out = torch.einsum("bsf,fd->bsd", h, w2)
        return out

    x = A_x
    x = x + _A_ref(_layernorm(x, axis=-1), A_wqkv, A_w_phi, A_wo)
    x = x + _B_ref(_layernorm(x, axis=-1), B_w1, B_w2)
    x = x + _A_ref(_layernorm(x, axis=-1), A_wqkv, A_w_phi, A_wo)
    x = x + _B_ref(_layernorm(x, axis=-1), B_w1, B_w2)
    return x
```

```cpp
#include <hip/hip_runtime.h>
#include <hip/hip_bf16.h>
#include <stdint.h>

typedef unsigned int u32;
typedef u32 v4u __attribute__((ext_vector_type(4)));
typedef u32 v2u __attribute__((ext_vector_type(2)));
typedef short s16x4 __attribute__((ext_vector_type(4)));
typedef __bf16 bf16x8 __attribute__((ext_vector_type(8)));
typedef __bf16 bf16x2 __attribute__((ext_vector_type(2)));
typedef float f32x2 __attribute__((ext_vector_type(2)));
typedef float f32x4 __attribute__((ext_vector_type(4)));
typedef float f32x16 __attribute__((ext_vector_type(16)));
#define LAS __attribute__((address_space(3)))
#define GAS __attribute__((address_space(1)))
#define DI __device__ __forceinline__
typedef __amdgpu_buffer_rsrc_t rsrc_t;

constexpr int S = 2048, D = 512, NH = 32, FF = 1792;
constexpr u32 CTRL = 0;
constexpr u32 WQKV_T = 65536;
constexpr u32 WPHI_T = WQKV_T + 16 * 98304;
constexpr u32 WO_T = WPHI_T + 32768;
constexpr u32 W1_T = WO_T + 524288;
constexpr u32 W2_T = W1_T + 16 * 114688;
constexpr u32 CS_QKV = W2_T + 8 * 4 * 28 * 2048;
constexpr u32 CS_W1 = CS_QKV + 8192;
constexpr u32 XT = CS_W1 + 8192;
constexpr u32 XT_SZ = 2097152;
constexpr u32 ST = XT + 4 * XT_SZ;
constexpr u32 ST_SZ = 262144;
constexpr u32 PHIQ = ST + 4 * ST_SZ;
constexpr u32 PHIQ_SZ = 4194304;
constexpr u32 HB = PHIQ + 2 * PHIQ_SZ;
constexpr u32 HB_SZ = 64 * 114688;
constexpr u32 KVP = HB + 2 * HB_SZ;
constexpr u32 KVP_SZ = 64 * 16 * 1088;
constexpr u32 KVF = KVP + 2 * KVP_SZ;
constexpr u32 KVF_SZ = 65536;
constexpr u32 WS_END = KVF + 2 * KVF_SZ;

constexpr float LOG2E = 1.4426950408889634f;

DI u32 pk(float a, float b) {
  f32x2 v = {a, b};
  bf16x2 r = __builtin_convertvector(v, bf16x2);
  return __builtin_bit_cast(u32, r);
}
DI float bflo(u32 u) { return __builtin_bit_cast(float, u << 16); }
DI float bfhi(u32 u) { return __builtin_bit_cast(float, u & 0xffff0000u); }
DI f32x16 mfma32(v4u a, v4u b, f32x16 c) {
  return __builtin_amdgcn_mfma_f32_32x32x16_bf16(__builtin_bit_cast(bf16x8, a), __builtin_bit_cast(bf16x8, b), c, 0, 0, 0);
}
DI f32x4 mfma16(v4u a, v4u b, f32x4 c) {
  return __builtin_amdgcn_mfma_f32_16x16x32_bf16(__builtin_bit_cast(bf16x8, a), __builtin_bit_cast(bf16x8, b), c, 0, 0, 0);
}
template <int AUX> DI v4u ld128(rsrc_t rs, u32 voff, u32 soff) { return __builtin_amdgcn_raw_buffer_load_b128(rs, voff, soff, AUX); }
template <int AUX> DI v2u ld64(rsrc_t rs, u32 voff, u32 soff) { return __builtin_amdgcn_raw_buffer_load_b64(rs, voff, soff, AUX); }
template <int AUX> DI void st128(v4u v, rsrc_t rs, u32 voff, u32 soff) { __builtin_amdgcn_raw_buffer_store_b128(v, rs, voff, soff, AUX); }
template <int AUX> DI void st64(v2u v, rsrc_t rs, u32 voff, u32 soff) { __builtin_amdgcn_raw_buffer_store_b64(v, rs, voff, soff, AUX); }
DI rsrc_t mkrsrc(const void* p, u32 bytes) { return __builtin_amdgcn_make_buffer_rsrc((void*)p, 0, (int)bytes, 0x00020000); }
DI float xhalf(float v) {
  u32 u = __builtin_bit_cast(u32, v);
  auto r = __builtin_amdgcn_permlane32_swap(u, u, false, false);
  return __builtin_bit_cast(float, (threadIdx.x & 32) ? r[0] : r[1]);
}
DI v2u trread(LAS unsigned char* p) {
  return __builtin_bit_cast(v2u, __builtin_amdgcn_ds_read_tr16_b64_v4i16((LAS s16x4*)p));
}

DI int rho32(int reg, int hf) { return (reg & 3) + 8 * (reg >> 2) + 4 * hf; }

__global__ void __launch_bounds__(256) k_prep(const uint16_t* __restrict__ x, const uint16_t* __restrict__ wqkv,
                                              const uint16_t* __restrict__ wphi, const uint16_t* __restrict__ wo,
                                              const uint16_t* __restrict__ w1, const uint16_t* __restrict__ w2,
                                              unsigned char* __restrict__ ws) {
  __shared__ float red[4][32];
  const int b = blockIdx.x, tid = threadIdx.x, lane = tid & 63, w = tid >> 6;
  if (b < 184) {
    const uint16_t* W;
    int ld, nb, kbase = 0, nkb = 64, kind;
    if (b < 48) { W = wqkv; ld = 1536; nb = b; kind = 0; }
    else if (b < 104) { W = w1; ld = 1792; nb = b - 48; kind = 1; }
    else if (b < 120) { W = wo; ld = 512; nb = b - 104; kind = 2; }
    else { W = w2; ld = 512; nb = (b - 120) & 15; kbase = ((b - 120) >> 4) * 448; nkb = 56; kind = 3; }
    const int kb = tid >> 2, nq = tid & 3;
    const bool act = kb < nkb;
    const int k0 = kbase + 8 * (act ? kb : 0), n0 = 32 * nb + 8 * nq;
    v4u R[8];
#pragma unroll
    for (int r = 0; r < 8; ++r) R[r] = *(const v4u*)(W + (size_t)(k0 + r) * ld + n0);
    const int kc = k0 >> 3;
    if (act) {
#pragma unroll
      for (int i = 0; i < 8; ++i) {
        v4u o;
#pragma unroll
        for (int m = 0; m < 4; ++m)
          o[m] = __builtin_amdgcn_perm(R[2 * m + 1][i >> 1], R[2 * m][i >> 1], (i & 1) ? 0x07060302u : 0x05040100u);
        const int n = n0 + i;
        u32 addr;
        if (kind == 0) {
          const int sec = nb >> 4, hp = nb & 15, tile = sec == 0 ? 2 : sec - 1, c = n & 31;
          addr = WQKV_T + hp * 98304 + tile * 32768 + (kc >> 1) * 1024 + ((kc & 1) * 32 + rho32(c & 15, c >> 4)) * 16;
        } else if (kind == 1) {
          const int fb = n / 112, c = n - fb * 112;
          int tile, rho;
          if (c < 96) { const int cc = c & 31; tile = 2 * (c >> 5) + ((cc >> 2) & 1); rho = 4 * (cc >> 3) + (cc & 3); }
          else { tile = 6; rho = c - 96; }
          addr = W1_T + fb * 114688 + ((kc >> 2) * 7 + tile) * 1024 + ((kc & 3) * 16 + rho) * 16;
        } else {
          const int c = n & 31, rho = rho32(8 * (c >> 4) + (c & 7), (c >> 3) & 1), ks = kc >> 1;
          if (kind == 2)
            addr = WO_T + ((n >> 7) * 32 + ks) * 4096 + ((n >> 5) & 3) * 1024 + ((kc & 1) * 32 + rho) * 16;
          else
            addr = W2_T + ((n >> 6) * 112 + ks) * 2048 + ((n >> 5) & 1) * 1024 + ((kc & 1) * 32 + rho) * 16;
        }
        *(v4u*)(ws + addr) = o;
      }
    }
    if (kind < 2) {
      float cs[8];
#pragma unroll
      for (int i = 0; i < 8; ++i) {
        float s = 0.f;
#pragma unroll
        for (int r = 0; r < 8; ++r) s += (i & 1) ? bfhi(R[r][i >> 1]) : bflo(R[r][i >> 1]);
        s += __shfl_xor(s, 4);
        s += __shfl_xor(s, 8);
        s += __shfl_xor(s, 16);
        s += __shfl_xor(s, 32);
        cs[i] = s;
      }
      if (lane < 4) {
#pragma unroll
        for (int i = 0; i < 8; ++i) red[w][8 * lane + i] = cs[i];
      }
      __syncthreads();
      if (tid < 32) {
        const float s = (red[0][tid] + red[1][tid]) + (red[2][tid] + red[3][tid]);
        ((float*)(ws + (kind == 0 ? CS_QKV : CS_W1)))[32 * nb + tid] = s;
      }
    }
  } else if (b < 248) {
    const int tt = b - 184;
    v4u R[8];
#pragma unroll
    for (int r = 0; r < 8; ++r) R[r] = *(const v4u*)(x + (size_t)(32 * tt + 8 * w + r) * 512 + 8 * lane);
#pragma unroll
    for (int r = 0; r < 8; ++r) {
      const int tok = 8 * w + r;
      *(v4u*)(ws + XT + tt * 32768 + lane * 512 + tok * 16) = R[r];
      float v[8];
#pragma unroll
      for (int m = 0; m < 4; ++m) { v[2 * m] = bflo(R[r][m]); v[2 * m + 1] = bfhi(R[r][m]); }
      float s = ((v[0] + v[1]) + (v[2] + v[3])) + ((v[4] + v[5]) + (v[6] + v[7]));
      s += __shfl_xor(s, 1);
      s += __shfl_xor(s, 2);
      const float mu = s * (1.f / 32.f);
      float q = 0.f;
#pragma unroll
      for (int m = 0; m < 8; ++m) { const float d = v[m] - mu; q += d * d; }
      q += __shfl_xor(q, 1);
      q += __shfl_xor(q, 2);
      if ((lane & 3) == 0) *(f32x2*)(ws + ST + tt * 4096 + (lane >> 2) * 256 + tok * 8) = f32x2{s, q};
    }
  } else {
    const int idx = (b - 248) * 256 + tid;
    const int l = idx & 63, s = (idx >> 6) & 1, hp = idx >> 7;
    const int rho = l & 31, hfp = l >> 5, hf = (rho >> 2) & 1, f = (rho & 3) + 4 * (rho >> 3);
    v4u o = {0u, 0u, 0u, 0u};
    if (hf == hfp) {
      const uint16_t* p = wphi + (2 * hp + hfp) * 256 + (8 * s) * 16 + f;
#pragma unroll
      for (int m = 0; m < 4; ++m) o[m] = (u32)p[(2 * m) * 16] | ((u32)p[(2 * m + 1) * 16] << 16);
    }
    *(v4u*)(ws + WPHI_T + idx * 16) = o;
  }
}

template <int N> DI void stat_merge(const f32x2 (&p)[N], float& sum, float& m2) {
  float s = 0.f, q = 0.f;
#pragma unroll
  for (int i = 0; i < N; ++i) { s += p[i].x; q += p[i].y; }
  const float mu = s * (1.f / N);
  float dv = 0.f;
#pragma unroll
  for (int i = 0; i < N; ++i) { const float d = p[i].x - mu; dv += d * d; }
  sum = s;
  m2 = q + dv * (1.f / 32.f);
}
DI void stat_merge2(float& sum, float& m2, float sum2, float m22, float inv2n) {
  const float d = sum - sum2;
  m2 = m2 + m22 + d * d * inv2n;
  sum = sum + sum2;
}

DI void dual_softmax(const f32x16& p, float c, u32 (&o)[16]) {
  float mx = p[0], mn = p[0];
#pragma unroll
  for (int i = 1; i < 16; ++i) { mx = fmaxf(mx, p[i]); mn = fminf(mn, p[i]); }
  const float ap = -mx * c, an = mn * c;
  float ep[16], en[16], sp = 0.f, sn = 0.f;
#pragma unroll
  for (int i = 0; i < 16; ++i) {
    ep[i] = __builtin_amdgcn_exp2f(__builtin_fmaf(p[i], c, ap));
    en[i] = __builtin_amdgcn_exp2f(__builtin_fmaf(p[i], -c, an));
    sp += ep[i];
    sn += en[i];
  }
  const float rp = __builtin_amdgcn_rcpf(sp), rn = __builtin_amdgcn_rcpf(sn);
#pragma unroll
  for (int i = 0; i < 8; ++i) {
    o[i] = pk(ep[2 * i] * rp, ep[2 * i + 1] * rp);
    o[8 + i] = pk(en[2 * i] * rn, en[2 * i + 1] * rn);
  }
}
DI v4u pack8(const f32x16& a, int s) {
  v4u r;
#pragma unroll
  for (int m = 0; m < 4; ++m) r[m] = pk(a[8 * s + 2 * m], a[8 * s + 2 * m + 1]);
  return r;
}

template <int NKB> DI void dma_to_lds(const unsigned char* src, LAS unsigned char* dst, int w, int lane) {
#pragma unroll
  for (int i = 0; i < NKB / 4; ++i) {
    const int blk = w * (NKB / 4) + i;
    __builtin_amdgcn_global_load_lds((const GAS u32*)(src + blk * 1024 + lane * 16), (LAS u32*)(dst + blk * 1024), 16, 0, 0);
  }
}

constexpr int PK_OFF = 98304, PK_STR = 144, VV_OFF = PK_OFF + 128 * PK_STR, VV_STR = 80;
constexpr int LDS_P1 = VV_OFF + 128 * VV_STR;

template <int AUX> DI void phase1(unsigned char* ws, LAS unsigned char* L, int layer) {
  const int b = blockIdx.x, tid = threadIdx.x, lane = tid & 63, w = __builtin_amdgcn_readfirstlane(tid >> 6);
  const int xcd = b & 7, j = b >> 3, rt = 2 * xcd + (j >> 4), hp = j & 15, tt = 4 * rt + w;
  const int r = lane & 31, hf = lane >> 5;
  const rsrc_t rs = mkrsrc(ws, WS_END);
  dma_to_lds<96>(ws + WQKV_T + hp * 98304, L, w, lane);
  f32x2 sp[8];
  const u32 stb = ST + (2 * layer) * ST_SZ + tt * 4096;
#pragma unroll
  for (int i = 0; i < 8; ++i) sp[i] = __builtin_bit_cast(f32x2, ld64<AUX>(rs, (8 * hf + i) * 256 + r * 8, stb));
  f32x4 cs[3][4];
#pragma unroll
  for (int t = 0; t < 3; ++t)
#pragma unroll
    for (int i = 0; i < 4; ++i)
      cs[t][i] = __builtin_bit_cast(f32x4, ld128<0>(rs, hf * 64 + i * 16, CS_QKV + ((t == 2 ? 0 : t + 1) * 512 + 32 * hp) * 4));
  v4u wph[2];
#pragma unroll
  for (int s = 0; s < 2; ++s) wph[s] = ld128<0>(rs, lane * 16, WPHI_T + hp * 2048 + s * 1024);
  v4u xf[32];
  const u32 xb = XT + (2 * layer) * XT_SZ + tt * 32768;
#pragma unroll
  for (int ks = 0; ks < 32; ++ks) xf[ks] = ld128<AUX>(rs, lane * 16, xb + ks * 1024);
  float sum, m2;
  stat_merge<8>(sp, sum, m2);
  stat_merge2(sum, m2, xhalf(sum), xhalf(m2), 1.f / 512.f);
  const float mean = sum * (1.f / 512.f);
  const float rstd = __builtin_amdgcn_rsqf(m2 * (1.f / 512.f) + 1e-5f);
  f32x16 aK, aV, aQ;
#pragma unroll
  for (int i = 0; i < 16; ++i) {
    aK[i] = -mean * cs[0][i >> 2][i & 3];
    aV[i] = -mean * cs[1][i >> 2][i & 3];
    aQ[i] = -mean * cs[2][i >> 2][i & 3];
  }
  asm volatile("s_waitcnt vmcnt(0)" ::: "memory");
  __syncthreads();
  LAS v4u* WL = (LAS v4u*)L + lane;
#pragma unroll
  for (int ks = 0; ks < 32; ++ks) {
    aK = mfma32(WL[ks * 64], xf[ks], aK);
    aV = mfma32(WL[(32 + ks) * 64], xf[ks], aV);
  }
#pragma unroll
  for (int ks = 0; ks < 32; ++ks) aQ = mfma32(WL[(64 + ks) * 64], xf[ks], aQ);
  const float c = rstd * LOG2E;
  const f32x16 zero16 = {0.f, 0.f, 0.f, 0.f, 0.f, 0.f, 0.f, 0.f, 0.f, 0.f, 0.f, 0.f, 0.f, 0.f, 0.f, 0.f};
  {
    f32x16 p = mfma32(wph[0], pack8(aK, 0), zero16);
    p = mfma32(wph[1], pack8(aK, 1), p);
    u32 o[16];
    dual_softmax(p, c, o);
    LAS unsigned char* pkrow = L + PK_OFF + (32 * w + r) * PK_STR + hf * 64;
#pragma unroll
    for (int i = 0; i < 4; ++i) *(LAS v4u*)(pkrow + i * 16) = v4u{o[4 * i], o[4 * i + 1], o[4 * i + 2], o[4 * i + 3]};
    LAS unsigned char* vrow = L + VV_OFF + (32 * w + r) * VV_STR + hf * 32;
#pragma unroll
    for (int i = 0; i < 16; ++i) aV[i] *= rstd;
    *(LAS v4u*)(vrow) = pack8(aV, 0);
    *(LAS v4u*)(vrow + 16) = pack8(aV, 1);
  }
  __syncthreads();
  {
    const int hl = w >> 1, sg = w & 1;
    const int g = lane >> 4, q = (lane & 15) >> 2, pp = lane & 3;
    LAS unsigned char* pa = L + PK_OFF + (8 * g + q) * PK_STR + hl * 64 + sg * 32 + pp * 8;
    LAS unsigned char* pb = L + VV_OFF + (8 * g + q) * VV_STR + hl * 32 + pp * 8;
    f32x4 kv = {0.f, 0.f, 0.f, 0.f}, zz = {0.f, 0.f, 0.f, 0.f};
    const v4u ones = {0x3f803f80u, 0x3f803f80u, 0x3f803f80u, 0x3f803f80u};
#pragma unroll
    for (int ks = 0; ks < 4; ++ks) {
      const v2u a0 = trread(pa + (32 * ks) * PK_STR), a1 = trread(pa + (32 * ks + 4) * PK_STR);
      const v2u b0 = trread(pb + (32 * ks) * VV_STR), b1 = trread(pb + (32 * ks + 4) * VV_STR);
      const v4u A = {a0.x, a0.y, a1.x, a1.y}, B = {b0.x, b0.y, b1.x, b1.y};
      kv = mfma16(A, B, kv);
      zz = mfma16(A, ones, zz);
    }
    const u32 pbase = KVP + layer * KVP_SZ + (((2 * hp + hl) * 2 + sg) * 16 + rt) * 1088;
    st128<AUX>(__builtin_bit_cast(v4u, kv), rs, lane * 16, pbase);
    if ((lane & 15) == 0) st128<AUX>(__builtin_bit_cast(v4u, zz), rs, 1024 + g * 16, pbase);
  }
  {
    f32x16 p = mfma32(wph[0], pack8(aQ, 0), zero16);
    p = mfma32(wph[1], pack8(aQ, 1), p);
    u32 o[16];
    dual_softmax(p, c, o);
    const u32 qb = PHIQ + layer * PHIQ_SZ + tt * 65536 + (2 * hp) * 2048;
#pragma unroll
    for (int i = 0; i < 4; ++i)
      st128<AUX>(v4u{o[4 * i], o[4 * i + 1], o[4 * i + 2], o[4 * i + 3]}, rs, hf * 2048 + i * 512 + r * 16, qb);
  }
}

template <int AUX> DI void kv_reduce(unsigned char* ws, int layer, int hs, int lane) {
  const rsrc_t rs = mkrsrc(ws, WS_END);
  const u32 pbase = KVP + layer * KVP_SZ + hs * 16 * 1088;
  f32x4 v[16], z[16];
#pragma unroll
  for (int i = 0; i < 16; ++i) v[i] = __builtin_bit_cast(f32x4, ld128<AUX>(rs, lane * 16, pbase + i * 1088));
#pragma unroll
  for (int i = 0; i < 16; ++i) z[i] = __builtin_bit_cast(f32x4, ld128<AUX>(rs, 1024 + (lane >> 4) * 16, pbase + i * 1088));
  f32x4 sv = v[0], sz = z[0];
#pragma unroll
  for (int i = 1; i < 16; ++i) { sv += v[i]; sz += z[i]; }
  const int d = lane & 15, g = lane >> 4;
  const int rho = rho32(d & 7, d >> 3);
  const u32 fbase = KVF + layer * KVF_SZ + hs * 544;
  st64<AUX>(v2u{pk(sv[0], sv[1]), pk(sv[2], sv[3])}, rs, (g >> 1) * 272 + rho * 16 + (g & 1) * 8, fbase);
  if (d == 0) st64<AUX>(v2u{pk(sz[0], sz[1]), pk(sz[2], sz[3])}, rs, (g >> 1) * 272 + 256 + (g & 1) * 8, fbase);
}

DI f32x16 reduce4(f32x16 (&acc)[4], LAS unsigned char* L, int w, int lane) {
#pragma unroll
  for (int t = 0; t < 4; ++t) {
    if (t != w) {
      const int i = (w - t - 1) & 3;
      LAS f32x4* p = (LAS f32x4*)(L + (t * 3 + i) * 4096) + lane;
#pragma unroll
      for (int q = 0; q < 4; ++q) p[q * 64] = f32x4{acc[t][4 * q], acc[t][4 * q + 1], acc[t][4 * q + 2], acc[t][4 * q + 3]};
    }
  }
  __syncthreads();
  f32x16 r = w == 0 ? acc[0] : w == 1 ? acc[1] : w == 2 ? acc[2] : acc[3];
#pragma unroll
  for (int i = 0; i < 3; ++i) {
    LAS f32x4* p = (LAS f32x4*)(L + (w * 3 + i) * 4096) + lane;
#pragma unroll
    for (int q = 0; q < 4; ++q) {
      const f32x4 v = p[q * 64];
      r[4 * q] += v[0]; r[4 * q + 1] += v[1]; r[4 * q + 2] += v[2]; r[4 * q + 3] += v[3];
    }
  }
  return r;
}

template <int AUX, bool FINAL>
DI void resid_epilogue(f32x16 y, const v4u (&res)[2], rsrc_t rs, rsrc_t rsout, int xo, int tt, int n0, int lane) {
  const int r = lane & 31, hf = lane >> 5;
#pragma unroll
  for (int s = 0; s < 2; ++s)
#pragma unroll
    for (int m = 0; m < 4; ++m) {
      y[8 * s + 2 * m] += bflo(res[s][m]);
      y[8 * s + 2 * m + 1] += bfhi(res[s][m]);
    }
  if (FINAL) {
#pragma unroll
    for (int s = 0; s < 2; ++s) st128<0>(pack8(y, s), rsout, r * 1024 + hf * 16 + s * 32, tt * 32768 + n0 * 2);
  } else {
#pragma unroll
    for (int s = 0; s < 2; ++s) st128<AUX>(pack8(y, s), rs, lane * 16 + s * 1024, XT + xo * XT_SZ + tt * 32768 + (n0 >> 3) * 512);
    float sum = 0.f;
#pragma unroll
    for (int i = 0; i < 16; ++i) sum += y[i];
    const float mu = sum * (1.f / 16.f);
    float m2 = 0.f;
#pragma unroll
    for (int i = 0; i < 16; ++i) { const float d = y[i] - mu; m2 += d * d; }
    stat_merge2(sum, m2, xhalf(sum), xhalf(m2), 1.f / 32.f);
    if (hf == 0) st64<AUX>(__builtin_bit_cast(v2u, f32x2{sum, m2}), rs, r * 8, ST + xo * ST_SZ + tt * 4096 + (n0 >> 5) * 256);
  }
}

template <int AUX> DI void phase2(unsigned char* ws, LAS unsigned char* L, int layer) {
  const int b = blockIdx.x, tid = threadIdx.x, lane = tid & 63, w = __builtin_amdgcn_readfirstlane(tid >> 6);
  const int xcd = b & 7, j = b >> 3, tt = 8 * xcd + (j >> 2), cb = j & 3;
  const rsrc_t rs = mkrsrc(ws, WS_END);
  v4u kvf[16], ph[16], wo[32], res[2];
  const int rho = lane & 31, hfp = lane >> 5;
  const u32 kvo = hfp * 272 + (rho < 16 ? rho : 16) * 16;
#pragma unroll
  for (int i = 0; i < 16; ++i) kvf[i] = ld128<AUX>(rs, kvo, KVF + layer * KVF_SZ + (16 * w + i) * 544);
#pragma unroll
  for (int i = 0; i < 16; ++i) ph[i] = ld128<AUX>(rs, lane * 16, PHIQ + layer * PHIQ_SZ + tt * 65536 + (16 * w + i) * 1024);
#pragma unroll
  for (int i = 0; i < 32; ++i) wo[i] = ld128<0>(rs, lane * 16, WO_T + (cb * 4 + w) * 32768 + i * 1024);
  const int n0 = 128 * cb + 32 * w;
#pragma unroll
  for (int s = 0; s < 2; ++s) res[s] = ld128<AUX>(rs, lane * 16 + s * 1024, XT + (2 * layer) * XT_SZ + tt * 32768 + (n0 >> 3) * 512);
  const f32x16 zero16 = {0.f, 0.f, 0.f, 0.f, 0.f, 0.f, 0.f, 0.f, 0.f, 0.f, 0.f, 0.f, 0.f, 0.f, 0.f, 0.f};
  f32x16 acc[4] = {zero16, zero16, zero16, zero16};
#pragma unroll
  for (int h = 0; h < 8; ++h) {
    f32x16 nd = mfma32(kvf[2 * h], ph[2 * h], zero16);
    nd = mfma32(kvf[2 * h + 1], ph[2 * h + 1], nd);
    const float inv = __builtin_amdgcn_rcpf(nd[8] + 1e-6f);
    v4u ob;
#pragma unroll
    for (int m = 0; m < 4; ++m) ob[m] = pk(nd[2 * m] * inv, nd[2 * m + 1] * inv);
#pragma unroll
    for (int ct = 0; ct < 4; ++ct) acc[ct] = mfma32(wo[4 * h + ct], ob, acc[ct]);
  }
  const f32x16 y = reduce4(acc, L, w, lane);
  resid_epilogue<AUX, false>(y, res, rs, rs, 2 * layer + 1, tt, n0, lane);
}

template <int AUX> DI void phase3(unsigned char* ws, LAS unsigned char* L, int layer) {
  const int b = blockIdx.x, tid = threadIdx.x, lane = tid & 63, w = __builtin_amdgcn_readfirstlane(tid >> 6);
  const int xcd = b & 7, j = b >> 3, rt = 2 * xcd + (j >> 4), fb = j & 15, tt = 4 * rt + w;
  const int c = lane & 15, g = lane >> 4;
  const rsrc_t rs = mkrsrc(ws, WS_END);
  dma_to_lds<112>(ws + W1_T + fb * 114688, L, w, lane);
  f32x2 sp[2][4];
  const u32 stb = ST + (2 * layer + 1) * ST_SZ + tt * 4096;
#pragma unroll
  for (int u = 0; u < 2; ++u)
#pragma unroll
    for (int i = 0; i < 4; ++i) sp[u][i] = __builtin_bit_cast(f32x2, ld64<AUX>(rs, (4 * g + i) * 256 + (16 * u + c) * 8, stb));
  f32x4 cs[7];
#pragma unroll
  for (int t = 0; t < 6; ++t) cs[t] = __builtin_bit_cast(f32x4, ld128<0>(rs, g * 32, CS_W1 + (112 * fb + 32 * (t >> 1) + 4 * (t & 1)) * 4));
  cs[6] = __builtin_bit_cast(f32x4, ld128<0>(rs, g * 16, CS_W1 + (112 * fb + 96) * 4));
  v4u xf[16][2];
  const u32 xb = XT + (2 * layer + 1) * XT_SZ + tt * 32768;
#pragma unroll
  for (int ks = 0; ks < 16; ++ks)
#pragma unroll
    for (int u = 0; u < 2; ++u) xf[ks][u] = ld128<AUX>(rs, g * 512 + c * 16 + u * 256, xb + ks * 2048);
  float mean[2], rstd[2];
#pragma unroll
  for (int u = 0; u < 2; ++u) {
    float sum, m2;
    stat_merge<4>(sp[u], sum, m2);
    stat_merge2(sum, m2, __shfl_xor(sum, 16), __shfl_xor(m2, 16), 1.f / 256.f);
    stat_merge2(sum, m2, xhalf(sum), xhalf(m2), 1.f / 512.f);
    mean[u] = sum * (1.f / 512.f);
    rstd[u] = __builtin_amdgcn_rsqf(m2 * (1.f / 512.f) + 1e-5f);
  }
  f32x4 acc[2][7];
#pragma unroll
  for (int u = 0; u < 2; ++u)
#pragma unroll
    for (int t = 0; t < 7; ++t) acc[u][t] = cs[t] * (-mean[u]);
  asm volatile("s_waitcnt vmcnt(0)" ::: "memory");
  __syncthreads();
  LAS v4u* WL = (LAS v4u*)L + lane;
#pragma unroll
  for (int ks = 0; ks < 16; ++ks)
#pragma unroll
    for (int t = 0; t < 7; ++t) {
      const v4u a = WL[(ks * 7 + t) * 64];
      acc[0][t] = mfma16(a, xf[ks][0], acc[0][t]);
      acc[1][t] = mfma16(a, xf[ks][1], acc[1][t]);
    }
  const u32 hb = HB + layer * HB_SZ + tt * 114688 + (14 * fb) * 512;
#pragma unroll
  for (int u = 0; u < 2; ++u) {
    const float nc = -rstd[u] * LOG2E;
    u32 o[14];
#pragma unroll
    for (int t = 0; t < 7; ++t) {
      float hv[4];
#pragma unroll
      for (int i = 0; i < 4; ++i) {
        const float e = __builtin_amdgcn_exp2f(acc[u][t][i] * nc);
        hv[i] = acc[u][t][i] * rstd[u] * __builtin_amdgcn_rcpf(1.f + e);
      }
      o[2 * t] = pk(hv[0], hv[1]);
      o[2 * t + 1] = pk(hv[2], hv[3]);
    }
#pragma unroll
    for (int p = 0; p < 3; ++p)
      st128<AUX>(v4u{o[4 * p], o[4 * p + 1], o[4 * p + 2], o[4 * p + 3]}, rs, g * 512 + (16 * u + c) * 16, hb + p * 2048);
    st64<AUX>(v2u{o[12], o[13]}, rs, (g >> 1) * 512 + (16 * u + c) * 16 + (g & 1) * 8, hb + 12 * 512);
  }
}

template <int AUX, bool FINAL> DI void phase4(unsigned char* ws, unsigned char* out, LAS unsigned char* L, int layer) {
  const int b = blockIdx.x, tid = threadIdx.x, lane = tid & 63, w = __builtin_amdgcn_readfirstlane(tid >> 6);
  const int xcd = b & 7, j = b >> 3, rt2 = 4 * xcd + (j >> 3), cb = j & 7;
  const rsrc_t rs = mkrsrc(ws, WS_END);
  const rsrc_t rso = mkrsrc(out, S * D * 2);
  const int tte = 2 * rt2 + (w >> 1), n0 = 64 * cb + 32 * (w & 1);
  v4u res[2];
#pragma unroll
  for (int s = 0; s < 2; ++s) res[s] = ld128<AUX>(rs, lane * 16 + s * 1024, XT + (2 * layer + 1) * XT_SZ + tte * 32768 + (n0 >> 3) * 512);
  const f32x16 zero16 = {0.f, 0.f, 0.f, 0.f, 0.f, 0.f, 0.f, 0.f, 0.f, 0.f, 0.f, 0.f, 0.f, 0.f, 0.f, 0.f};
  f32x16 acc[4] = {zero16, zero16, zero16, zero16};
  const u32 wb = W2_T + (cb * 4 + w) * 57344;
  const u32 hb = HB + layer * HB_SZ + (2 * rt2) * 114688 + (28 * w) * 1024;
  constexpr int WIN = 14;
  v4u wf[WIN][2], hf_[WIN][2];
#pragma unroll
  for (int i = 0; i < WIN; ++i) {
    wf[i][0] = ld128<0>(rs, lane * 16, wb + i * 2048);
    wf[i][1] = ld128<0>(rs, lane * 16, wb + i * 2048 + 1024);
    hf_[i][0] = ld128<AUX>(rs, lane * 16, hb + i * 1024);
    hf_[i][1] = ld128<AUX>(rs, lane * 16, hb + 114688 + i * 1024);
  }
  __builtin_amdgcn_sched_barrier(0);
#pragma unroll
  for (int ks = 0; ks < 28; ++ks) {
    const int i = ks % WIN;
    const v4u a0 = wf[i][0], a1 = wf[i][1], b0 = hf_[i][0], b1 = hf_[i][1];
    if (ks + WIN < 28) {
      wf[i][0] = ld128<0>(rs, lane * 16, wb + (ks + WIN) * 2048);
      wf[i][1] = ld128<0>(rs, lane * 16, wb + (ks + WIN) * 2048 + 1024);
      hf_[i][0] = ld128<AUX>(rs, lane * 16, hb + (ks + WIN) * 1024);
      hf_[i][1] = ld128<AUX>(rs, lane * 16, hb + 114688 + (ks + WIN) * 1024);
    }
    acc[0] = mfma32(a0, b0, acc[0]);
    acc[1] = mfma32(a1, b0, acc[1]);
    acc[2] = mfma32(a0, b1, acc[2]);
    acc[3] = mfma32(a1, b1, acc[3]);
    __builtin_amdgcn_sched_barrier(0);
  }
  const f32x16 y = reduce4(acc, L, w, lane);
  resid_epilogue<AUX, FINAL>(y, res, rs, rso, 2 * layer + 2, tte, n0, lane);
}

extern __shared__ __attribute__((aligned(16))) unsigned char smem[];
__global__ void __launch_bounds__(256, 1) k_p1(unsigned char* ws, int layer) { phase1<0>(ws, (LAS unsigned char*)smem, layer); }
__global__ void __launch_bounds__(64) k_red(unsigned char* ws, int layer) { kv_reduce<0>(ws, layer, blockIdx.x, threadIdx.x); }
__global__ void __launch_bounds__(256, 1) k_p2(unsigned char* ws, int layer) { phase2<0>(ws, (LAS unsigned char*)smem, layer); }
__global__ void __launch_bounds__(256, 1) k_p3(unsigned char* ws, int layer) { phase3<0>(ws, (LAS unsigned char*)smem, layer); }
template <bool FINAL>
__global__ void __launch_bounds__(256, 1) k_p4(unsigned char* ws, unsigned char* out, int layer) { phase4<0, FINAL>(ws, out, (LAS unsigned char*)smem, layer); }

extern "C" void kernel_launch(void* const* d_in, const int* in_sizes, int n_in, void* d_out, int out_size, void* d_ws,
                              size_t ws_size, hipStream_t stream) {
  const uint16_t* x = (const uint16_t*)d_in[0];
  const uint16_t* wqkv = (const uint16_t*)d_in[1];
  const uint16_t* wphi = (const uint16_t*)d_in[2];
  const uint16_t* wo = (const uint16_t*)d_in[3];
  const uint16_t* w1 = (const uint16_t*)d_in[5];
  const uint16_t* w2 = (const uint16_t*)d_in[6];
  unsigned char* ws = (unsigned char*)d_ws;
  (void)hipFuncSetAttribute((const void*)k_p1, hipFuncAttributeMaxDynamicSharedMemorySize, LDS_P1);
  (void)hipFuncSetAttribute((const void*)k_p3, hipFuncAttributeMaxDynamicSharedMemorySize, 114688);
  k_prep<<<256, 256, 0, stream>>>(x, wqkv, wphi, wo, w1, w2, ws);
  for (int layer = 0; layer < 2; ++layer) {
    k_p1<<<256, 256, LDS_P1, stream>>>(ws, layer);
    k_red<<<64, 64, 0, stream>>>(ws, layer);
    k_p2<<<256, 256, 49152, stream>>>(ws, layer);
    k_p3<<<256, 256, 114688, stream>>>(ws, layer);
    if (layer == 0) k_p4<false><<<256, 256, 49152, stream>>>(ws, (unsigned char*)d_out, layer);
    else k_p4<true><<<256, 256, 49152, stream>>>(ws, (unsigned char*)d_out, layer);
  }
}
```

```cpp
#include <hip/hip_runtime.h>
#include <hip/hip_bf16.h>
#include <stdint.h>
#include <utility>

typedef unsigned int u32;
typedef u32 v4u __attribute__((ext_vector_type(4)));
typedef u32 v2u __attribute__((ext_vector_type(2)));
typedef short s16x4 __attribute__((ext_vector_type(4)));
typedef __bf16 bf16x8 __attribute__((ext_vector_type(8)));
typedef __bf16 bf16x2 __attribute__((ext_vector_type(2)));
typedef float f32x2 __attribute__((ext_vector_type(2)));
typedef float f32x4 __attribute__((ext_vector_type(4)));
typedef float f32x16 __attribute__((ext_vector_type(16)));
#define LAS __attribute__((address_space(3)))
#define GAS __attribute__((address_space(1)))
#define DI __device__ __forceinline__
typedef __amdgpu_buffer_rsrc_t rsrc_t;

#define DXV 12
#define WINH 14
#define HSIG 56
#define KVCHK 3
#define BPEEK 4
#define TLBW 262144
#define W2D 4
#define W2B 3
constexpr int S = 2048, D = 512;
constexpr u32 CTRL = 0;
constexpr u32 WQKV_T = 262144;
constexpr u32 WPHI_T = WQKV_T + 16 * 98304;
constexpr u32 WO_T = WPHI_T + 32768;
constexpr u32 W1_T = WO_T + 524288;
constexpr u32 W2_T = W1_T + 14 * 131072;
constexpr u32 CS_QKV = W2_T + 8 * 4 * 28 * 2048;
constexpr u32 CS_W1 = CS_QKV + 65536;
constexpr u32 XT = CS_W1 + 65536;
constexpr u32 XT_SZ = 2097152;
constexpr u32 ST = XT + 4 * XT_SZ;
constexpr u32 ST_SZ = 262144;
constexpr u32 OB = ST + 4 * ST_SZ;
constexpr u32 HB = OB + 2 * XT_SZ;
constexpr u32 HB_SZ = 64 * 114688;
constexpr u32 KVP = HB + 2 * HB_SZ;
constexpr u32 KVP_HS = 16 * 1024 + 1024;
constexpr u32 KVP_SZ = 64 * KVP_HS;
constexpr u32 WS_END = KVP + 2 * KVP_SZ;
#define CSTR 128
enum { C_X = 0, C_KV = 64, C_O = 192, C_X1 = 448, C_H = 576, C_P = 1088, C_J = 1120, C_TMO = 1151, NCNT = 1152 };
constexpr u32 INIT_OFF = CTRL + NCNT * CSTR, EPOCH_OFF = INIT_OFF + 16 * CSTR;

#define STAMP(i)
constexpr float LOG2E = 1.4426950408889634f;
constexpr float LN2 = 0.6931471805599453f;

DI u32 pk(float a, float b) {
  f32x2 v = {a, b};
  bf16x2 r = __builtin_convertvector(v, bf16x2);
  return __builtin_bit_cast(u32, r);
}
DI float bflo(u32 u) { return __builtin_bit_cast(float, u << 16); }
DI float bfhi(u32 u) { return __builtin_bit_cast(float, u & 0xffff0000u); }
DI f32x16 mfma32(v4u a, v4u b, f32x16 c) {
  f32x16 d = __builtin_amdgcn_mfma_f32_32x32x16_bf16(__builtin_bit_cast(bf16x8, a), __builtin_bit_cast(bf16x8, b), c, 0, 0, 0);
  asm volatile("" : "+a"(d));
  return d;
}
DI f32x4 mfma16(v4u a, v4u b, f32x4 c) {
  return __builtin_amdgcn_mfma_f32_16x16x32_bf16(__builtin_bit_cast(bf16x8, a), __builtin_bit_cast(bf16x8, b), c, 0, 0, 0);
}
DI f32x16 mfma32f(float a, float b, f32x16 c) {
  f32x16 d = __builtin_amdgcn_mfma_f32_32x32x2f32(a, b, c, 0, 0, 0);
  asm volatile("" : "+a"(d));
  return d;
}
DI float dot2(u32 a, u32 b, float c) {
  return __builtin_amdgcn_fdot2_f32_bf16(__builtin_bit_cast(bf16x2, a), __builtin_bit_cast(bf16x2, b), c, false);
}
template <int AUX> DI v4u ld128(rsrc_t rs, u32 voff, u32 soff) { return __builtin_amdgcn_raw_buffer_load_b128(rs, voff, soff, AUX); }
template <int AUX> DI v2u ld64(rsrc_t rs, u32 voff, u32 soff) { return __builtin_amdgcn_raw_buffer_load_b64(rs, voff, soff, AUX); }
template <int AUX> DI u32 ld32(rsrc_t rs, u32 voff, u32 soff) { return __builtin_amdgcn_raw_buffer_load_b32(rs, voff, soff, AUX); }
template <int AUX> DI void st128(v4u v, rsrc_t rs, u32 voff, u32 soff) {
  __builtin_amdgcn_raw_buffer_store_b128(v, rs, voff, soff, AUX);
  asm volatile("s_nop 1" ::"v"(v) : "memory");
}
template <int AUX> DI void st64(v2u v, rsrc_t rs, u32 voff, u32 soff) { __builtin_amdgcn_raw_buffer_store_b64(v, rs, voff, soff, AUX); }
DI rsrc_t mkrsrc(const void* p, u32 bytes) { return __builtin_amdgcn_make_buffer_rsrc((void*)p, 0, (int)bytes, 0x00020000); }
DI float sum32(float v) {
  u32 u = __builtin_bit_cast(u32, v);
  auto r = __builtin_amdgcn_permlane32_swap(u, u, false, false);
  const u32 r0 = r[0], r1 = r[1];
  return __builtin_bit_cast(float, r0) + __builtin_bit_cast(float, r1);
}
DI float sum16(float v) {
  u32 u = __builtin_bit_cast(u32, v);
  auto r = __builtin_amdgcn_permlane16_swap(u, u, false, false);
  const u32 r0 = r[0], r1 = r[1];
  return __builtin_bit_cast(float, r0) + __builtin_bit_cast(float, r1);
}
DI v2u trread(LAS unsigned char* p) {
  return __builtin_bit_cast(v2u, __builtin_amdgcn_ds_read_tr16_b64_v4i16((LAS s16x4*)p));
}
DI int rho32(int reg, int hf) { return (reg & 3) + 8 * (reg >> 2) + 4 * hf; }
constexpr u32 ONES2 = 0x3f803f80u;

DI v4u pack8(const f32x16& a, int s) {
  v4u r;
#pragma unroll
  for (int m = 0; m < 4; ++m) r[m] = pk(a[8 * s + 2 * m], a[8 * s + 2 * m + 1]);
  return r;
}

#define SB() __builtin_amdgcn_sched_barrier(0)
#define PIN(x) asm volatile("" : "+v"(x))
#define BAR() asm volatile("s_waitcnt lgkmcnt(0)\n\ts_barrier" ::: "memory")
#define DRAIN() asm volatile("s_waitcnt vmcnt(0)" ::: "memory")
#define RLX __ATOMIC_RELAXED, __HIP_MEMORY_SCOPE_AGENT
constexpr int SC1 = 16;

DI u32 opq(u32 x) { asm volatile("" : "+s"(x)); return x; }
DI u32 opqv(u32 x) { asm volatile("" : "+v"(x)); return x; }
template <int AUX> DI v4u ldf(rsrc_t rs, u32 vo, u32 sbase, u32 off) { return ld128<AUX>(rs, vo + (off & 4095u), sbase + (off & ~4095u)); }
template <int I> DI void dma1(rsrc_t rs, u32 vo, u32 sbase, LAS unsigned char* d) {
  __builtin_amdgcn_raw_ptr_buffer_load_lds(rs, (LAS u32*)(d + (I >> 2) * 4096), 16, vo, sbase + (I >> 2) * 4096, (I & 3) * 1024, SC1);
}
template <int I0, int I1> DI void dma_range(rsrc_t rs, u32 vo, u32 sbase, LAS unsigned char* d) {
  if constexpr (I0 < I1) {
    dma1<I0>(rs, vo, sbase, d);
    dma_range<I0 + 1, I1>(rs, vo, sbase, d);
  }
}
template <class F, int... I> DI void static_for_impl(F&& f, std::integer_sequence<int, I...>) { (f(std::integral_constant<int, I>{}), ...); }
template <int N, class F> DI void static_for(F&& f) { static_for_impl(f, std::make_integer_sequence<int, N>{}); }
DI u32 cnt_off(int idx) { return CTRL + idx * CSTR; }
DI u32 lane0_off() { return (threadIdx.x & 63) == 0 ? 0u : 0x80000000u; }
DI void add1(rsrc_t rs, u32 l0, u32 coff) { (void)__builtin_amdgcn_raw_ptr_buffer_atomic_add_i32(1, rs, l0, coff, 0); }
template <int N> DI void wsignal(rsrc_t rs, u32 l0, u32 coff) {
  SB();
  asm volatile("s_waitcnt vmcnt(%0)" ::"n"(N) : "memory");
  add1(rs, l0, coff);
  SB();
}
DI u32 peek(rsrc_t rs, u32 l0, u32 coff) { return __builtin_amdgcn_raw_ptr_buffer_atomic_add_i32(0, rs, l0, coff, 0); }
DI void wfinish(rsrc_t rs, u32 l0, u32 coff, u32 n, u32 pv) {
  if (__builtin_amdgcn_readfirstlane(pv) < n) {
#pragma unroll 1
    for (u32 spins = 0;; ++spins) {
      if (__builtin_amdgcn_readfirstlane(peek(rs, l0, coff)) >= n) break;
      if (spins > (1u << 16)) { __builtin_amdgcn_raw_buffer_store_b32(1u, rs, 0, cnt_off(C_TMO), SC1); break; }
    }
  }
  asm volatile("" ::: "memory");
}
DI void wwait(rsrc_t rs, u32 l0, u32 coff, u32 n) { wfinish(rs, l0, coff, n, peek(rs, l0, coff)); }
DI void poll2(rsrc_t rs, u32 c0, u32 n0, u32 c1, u32 n1, u32 lane) {
  const u32 vo = lane == 0 ? c0 : lane == 1 ? c1 : 0x80000000u, need = (lane & 1) ? n1 : n0;
#pragma unroll 1
  for (u32 spins = 0;; ++spins) {
    const u32 v = peek(rs, vo, 0);
    if ((__builtin_amdgcn_ballot_w64(v >= need) & 3ull) == 3ull) break;
    if (spins > (1u << 16)) { __builtin_amdgcn_raw_buffer_store_b32(1u, rs, 0, cnt_off(C_TMO), SC1); break; }
  }
  asm volatile("" ::: "memory");
}
template <int CTRL_> DI float dpp_add(float v) {
  return v + __builtin_bit_cast(float, __builtin_amdgcn_update_dpp(0, __builtin_bit_cast(int, v), CTRL_, 0xf, 0xf, true));
}

constexpr int SCR = 131072;
constexpr int PK_OFF = SCR, PK_STR = 144, VV_OFF = PK_OFF + 128 * PK_STR, VV_STR = 80, KF_OFF = VV_OFF + 128 * VV_STR;
constexpr int RED_OFF = SCR;
constexpr int LDS_BYTES = 163840;
static_assert(KF_OFF + 4 * 544 <= LDS_BYTES, "lds");

DI void lds_put1(LAS unsigned char* p, const f32x16& a, int q) {
  *(LAS f32x4*)(p + q * 1024) = f32x4{a[4 * q], a[4 * q + 1], a[4 * q + 2], a[4 * q + 3]};
}

extern __shared__ __attribute__((aligned(16))) unsigned char smem[];

template <int NP, int KP, int WIN, u32 PSTR, class Pre, class Mid>
DI void splitk_phase(rsrc_t rs, const v4u (&wf)[NP * KP][2], u32 a_own, u32 a_oth, LAS unsigned char* L, int w, u32 l16, float scale,
                     v4u (&res)[2], Pre&& pre_loop, Mid&& mid) {
  const f32x16 zero16 = {0.f, 0.f, 0.f, 0.f, 0.f, 0.f, 0.f, 0.f, 0.f, 0.f, 0.f, 0.f, 0.f, 0.f, 0.f, 0.f};
  constexpr int NS = 2 * NP * KP, E0 = NS - KP;
  v4u af[WIN];
  auto ld = [&](int i) { return ldf<SC1>(rs, l16, (i % (2 * KP)) < KP ? a_oth : a_own, (i / (2 * KP)) * PSTR + (i % KP) * 1024); };
#pragma unroll
  for (int i = 0; i < WIN; ++i) { af[i] = ld(i); SB(); }
  LAS unsigned char* r2 = L + opqv(RED_OFF + l16 + (w ^ 2) * 8192);
  LAS unsigned char* r3 = L + opqv(RED_OFF + l16 + (w ^ 3) * 8192 + 4096);
  LAS unsigned char* r0 = L + opqv(RED_OFF + l16 + w * 8192);
  LAS unsigned char* r1 = L + opqv(RED_OFF + l16 + (w ^ 1) * 8192);
  pre_loop();
  SB();
  f32x16 a0 = zero16, a1 = zero16, a2 = zero16, a3 = zero16;
  f32x4 p[8];
  static_for<NS>([&](auto I) {
    constexpr int i = I, ks = (i / (2 * KP)) * KP + i % KP, e = i - E0;
    const v4u bb = af[i % WIN];
    if constexpr (e == 5) BAR();
    if constexpr ((i % (2 * KP)) < KP) a2 = mfma32(wf[ks][0], bb, a2);
    else a1 = mfma32(wf[ks][1], bb, a1);
    SB();
    if constexpr (e >= 1 && e <= 4) lds_put1(r2, a2, e - 1);
    if constexpr (e == 5 || e == 6) { p[4 * (e - 5)] = *(LAS f32x4*)(r0 + (4 * (e - 5)) * 1024); p[4 * (e - 5) + 1] = *(LAS f32x4*)(r0 + (4 * (e - 5) + 1) * 1024); }
    mid(I);
    if constexpr (i + WIN < NS) af[i % WIN] = ld(i + WIN);
    SB();
    if constexpr ((i % (2 * KP)) < KP) a3 = mfma32(wf[ks][1], bb, a3);
    else a0 = mfma32(wf[ks][0], bb, a0);
    SB();
    if constexpr (e >= 1 && e <= 4) lds_put1(r3, a3, e - 1);
    if constexpr (e == 5 || e == 6) { p[4 * (e - 5) + 2] = *(LAS f32x4*)(r0 + (4 * (e - 5) + 2) * 1024); p[4 * (e - 5) + 3] = *(LAS f32x4*)(r0 + (4 * (e - 5) + 3) * 1024); }
    SB();
  });
  f32x4 t[4];
#pragma unroll
  for (int q = 0; q < 4; ++q) { t[q] = p[q] + p[4 + q]; PIN(t[q]); }
  SB();
#pragma unroll
  for (int q = 0; q < 4; ++q) lds_put1(r0, a1, q);
  SB();
#pragma unroll
  for (int q = 0; q < 4; ++q) {
    const u32 ra = res[q >> 1][2 * (q & 1)], rb = res[q >> 1][2 * (q & 1) + 1];
    t[q] = (t[q] + f32x4{a0[4 * q], a0[4 * q + 1], a0[4 * q + 2], a0[4 * q + 3]}) * scale + f32x4{bflo(ra), bfhi(ra), bflo(rb), bfhi(rb)};
    PIN(t[q]);
  }
  BAR();
#pragma unroll
  for (int q = 0; q < 4; ++q) p[q] = *(LAS f32x4*)(r1 + q * 1024);
  SB();
#pragma unroll
  for (int q = 0; q < 4; ++q) {
    t[q] = p[q] * scale + t[q];
    res[q >> 1][2 * (q & 1)] = pk(t[q][0], t[q][1]);
    res[q >> 1][2 * (q & 1) + 1] = pk(t[q][2], t[q][3]);
  }
}
DI void resid_store(rsrc_t rs, const v4u (&res)[2], u32 l16, u32 sto, u32 xoff, u32 soff) {
#pragma unroll
  for (int s = 0; s < 2; ++s) st128<SC1>(res[s], rs, l16 + s * 1024, xoff);
  SB();
  float s0 = 0.f, q0 = 0.f, s1 = 0.f, q1 = 0.f;
#pragma unroll
  for (int m = 0; m < 4; ++m) {
    s0 = dot2(res[0][m], ONES2, s0); q0 = dot2(res[0][m], res[0][m], q0);
    s1 = dot2(res[1][m], ONES2, s1); q1 = dot2(res[1][m], res[1][m], q1);
  }
  st64<SC1>(__builtin_bit_cast(v2u, f32x2{sum32(s0 + s1), sum32(q0 + q1)}), rs, sto, soff);
}

template <class Hook> DI void dual_softmax(const f32x16& p, float c, u32 (&o)[16], Hook&& hook) {
  float mx = p[0], mn = p[0];
#pragma unroll
  for (int i = 1; i < 16; ++i) { mx = fmaxf(mx, p[i]); mn = fminf(mn, p[i]); }
  const float ap = -mx * c, an = mn * c;
  f32x2 ep[8], en[8];
#pragma unroll
  for (int i = 0; i < 8; ++i) {
    const f32x2 pv = {p[2 * i], p[2 * i + 1]};
    ep[i] = pv * c + ap;
    en[i] = pv * (-c) + an;
    PIN(ep[i]);
    PIN(en[i]);
  }
  SB();
  f32x2 sa, sc;
  static_for<8>([&](auto I) {
    constexpr int i = I;
    ep[i] = f32x2{__builtin_amdgcn_exp2f(ep[i].x), __builtin_amdgcn_exp2f(ep[i].y)};
    en[i] = f32x2{__builtin_amdgcn_exp2f(en[i].x), __builtin_amdgcn_exp2f(en[i].y)};
    PIN(ep[i]);
    PIN(en[i]);
    if constexpr (i == 2) { sa = ep[0] + ep[1]; sc = en[0] + en[1]; PIN(sa); PIN(sc); }
    if constexpr (i > 2) { sa += ep[i - 1]; sc += en[i - 1]; PIN(sa); PIN(sc); }
    SB();
    hook(I);
    SB();
  });
  sa += ep[7];
  sc += en[7];
  const float rp = __builtin_amdgcn_rcpf(sa.x + sa.y), rn = __builtin_amdgcn_rcpf(sc.x + sc.y);
#pragma unroll
  for (int i = 0; i < 8; ++i) { ep[i] *= rp; PIN(ep[i]); }
#pragma unroll
  for (int i = 0; i < 8; ++i) { en[i] *= rn; PIN(en[i]); }
  SB();
#pragma unroll
  for (int i = 0; i < 8; ++i) { o[i] = pk(ep[i].x, ep[i].y); o[8 + i] = pk(en[i].x, en[i].y); }
}
constexpr int p3_g0(int v) { return (v * 58) / 48; }
constexpr bool p3_w2gap(int gi) { return (gi * W2B) / 8 != ((gi + 1) * W2B) / 8; }
constexpr int p3_younger(int layer) {
  int n = 0;
  const int g3 = p3_g0(31) + 5;
  for (int q = 4; q < 6; ++q) n += (p3_g0(8 * q + 7) + 5 <= HSIG);
  if (layer == 0) for (int g = g3 + 1; g <= HSIG; ++g) n += p3_w2gap(g);
  return n;
}
static_assert(p3_g0(32) >= 35 && p3_g0(47) + 5 < 64 && p3_g0(31) + 5 < HSIG, "P3 schedule");

__global__ void __launch_bounds__(256, 1)
k_main(const uint16_t* __restrict__ x, const uint16_t* __restrict__ wqkv, const uint16_t* __restrict__ wphi,
       const uint16_t* __restrict__ wo_, const uint16_t* __restrict__ w1, const uint16_t* __restrict__ w2,
       unsigned char* ws, unsigned char* out) {
  LAS unsigned char* L = (LAS unsigned char*)smem;
  const int b = blockIdx.x, tid = threadIdx.x, lane = tid & 63, w = __builtin_amdgcn_readfirstlane(tid >> 6);
  const int xcd = b & 7, j = b >> 3;
  const int rt = 4 * (xcd >> 1) + (j >> 3), hp = 8 * (xcd & 1) + (j & 7), tt = 4 * rt + w;
  const bool p3on = (j & 7) < 7;
  const int fb = p3on ? 7 * (xcd & 1) + (j & 7) : 0;
  const int rt2 = 4 * xcd + (j >> 3), cb = j & 7;
  const int tte = 2 * rt2 + (w >> 1), n0 = 64 * cb + 32 * (w & 1);
  const rsrc_t rs = mkrsrc(ws, WS_END);
  const f32x16 zero16 = {0.f, 0.f, 0.f, 0.f, 0.f, 0.f, 0.f, 0.f, 0.f, 0.f, 0.f, 0.f, 0.f, 0.f, 0.f, 0.f};
  STAMP(63);
  v4u res[2];
  float csq[3], cs1[4];
  v4u wph[2];
  u32 tag_lo, tag_hi;
  const u32 l0 = lane0_off();
  const u32 nlh = !p3on ? 0u : (fb == 3 || fb == 10) ? 2u : 1u;
  const u32 lh = opqv((u32)lane < nlh ? lane * CSTR : 0x80000000u);
  {
    if (b >= 248 && w < 2) {
#pragma unroll
      for (int i = 0; i < NCNT * CSTR / 16384; ++i) st128<SC1>(v4u{0u, 0u, 0u, 0u}, rs, lane * 16, CTRL + ((b - 248) * 2 + w) * (NCNT * CSTR / 16) + i * 1024);
    }
    const v2u ep = ld64<SC1>(rs, 0, EPOCH_OFF);
    u32 tw = ld32<0>(rs, (u32)(lane + 64 * w) * TLBW, 0);
    int kind, nb = 0, kq = 0, ld = 512;
    const uint16_t* W = x;
    const int hw = w & 1;
    if (w < 2) {
      if (b < 128) kind = 4;
      else if (b < 224) { kind = 0; W = wqkv; ld = 1536; nb = (b - 128) >> 2; kq = (b & 3) * 2 + hw; }
      else kind = b < 232 ? 5 : 6;
    } else {
      if (b < 112) { kind = 1; W = w1; ld = 1792; nb = b >> 2; kq = (b & 3) * 2 + hw; }
      else if (b < 144) { kind = 2; W = wo_; nb = (b - 112) >> 2; kq = (b & 3) * 2 + hw; }
      else { kind = 3; W = w2; nb = (b - 144) / 14; kq = ((b - 144) - 14 * nb) * 2 + hw; }
    }
    const int kb = lane >> 3, nq = lane & 7;
    const uint16_t* src = kind < 4 ? W + (size_t)(64 * kq + 8 * kb) * ld + 64 * nb + 8 * nq : x + (size_t)(16 * (b & 127) + 8 * hw) * 512 + 8 * lane;
    v4u R[8];
    if (kind < 5) {
#pragma unroll
      for (int r = 0; r < 8; ++r) R[r] = *(const v4u*)(src + (size_t)r * ld);
    }
#pragma unroll
    for (int s = 0; s < 2; ++s) res[s] = *(const v4u*)(x + (size_t)(32 * tte + (lane & 31)) * 512 + n0 + 16 * s + 8 * (lane >> 5));
    {
      const unsigned long long e = ((unsigned long long)__builtin_amdgcn_readfirstlane(ep.y) << 32) | __builtin_amdgcn_readfirstlane(ep.x);
      unsigned long long t = e * 0x9E3779B97F4A7C15ull + 0x632BE59BD9B4E019ull;
      if (t == e) t = ~e;
      tag_lo = (u32)t;
      tag_hi = (u32)(t >> 32);
    }
    if (b >= 248 && w < 2) {
      DRAIN();
      st64<SC1>(v2u{tag_lo, tag_hi}, rs, l0, INIT_OFF + ((b - 248) * 2 + w) * CSTR);
    }
    asm volatile("" ::"v"(tw));
    STAMP(58);
    LAS unsigned char* img = L + SCR + w * 8192;
    v4u o[8];
    u32 ovo = lane * 16, obase = 0, cstr = 0, kstr = 0, csoff = 0;
    float csv = 0.f;
    if (kind < 4) {
      const int ct = nq >> 2;
#pragma unroll
      for (int i = 0; i < 8; ++i) {
        v4u t;
#pragma unroll
        for (int m = 0; m < 4; ++m)
          t[m] = __builtin_amdgcn_perm(R[2 * m + 1][i >> 1], R[2 * m][i >> 1], (i & 1) ? 0x07060302u : 0x05040100u);
        const int c = 8 * (nq & 3) + i;
        const int rho = kind == 0 ? rho32(c & 15, c >> 4) : rho32(8 * (c >> 4) + (c & 7), (c >> 3) & 1);
        *(LAS v4u*)(img + ct * 4096 + kb * 512 + ((rho ^ ((rho >> 4) | (ct << 1))) * 16)) = t;
        if (kind < 2) {
          float s = 0.f;
#pragma unroll
          for (int m = 0; m < 4; ++m) s = dot2(t[m], ONES2, s);
          s = sum32(sum16(dpp_add<0x128>(s)));
          csv = kb == i ? s : csv;
        }
      }
      const int t32 = 2 * nb, sec = t32 >> 4, tile = sec == 0 ? 2 : sec - 1;
      if (kind == 0) { obase = WQKV_T + (t32 & 15) * 98304 + tile * 32768 + kq * 4096; cstr = 98304; kstr = 1024; }
      else if (kind == 1) { obase = W1_T + t32 * 32768 + kq * 4096; cstr = 32768; kstr = 1024; }
      else if (kind == 2) { obase = WO_T + (nb * 32 + kq * 4) * 2048; cstr = 1024; kstr = 2048; }
      else { obase = W2_T + (nb * 112 + (kq & 1) * 56 + (kq >> 1) * 4) * 2048; cstr = 1024; kstr = 2048; }
      const int c = 8 * (nq & 3) + kb;
      if (kind == 0) csoff = CS_QKV + ((((t32 & 15) + ct) * 3 + tile) * 8 + kq) * 128 + rho32(c & 15, c >> 4) * 4;
      else csoff = CS_W1 + ((t32 + ct) * 8 + kq) * 128 + rho32(8 * (c >> 4) + (c & 7), (c >> 3) & 1) * 4;
      asm volatile("s_waitcnt lgkmcnt(0)" ::: "memory");
      const int rho = lane & 31;
#pragma unroll
      for (int i = 0; i < 8; ++i)
        o[i] = *(LAS v4u*)(img + i * 1024 + (lane >> 5) * 512 + ((rho ^ ((rho >> 4) | ((i >> 2) << 1))) * 16));
    } else if (kind == 4) {
      f32x2 st[8];
#pragma unroll
      for (int r = 0; r < 8; ++r) {
        *(LAS v4u*)(img + lane * 128 + ((r ^ (lane & 7)) * 16)) = R[r];
        float s = 0.f, q = 0.f;
#pragma unroll
        for (int m = 0; m < 4; ++m) { s = dot2(R[r][m], ONES2, s); q = dot2(R[r][m], R[r][m], q); }
        st[r] = f32x2{dpp_add<0x4E>(dpp_add<0xB1>(s)), dpp_add<0x4E>(dpp_add<0xB1>(q))};
      }
      const int xt = b >> 1, u = (b & 1) * 2 + hw, e = lane & 3;
      const f32x2 a = e == 0 ? st[0] : e == 1 ? st[2] : e == 2 ? st[4] : st[6];
      const f32x2 c = e == 0 ? st[1] : e == 1 ? st[3] : e == 2 ? st[5] : st[7];
      st128<SC1>(__builtin_bit_cast(v4u, f32x4{a.x, a.y, c.x, c.y}), rs, (lane >> 2) * 256 + e * 16, ST + xt * 4096 + u * 64);
      asm volatile("s_waitcnt lgkmcnt(0)" ::: "memory");
#pragma unroll
      for (int i = 0; i < 8; ++i) o[i] = *(LAS v4u*)(img + i * 1024 + kb * 128 + ((nq ^ kb) * 16));
      ovo = kb * 512 + nq * 16;
      obase = XT + xt * 32768 + u * 128;
      cstr = 16384;
      kstr = 4096;
    } else if (kind == 5) {
      const int php = 2 * (b - 224) + hw;
      const int rho = lane & 31, hfp = lane >> 5, hf = (rho >> 2) & 1, f = (rho & 3) + 4 * (rho >> 3);
#pragma unroll
      for (int s = 0; s < 2; ++s) {
        v4u t = {0u, 0u, 0u, 0u};
        if (hf == hfp) {
          const uint16_t* p = wphi + (2 * php + hfp) * 256 + (8 * s) * 16 + f;
#pragma unroll
          for (int m = 0; m < 4; ++m) t[m] = (u32)p[(2 * m) * 16] | ((u32)p[(2 * m + 1) * 16] << 16);
        }
        st128<SC1>(t, rs, lane * 16, WPHI_T + php * 2048 + s * 1024);
      }
    }
    auto flush = [&]() {
#pragma unroll
      for (int i = 0; i < 8; ++i) st128<SC1>(o[i], rs, ovo, obase + (i >> 2) * cstr + (i & 3) * kstr);
      if (kind < 2) __builtin_amdgcn_raw_buffer_store_b32(__builtin_bit_cast(u32, csv), rs, csoff, 0, SC1);
    };
    if (w < 2 && kind < 5) flush();
    STAMP(59);
    if (w == 0) {
#pragma unroll 1
      for (u32 spins = 0;; ++spins) {
        asm volatile("" ::: "memory");
        const v2u t = ld64<SC1>(rs, (lane & 15) * CSTR, INIT_OFF);
        if (__all(t.x == tag_lo && t.y == tag_hi)) break;
        if (spins > (1u << 16)) break;
      }
    }
    DRAIN();
    BAR();
    if (b < 232 && w == 0) add1(rs, l0, cnt_off(C_P + (b < 128 ? b >> 3 : 16 + (b < 224 ? ((b - 128) >> 2) & 7 : b - 224))));
    STAMP(60);
    if (w >= 2) flush();
    if (w == 0) poll2(rs, cnt_off(C_P + rt), 8, cnt_off(C_P + 16 + (hp >> 1)), 13, lane);
    BAR();
    STAMP(61);
    dma_range<0, 16>(rs, lane * 16, WQKV_T + hp * 98304 + w * 16384, L + w * 16384);
  }
  v4u w2f[28][2];

  static_for<2>([&](auto LAYER) __attribute__((always_inline)) {
    constexpr int layer = LAYER;
    constexpr int sb = layer * 29;
    v4u wo[8][2];
    const u32 wb = opq(W2_T + cb * 229376 + w * 28672);
    const u32 wsw = (w & 1) * 1024;
#define IC(n) std::integral_constant<int, n>{}
    {
      const u32 ln = opqv(lane), l16 = ln * 16, r = ln & 31, hf = ln >> 5;
      auto w2ld = [&](auto I) {
        constexpr int i = I;
        w2f[i >> 1][i & 1] = ld128<SC1>(rs, l16 + ((i >> 1) & 1) * 2048, wb + (i / 28) * 114688 + ((i % 28) >> 2) * 4096 + ((i & 1) ? 1024 - wsw : wsw));
      };
      STAMP(sb + 0);
      if constexpr (layer > 0) wwait(rs, l0, cnt_off(C_X + tt), 16);
      STAMP(sb + 1);
      constexpr int DX = DXV;
      v4u xf[32];
      const u32 xb = XT + (2 * layer) * XT_SZ + tt * 32768;
#pragma unroll
      for (int ks = 0; ks < DX; ++ks) { xf[ks] = ldf<SC1>(rs, l16, xb, ks * 1024); SB(); }
      asm volatile("s_waitcnt vmcnt(%0)" ::"n"(DX) : "memory");
      BAR();
      if constexpr (layer == 0) if (w == 0) add1(rs, l0, cnt_off(C_J + (b < 112 ? 16 + (b >> 3) : b < 144 ? (b - 112) >> 2 : (b - 144) / 14)));
      STAMP(sb + 26);
      f32x2 sp[8];
      const u32 stb = ST + (2 * layer) * ST_SZ + tt * 4096;
      const u32 wqb = opq(WQKV_T + hp * 98304 + 65536 + w * 8192);
      const u32 g4 = ln >> 4, c16 = ln & 15;
      LAS unsigned char* a_v = L + opqv(VV_OFF + (32 * w + r) * VV_STR + hf * 32);
      LAS unsigned char* a_pk = L + opqv(PK_OFF + (32 * w + r) * PK_STR + hf * 64);
      LAS unsigned char* a_pa = L + opqv(PK_OFF + (8 * g4 + (c16 >> 2)) * PK_STR + w * 32 + (ln & 3) * 8);
      LAS unsigned char* a_pb = L + opqv(VV_OFF + (8 * g4 + (c16 >> 2)) * VV_STR + (w >> 1) * 32 + (ln & 3) * 8);
      LAS unsigned char* a_kz = L + opqv(KF_OFF + w * 544 + (c16 >> 3) * 272 + 256 + (c16 & 7) * 2);
      LAS unsigned char* a_kw = L + opqv(KF_OFF + w * 544 + (g4 >> 1) * 272 + (g4 & 1) * 8 + rho32(c16 & 7, c16 >> 3) * 16);
      LAS unsigned char* a_kr = L + opqv(KF_OFF + hf * 272 + (r < 16 ? r : 16) * 16);
      const u32 o_z = opqv(c16 == 0 ? g4 * 16 : 0x80000000u), o_st = opqv(hf ? 0x80000000u : r * 8);
      u32 jv = 0, jneed = 0;
      LAS v4u* WL = (LAS v4u*)(L + l16);
      LAS v4u* WQ = (LAS v4u*)(L + opqv(l16 + 65536));
      v4u wk[3], wv[3];
#pragma unroll
      for (int i = 0; i < 2; ++i) { wk[i] = WL[i * 64]; wv[i] = WL[(32 + i) * 64]; }
      f32x16 aK = zero16, aV = zero16, aQ;
      float rstd, nmean, c, cp[3][4];
      SB();
      static_for<32>([&](auto KSI) {
        constexpr int ks = KSI;
        if constexpr (ks + DX < 32) xf[ks + DX] = ldf<SC1>(rs, l16, xb, (ks + DX) * 1024);
        if constexpr (ks >= 24) dma1<ks - 24>(rs, l16, wqb, L + 65536 + w * 8192);
        if constexpr (ks + 2 < 32) { wk[(ks + 2) % 3] = WL[(ks + 2) * 64]; wv[(ks + 2) % 3] = WL[(32 + ks + 2) * 64]; }
        if constexpr (ks < 4) {
#pragma unroll
          for (int i = 2 * ks; i < 2 * ks + 2; ++i) sp[i] = __builtin_bit_cast(f32x2, ld64<SC1>(rs, hf * 2048 + r * 8 + i * 256, stb));
        }
        if constexpr (layer == 0 && ks >= 4 && ks < 7) {
#pragma unroll
          for (int i = 0; i < 4; ++i) cp[ks - 4][i] = __builtin_bit_cast(float, ld32<SC1>(rs, ln * 4 + i * 256 + (ks - 4) * 1024, CS_QKV + hp * 3072));
        }
        if constexpr (layer == 0 && (ks == 21 || ks == 22)) wph[ks - 21] = ld128<SC1>(rs, l16 + (ks - 21) * 1024, WPHI_T + hp * 2048);
        aK = mfma32(wk[ks % 3], xf[ks], aK);
        aV = mfma32(wv[ks % 3], xf[ks], aV);
        if constexpr (layer == 0 && ks == 19) {
#pragma unroll
          for (int t = 0; t < 3; ++t) csq[t] = (cp[t][0] + cp[t][1]) + (cp[t][2] + cp[t][3]);
        }
        if constexpr (ks == 18) {
          f32x2 s2 = ((sp[0] + sp[1]) + (sp[2] + sp[3])) + ((sp[4] + sp[5]) + (sp[6] + sp[7]));
          nmean = sum32(s2.x) * (-1.f / 512.f);
          const float var = fmaxf(sum32(s2.y) * (1.f / 512.f) - nmean * nmean, 0.f);
          rstd = __builtin_amdgcn_rsqf(var + 1e-5f);
          c = rstd * LOG2E;
          PIN(c);
          PIN(rstd);
        }
        if constexpr (layer == 0 && ks == 31) {
          jv = peek(rs, w != 0 ? 0x80000000u : ln == 0 ? cnt_off(C_J + cb) : ln == 1 ? cnt_off(C_J + 16 + fb) : 0x80000000u, 0);
          jneed = w != 0 ? 0u : (ln & 1) ? 8u : 18u;
        }
        if constexpr (ks == 20) aK = mfma32f(csq[0], nmean, aK);
        if constexpr (ks == 22) aV = mfma32f(csq[1], nmean, aV);
        SB();
      });
      STAMP(sb + 2);
      const u32 w1b = opq(W1_T + fb * 131072);
      auto w1dma = [&](auto I) {
        constexpr int i = I;
        if constexpr (i < 16) dma1<i>(rs, l16, w1b + w * 16384, L + w * 16384);
        else if constexpr (i < 24) dma1<i - 16>(rs, l16, w1b + 98304 + w * 8192, L + 98304 + w * 8192);
        else dma1<i - 24>(rs, l16, w1b + 65536 + w * 8192, L + 65536 + w * 8192);
      };
      const u32 wob = opq(WO_T + (cb * 4 + w) * 16384);
      auto wold = [&](auto I) {
        constexpr int i = I;
        wo[i >> 1][i & 1] = ld128<SC1>(rs, l16 + ((i >> 1) & 1) * 2048, wob + (i >> 2) * 4096 + ((i & 1) ? 1024 - wsw : wsw));
      };
      {
        const v4u k0 = pack8(aK, 0), k1 = pack8(aK, 1);
        f32x16 p = mfma32(wph[0], k0, zero16);
        p = mfma32(wph[1], k1, p);
        SB();
#pragma unroll
        for (int i = 0; i < 16; ++i) { aV[i] *= rstd; PIN(aV[i]); }
        SB();
        *(LAS v4u*)(a_v) = pack8(aV, 0);
        *(LAS v4u*)(a_v + 16) = pack8(aV, 1);
        SB();
        u32 o[16];
        dual_softmax(p, c, o, [&](auto I) {});
#pragma unroll
        for (int i = 0; i < 4; ++i) *(LAS v4u*)(a_pk + i * 16) = v4u{o[4 * i], o[4 * i + 1], o[4 * i + 2], o[4 * i + 3]};
      }
      SB();
      aQ = mfma32f(csq[2], nmean, zero16);
      STAMP(sb + 3);
      DRAIN();
      if constexpr (layer == 0) {
        if ((__builtin_amdgcn_ballot_w64(jv >= jneed) & 3ull) != 3ull) poll2(rs, cnt_off(C_J + cb), 18, cnt_off(C_J + 16 + fb), 8, ln);
      }
      BAR();
      const u32 pbase = KVP + layer * KVP_SZ + (4 * hp + w) * KVP_HS;
      v4u wq[4];
      {
        f32x4 kv = {0.f, 0.f, 0.f, 0.f}, zz = {0.f, 0.f, 0.f, 0.f};
        const v4u ones = {ONES2, ONES2, ONES2, ONES2};
        v2u a0[4], a1[4], b0[4], b1[4];
#pragma unroll
        for (int ks = 0; ks < 4; ++ks) {
          a0[ks] = trread(a_pa + (32 * ks) * PK_STR); a1[ks] = trread(a_pa + (32 * ks + 4) * PK_STR);
          b0[ks] = trread(a_pb + (32 * ks) * VV_STR); b1[ks] = trread(a_pb + (32 * ks + 4) * VV_STR);
          SB();
        }
#pragma unroll
        for (int ks = 0; ks < 4; ++ks) {
          const v4u A = {a0[ks].x, a0[ks].y, a1[ks].x, a1[ks].y}, B = {b0[ks].x, b0[ks].y, b1[ks].x, b1[ks].y};
          kv = mfma16(A, B, kv);
          zz = mfma16(A, ones, zz);
          SB();
        }
#pragma unroll
        for (int ks = 0; ks < 4; ++ks) wq[ks] = WQ[ks * 64];
        SB();
        st128<SC1>(__builtin_bit_cast(v4u, kv), rs, l16, pbase + rt * 1024);
        st128<SC1>(__builtin_bit_cast(v4u, zz), rs, o_z, pbase + 16384 + rt * 64);
      }
      SB();
      STAMP(sb + 4);
      auto qstep = [&](auto I) {
        constexpr int i = I;
        aQ = mfma32(wq[i & 3], xf[i], aQ);
        SB();
        if constexpr (i + 4 < 32) wq[i & 3] = WQ[(i + 4) * 64];
      };
      static_for<18>([&](auto I) { qstep(I); if constexpr (I % 2 == 0) w1dma(IC(I / 2)); SB(); });
      const u32 ckv = cnt_off(C_KV + layer * 64 + hp * 4 + w);
      wsignal<9>(rs, l0, ckv);
      STAMP(sb + 5);
      static_for<14>([&](auto I) { qstep(IC(18 + I)); if constexpr (I % 2 == 0) wold(IC(I / 2)); SB(); });
      const u32 pvkv = peek(rs, l0, ckv);
      SB();
      const v4u q0 = pack8(aQ, 0), q1 = pack8(aQ, 1);
      f32x16 pq = mfma32(wph[0], q0, zero16);
      pq = mfma32(wph[1], q1, pq);
      SB();
      v4u phq[4];
      f32x4 v[16];
      float z[4];
      {
        u32 o[16];
        dual_softmax(pq, c, o, [&](auto I) {
          constexpr int i = I;
          if constexpr (i == KVCHK) {
            STAMP(sb + 6);
            wfinish(rs, l0, ckv, 16, pvkv);
            STAMP(sb + 7);
#pragma unroll
            for (int k = 0; k < 4; ++k) z[k] = __builtin_bit_cast(float, ld32<SC1>(rs, ln * 4 + k * 256, pbase + 16384));
#pragma unroll
            for (int k = 0; k < 16; ++k) { v[k] = __builtin_bit_cast(f32x4, ldf<SC1>(rs, l16, pbase, k * 1024)); SB(); }
          }
        });
#pragma unroll
        for (int m = 0; m < 4; ++m) {
          auto sw = __builtin_amdgcn_permlane32_swap(o[m], o[4 + m], false, false);
          phq[0][m] = sw[0];
          phq[2][m] = sw[1];
          auto sx = __builtin_amdgcn_permlane32_swap(o[8 + m], o[12 + m], false, false);
          phq[1][m] = sx[0];
          phq[3][m] = sx[1];
        }
#pragma unroll
        for (int m = 0; m < 4; ++m) PIN(phq[m]);
      }
      SB();
      static_for<9>([&](auto I) { wold(IC(7 + I)); });
      SB();
      {
        const float zs = sum32(sum16((z[0] + z[1]) + (z[2] + z[3])));
        *(LAS unsigned short*)(a_kz) = (unsigned short)pk(zs, zs);
        SB();
        f32x4 sv = v[0];
#pragma unroll
        for (int i = 1; i < 16; ++i) sv += v[i];
        *(LAS v2u*)(a_kw) = v2u{pk(sv[0], sv[1]), pk(sv[2], sv[3])};
      }
      BAR();
      STAMP(sb + 8);
      {
        v4u kvf[4];
#pragma unroll
        for (int i = 0; i < 4; ++i) kvf[i] = *(LAS v4u*)(a_kr + i * 544);
        SB();
        f32x16 nd[2];
        nd[0] = mfma32(kvf[0], phq[0], zero16);
        nd[0] = mfma32(kvf[1], phq[1], nd[0]);
        nd[1] = mfma32(kvf[2], phq[2], zero16);
        nd[1] = mfma32(kvf[3], phq[3], nd[1]);
        SB();
#pragma unroll
        for (int h = 0; h < 2; ++h) {
          const float inv = __builtin_amdgcn_rcpf(nd[h][8] + 1e-6f);
          v4u ob;
#pragma unroll
          for (int m = 0; m < 4; ++m) ob[m] = pk(nd[h][2 * m] * inv, nd[h][2 * m + 1] * inv);
          st128<SC1>(ob, rs, l16 + h * 1024, OB + layer * XT_SZ + tt * 32768 + (2 * hp) * 1024);
          SB();
        }
      }
      STAMP(sb + 9);
      static_for<8>([&](auto I) { w1dma(IC(9 + I)); });
      BAR();
      wsignal<8>(rs, l0, cnt_off(C_O + layer * 128 + (tt >> 1) * 4 + (hp >> 2)));
      STAMP(sb + 10);
      wwait(rs, l0, cnt_off(C_O + layer * 128 + rt2 * 4 + w), 8);
      STAMP(sb + 11);
      const u32 ab = OB + layer * XT_SZ + (8 * w) * 1024;
      splitk_phase<1, 8, DXV, 0>(rs, wo, ab + tte * 32768, ab + (tte ^ 1) * 32768, L, w, l16, 1.f, res, [&]() {}, [&](auto I) {
        if constexpr (I >= 4 && I < 15) w1dma(IC(17 + I - 4));
      });
      STAMP(sb + 12);
      resid_store(rs, res, l16, o_st, XT + (2 * layer + 1) * XT_SZ + tte * 32768 + (n0 >> 3) * 512, ST + (2 * layer + 1) * ST_SZ + tte * 4096 + (n0 >> 5) * 256);
      STAMP(sb + 13);
      static_for<4>([&](auto I) { w1dma(IC(28 + I)); });
      if constexpr (layer == 0) static_for<W2D>([&](auto I) { w2ld(I); });
      wsignal<layer == 0 ? 4 + W2D : 4>(rs, l0, cnt_off(C_X1 + layer * 64 + tte));
      STAMP(sb + 14);
    }
    u32 pvh;
    const u32 chA = cnt_off(C_H + (layer * 64 + rt2) * 4 + w), chB = chA + 128 * CSTR;
    const u32 shA = cnt_off(C_H + (layer * 64 + (tt >> 1)) * 4 + (2 * fb) / 7);
    {
      const u32 ln = opqv(lane), l16 = ln * 16, r = ln & 31, hf = ln >> 5;
      auto w2ld = [&](auto I) {
        constexpr int i = I;
        w2f[i >> 1][i & 1] = ld128<SC1>(rs, l16 + ((i >> 1) & 1) * 2048, wb + (i / 28) * 114688 + ((i % 28) >> 2) * 4096 + ((i & 1) ? 1024 - wsw : wsw));
      };
      if (p3on) {
        STAMP(sb + 15);
        wwait(rs, l0, cnt_off(C_X1 + layer * 64 + tt), 16);
        STAMP(sb + 16);
        constexpr int DX = DXV;
        v4u xf[32];
        const u32 xb = XT + (2 * layer + 1) * XT_SZ + tt * 32768;
#pragma unroll
        for (int ks = 0; ks < DX; ++ks) { xf[ks] = ldf<SC1>(rs, l16, xb, ks * 1024); SB(); }
        asm volatile("s_waitcnt vmcnt(%0)" ::"n"(DX) : "memory");
        BAR();
        f32x2 sp[8];
        const u32 stb = ST + (2 * layer + 1) * ST_SZ + tt * 4096;
        LAS v4u* WL = (LAS v4u*)(L + l16);
        LAS v4u* WH = (LAS v4u*)(L + opqv(l16 + 65536));
        v4u wa[3], wc[3], wq[4];
#pragma unroll
        for (int i = 0; i < 2; ++i) { wa[i] = WL[i * 64]; wc[i] = WL[(32 + i) * 64]; }
        f32x16 acc[4];
        acc[0] = zero16;
        acc[1] = zero16;
        float nmean, nc, cp[4][4];
        SB();
        static_for<32>([&](auto KSI) {
          constexpr int ks = KSI;
          acc[0] = mfma32(wa[ks % 3], xf[ks], acc[0]);
          SB();
          if constexpr (ks + 2 < 32) wa[(ks + 2) % 3] = WL[(ks + 2) * 64];
          else wq[2 * (ks - 30)] = WH[(2 * (ks - 30)) * 64];
          if constexpr (ks + DX < 32) xf[ks + DX] = ldf<SC1>(rs, l16, xb, (ks + DX) * 1024);
          if constexpr (layer == 0 && ks >= 20) w2ld(IC(W2D + ks - 20));
          SB();
          acc[1] = mfma32(wc[ks % 3], xf[ks], acc[1]);
          SB();
          if constexpr (ks + 2 < 32) wc[(ks + 2) % 3] = WL[(32 + ks + 2) * 64];
          else wq[2 * (ks - 30) + 1] = WH[(2 * (ks - 30) + 1) * 64];
          if constexpr (ks < 4) {
#pragma unroll
            for (int i = 2 * ks; i < 2 * ks + 2; ++i) sp[i] = __builtin_bit_cast(f32x2, ld64<SC1>(rs, hf * 2048 + r * 8 + i * 256, stb));
          }
          if constexpr (layer == 0 && ks >= 4 && ks < 8) {
#pragma unroll
            for (int i = 0; i < 4; ++i) cp[ks - 4][i] = __builtin_bit_cast(float, ld32<SC1>(rs, ln * 4 + i * 256 + (ks - 4) * 1024, CS_W1 + fb * 4096));
          }
          if constexpr (ks == 18) {
            f32x2 s2 = ((sp[0] + sp[1]) + (sp[2] + sp[3])) + ((sp[4] + sp[5]) + (sp[6] + sp[7]));
            nmean = sum32(s2.x) * (-1.f / 512.f);
            const float var = fmaxf(sum32(s2.y) * (1.f / 512.f) - nmean * nmean, 0.f);
            nc = __builtin_amdgcn_rsqf(var + 1e-5f) * -LOG2E;
            PIN(nc);
          }
          if constexpr (layer == 0 && ks == 20) {
#pragma unroll
            for (int t = 0; t < 4; ++t) cs1[t] = (cp[t][0] + cp[t][1]) + (cp[t][2] + cp[t][3]);
          }
          if constexpr (ks == 21) acc[0] = mfma32f(cs1[0], nmean, acc[0]);
          if constexpr (ks == 23) acc[1] = mfma32f(cs1[1], nmean, acc[1]);
          if constexpr (ks == 25) acc[2] = mfma32f(cs1[2], nmean, zero16);
          if constexpr (ks == 27) acc[3] = mfma32f(cs1[3], nmean, zero16);
          SB();
        });
        STAMP(sb + 17);
        const u32 hb = HB + layer * HB_SZ + tt * 114688 + fb * 4096;
        float tv[64], ev[64];
        v4u ho[8];
        auto stage = [&](auto V, auto ST_) {
          constexpr int v = V, st = ST_;
          if constexpr (st == 0) { tv[v] = acc[v >> 4][v & 15] * nc; PIN(tv[v]); }
          else if constexpr (st == 1) { ev[v] = __builtin_amdgcn_exp2f(tv[v]); PIN(ev[v]); }
          else if constexpr (st == 2) { ev[v] = 1.f + ev[v]; PIN(ev[v]); }
          else if constexpr (st == 3) { ev[v] = __builtin_amdgcn_rcpf(ev[v]); PIN(ev[v]); }
          else if constexpr (st == 4) { tv[v] = tv[v] * ev[v]; PIN(tv[v]); }
          else if constexpr (v & 1) {
            u32 hh = pk(tv[v - 1], tv[v]);
            PIN(hh);
            ho[v >> 3][(v >> 1) & 3] = hh;
            if constexpr ((v & 7) == 7) st128<SC1>(ho[v >> 3], rs, l16 + ((v >> 3) & 3) * 1024, hb + (v >> 5) * 57344);
          }
        };
        static_for<64>([&](auto GI) {
          constexpr int gi = GI, t = 2 + (gi >> 5), ks = gi & 31;
          acc[t] = mfma32(wq[gi & 3], xf[ks], acc[t]);
          SB();
          if constexpr (gi + 4 < 64) wq[gi & 3] = WH[(gi + 4) * 64];
          if constexpr (layer == 0 && p3_w2gap(gi)) w2ld(IC(W2D + 12 + (gi * W2B) / 8));
          SB();
          static_for<48>([&](auto V) {
            constexpr int d = gi - p3_g0(V);
            if constexpr (d >= 0 && d < 6) stage(V, IC(d));
          });
          if constexpr (gi == HSIG) wsignal<p3_younger(layer)>(rs, lh, shA);
          if constexpr (gi == 63) pvh = peek(rs, l0, chA);
          SB();
        });
        STAMP(sb + 18);
        constexpr int W2E = 56 - W2D - 12 - 8 * W2B;
        if constexpr (layer == 0) static_for<W2E>([&](auto E) { w2ld(IC(W2D + 12 + 8 * W2B + E)); });
        {
          f32x2 t2[8], e2[8];
#pragma unroll
          for (int m = 0; m < 8; ++m) { t2[m] = f32x2{acc[3][2 * m], acc[3][2 * m + 1]} * nc; PIN(t2[m]); }
          SB();
#pragma unroll
          for (int m = 0; m < 8; ++m) { e2[m] = f32x2{__builtin_amdgcn_exp2f(t2[m].x), __builtin_amdgcn_exp2f(t2[m].y)}; PIN(e2[m]); }
          SB();
#pragma unroll
          for (int m = 0; m < 8; ++m) { e2[m] = e2[m] + 1.f; PIN(e2[m]); }
          SB();
#pragma unroll
          for (int m = 0; m < 8; ++m) { e2[m] = f32x2{__builtin_amdgcn_rcpf(e2[m].x), __builtin_amdgcn_rcpf(e2[m].y)}; PIN(e2[m]); }
          SB();
#pragma unroll
          for (int m = 0; m < 8; ++m) { t2[m] = t2[m] * e2[m]; PIN(t2[m]); }
          SB();
#pragma unroll
          for (int s = 0; s < 2; ++s)
            st128<SC1>(v4u{pk(t2[4 * s].x, t2[4 * s].y), pk(t2[4 * s + 1].x, t2[4 * s + 1].y), pk(t2[4 * s + 2].x, t2[4 * s + 2].y), pk(t2[4 * s + 3].x, t2[4 * s + 3].y)},
                       rs, l16 + (2 + s) * 1024, hb + 57344);
        }
        SB();
        STAMP(sb + 19);
      } else {
        if constexpr (layer == 0) static_for<56 - W2D>([&](auto E) { w2ld(IC(W2D + E)); });
        DRAIN();
        BAR();
        pvh = 0;
      }
    }
    {
      const u32 ln = opqv(lane), l16 = ln * 16, r = ln & 31, hf = ln >> 5;
      STAMP(sb + 21);
      wfinish(rs, l0, chA, 8, pvh);
      STAMP(sb + 22);
      const u32 hb = HB + layer * HB_SZ + (14 * w) * 1024;
      const u32 wqb = opq(WQKV_T + hp * 98304 + w * 16384);
      u32 pvb;
      splitk_phase<2, 14, WINH, 57344>(rs, w2f, hb + tte * 114688, hb + (tte ^ 1) * 114688, L, w, l16, -LN2, res,
        [&]() { wsignal<WINH>(rs, lh, shA + 128 * CSTR); STAMP(sb + 20); },
        [&](auto I) {
          if constexpr (I == BPEEK) pvb = peek(rs, l0, chB);
          if constexpr (I == 28 - WINH) { STAMP(sb + 27); wfinish(rs, l0, chB, 8, pvb); STAMP(sb + 28); }
          if constexpr (layer == 0 && I >= 48) dma1<I - 48>(rs, l16, wqb, L + w * 16384);
        });
      STAMP(sb + 23);
      if constexpr (layer == 0) {
        resid_store(rs, res, l16, hf ? 0x80000000u : r * 8, XT + 2 * XT_SZ + tte * 32768 + (n0 >> 3) * 512, ST + 2 * ST_SZ + tte * 4096 + (n0 >> 5) * 256);
        STAMP(sb + 24);
        dma_range<8, 16>(rs, l16, wqb, L + w * 16384);
        wsignal<8>(rs, l0, cnt_off(C_X + tte));
        STAMP(sb + 25);
      } else {
        const rsrc_t rso = mkrsrc(out, S * D * 2);
#pragma unroll
        for (int s = 0; s < 2; ++s) st128<0>(res[s], rso, r * 1024 + hf * 16 + s * 32, tte * 32768 + n0 * 2);
        if (b == 255 && tid == 0) st64<SC1>(v2u{tag_lo, tag_hi}, rs, 0, EPOCH_OFF);
        STAMP(sb + 24);
      }
    }
  });
}

extern "C" void kernel_launch(void* const* d_in, const int* in_sizes, int n_in, void* d_out, int out_size, void* d_ws,
                              size_t ws_size, hipStream_t stream) {
  (void)hipFuncSetAttribute((const void*)k_main, hipFuncAttributeMaxDynamicSharedMemorySize, LDS_BYTES);
  k_main<<<256, 256, LDS_BYTES, stream>>>((const uint16_t*)d_in[0], (const uint16_t*)d_in[1], (const uint16_t*)d_in[2],
                                          (const uint16_t*)d_in[3], (const uint16_t*)d_in[5], (const uint16_t*)d_in[6],
                                          (unsigned char*)d_ws, (unsigned char*)d_out);
}
```

```cpp
#include <hip/hip_runtime.h>
#include <hip/hip_bf16.h>
#include <stdint.h>
#include <utility>

typedef unsigned int u32;
typedef u32 v4u __attribute__((ext_vector_type(4)));
typedef u32 v2u __attribute__((ext_vector_type(2)));
typedef short s16x4 __attribute__((ext_vector_type(4)));
typedef __bf16 bf16x8 __attribute__((ext_vector_type(8)));
typedef __bf16 bf16x2 __attribute__((ext_vector_type(2)));
typedef float f32x2 __attribute__((ext_vector_type(2)));
typedef float f32x4 __attribute__((ext_vector_type(4)));
typedef float f32x16 __attribute__((ext_vector_type(16)));
#define LAS __attribute__((address_space(3)))
#define GAS __attribute__((address_space(1)))
#define DI __device__ __forceinline__
typedef __amdgpu_buffer_rsrc_t rsrc_t;

#define DXV 12
#define WINH 14
#define HS0 36
#define HS1 56
#define PK0 52
#define HPRE 0
#define PK2 2
#define T3SIG -1
#define PK3 10
#define T3CHK 24
#define KVCHK 1
#define KVLD 2
#define ODR 8
#define OAUX 0
#define PS1 3
#define PS2 4
#define XMODE 0
#define KMODE 0
#define NQ1 14
#define NQ2 7
#define OPL0 4
#define OPL1 4
#define XDR 4
#define OMODE 1
#define PS4 8
#define KWPOS 1
#define PS3 4
#define WOK 2
#define ROT 1
#define XBAR 1
#define JPK 0
#define XA 8
#define XB 20
#define UB 10
#define FPK 4
#define FCK 6
#define KVPK1 6
#define TLBW 262144
#define W2D 12
#define W2B 4
constexpr int S = 2048, D = 512;
constexpr u32 CTRL = 0;
constexpr u32 WQKV_T = 262144;
constexpr u32 WPHI_T = WQKV_T + 16 * 98304;
constexpr u32 WO_T = WPHI_T + 32768;
constexpr u32 W1_T = WO_T + 524288;
constexpr u32 W2_T = W1_T + 14 * 131072;
constexpr u32 CS_QKV = W2_T + 8 * 4 * 28 * 2048;
constexpr u32 CS_W1 = CS_QKV + 65536;
constexpr u32 XT = CS_W1 + 65536;
constexpr u32 XT_SZ = 2097152;
constexpr u32 ST = XT + 4 * XT_SZ;
constexpr u32 ST_SZ = 262144;
constexpr u32 OB = ST + 4 * ST_SZ;
constexpr u32 HB = OB + 2 * XT_SZ;
constexpr u32 HB_SZ = 64 * 114688;
constexpr u32 KVP = HB + 2 * HB_SZ;
constexpr u32 KVP_HS = 16 * 512 + 2048;
constexpr u32 KVP_SZ = 64 * KVP_HS;
constexpr u32 WS_END = KVP + 2 * KVP_SZ;
#define CSTR 128
enum { C_X = 0, C_KV = 64, C_O = 192, C_X1 = 448, C_H = 576, C_P = 1600, C_J = 1632, C_TMO = 1663, NCNT = 1664 };
constexpr u32 INIT_OFF = CTRL + NCNT * CSTR, EPOCH_OFF = INIT_OFF + 16 * CSTR, FLAG_OFF = EPOCH_OFF + CSTR;
static_assert(FLAG_OFF + 192 * CSTR <= WQKV_T, "control area");

#define STAMP(i)
constexpr float LOG2E = 1.4426950408889634f;
constexpr float LN2 = 0.6931471805599453f;

DI u32 pk(float a, float b) {
  f32x2 v = {a, b};
  bf16x2 r = __builtin_convertvector(v, bf16x2);
  return __builtin_bit_cast(u32, r);
}
DI float bflo(u32 u) { return __builtin_bit_cast(float, u << 16); }
DI float bfhi(u32 u) { return __builtin_bit_cast(float, u & 0xffff0000u); }
DI f32x16 mfma32(v4u a, v4u b, f32x16 c) {
  f32x16 d = __builtin_amdgcn_mfma_f32_32x32x16_bf16(__builtin_bit_cast(bf16x8, a), __builtin_bit_cast(bf16x8, b), c, 0, 0, 0);
  asm volatile("" : "+a"(d));
  return d;
}
DI f32x4 mfma16(v4u a, v4u b, f32x4 c) {
  return __builtin_amdgcn_mfma_f32_16x16x32_bf16(__builtin_bit_cast(bf16x8, a), __builtin_bit_cast(bf16x8, b), c, 0, 0, 0);
}
DI f32x16 mfma32f(float a, float b, f32x16 c) {
  f32x16 d = __builtin_amdgcn_mfma_f32_32x32x2f32(a, b, c, 0, 0, 0);
  asm volatile("" : "+a"(d));
  return d;
}
template <int N> DI f32x16 mfma32f_pad(float a, float b, f32x16 c) {
  asm volatile("s_nop %1" : "+a"(c) : "n"(N));
  return mfma32f(a, b, c);
}
DI float dot2(u32 a, u32 b, float c) {
  return __builtin_amdgcn_fdot2_f32_bf16(__builtin_bit_cast(bf16x2, a), __builtin_bit_cast(bf16x2, b), c, false);
}
template <int AUX> DI v4u ld128(rsrc_t rs, u32 voff, u32 soff) { return __builtin_amdgcn_raw_buffer_load_b128(rs, voff, soff, AUX); }
template <int AUX> DI v2u ld64(rsrc_t rs, u32 voff, u32 soff) { return __builtin_amdgcn_raw_buffer_load_b64(rs, voff, soff, AUX); }
template <int AUX> DI u32 ld32(rsrc_t rs, u32 voff, u32 soff) { return __builtin_amdgcn_raw_buffer_load_b32(rs, voff, soff, AUX); }
template <int AUX> DI void st128(v4u v, rsrc_t rs, u32 voff, u32 soff) {
  __builtin_amdgcn_raw_buffer_store_b128(v, rs, voff, soff, AUX);
  asm volatile("s_nop 1" ::"v"(v) : "memory");
}
template <int AUX> DI void st64(v2u v, rsrc_t rs, u32 voff, u32 soff) { __builtin_amdgcn_raw_buffer_store_b64(v, rs, voff, soff, AUX); }
DI rsrc_t mkrsrc(const void* p, u32 bytes) { return __builtin_amdgcn_make_buffer_rsrc((void*)p, 0, (int)bytes, 0x00020000); }
DI float sum32(float v) {
  u32 u = __builtin_bit_cast(u32, v);
  auto r = __builtin_amdgcn_permlane32_swap(u, u, false, false);
  const u32 r0 = r[0], r1 = r[1];
  return __builtin_bit_cast(float, r0) + __builtin_bit_cast(float, r1);
}
DI float sum16(float v) {
  u32 u = __builtin_bit_cast(u32, v);
  auto r = __builtin_amdgcn_permlane16_swap(u, u, false, false);
  const u32 r0 = r[0], r1 = r[1];
  return __builtin_bit_cast(float, r0) + __builtin_bit_cast(float, r1);
}
DI float mix16(float a, float b) {
  auto r = __builtin_amdgcn_permlane16_swap(__builtin_bit_cast(u32, a), __builtin_bit_cast(u32, b), false, false);
  const u32 r0 = r[0], r1 = r[1];
  return __builtin_bit_cast(float, r0) + __builtin_bit_cast(float, r1);
}
DI v2u trread(LAS unsigned char* __restrict__ p) {
  return __builtin_bit_cast(v2u, __builtin_amdgcn_ds_read_tr16_b64_v4i16((LAS s16x4*)p));
}
DI int rho32(int reg, int hf) { return (reg & 3) + 8 * (reg >> 2) + 4 * hf; }
constexpr u32 ONES2 = 0x3f803f80u;

DI v4u pack8(const f32x16& a, int s) {
  v4u r;
#pragma unroll
  for (int m = 0; m < 4; ++m) r[m] = pk(a[8 * s + 2 * m], a[8 * s + 2 * m + 1]);
  return r;
}

#define SB() __builtin_amdgcn_sched_barrier(0)
#define PIN(x) asm volatile("" : "+v"(x))
#define BAR() asm volatile("s_waitcnt lgkmcnt(0)\n\ts_barrier" ::: "memory")
#define DRAIN() asm volatile("s_waitcnt vmcnt(0)" ::: "memory")
#define RLX __ATOMIC_RELAXED, __HIP_MEMORY_SCOPE_AGENT
constexpr int SC1 = 16;

DI u32 opq(u32 x) { asm volatile("" : "+s"(x)); return x; }
DI u32 opqv(u32 x) { asm volatile("" : "+v"(x)); return x; }
template <int AUX> DI v4u ldf(rsrc_t rs, u32 vo, u32 sbase, u32 off) { return ld128<AUX>(rs, vo + (off & 4095u), sbase + (off & ~4095u)); }
template <int I> DI void dma1(rsrc_t rs, u32 vo, u32 sbase, LAS unsigned char* d) {
  __builtin_amdgcn_raw_ptr_buffer_load_lds(rs, (LAS u32*)(d + (I >> 2) * 4096), 16, vo, sbase + (I >> 2) * 4096, (I & 3) * 1024, SC1);
}
template <int I0, int I1> DI void dma_range(rsrc_t rs, u32 vo, u32 sbase, LAS unsigned char* d) {
  if constexpr (I0 < I1) {
    dma1<I0>(rs, vo, sbase, d);
    dma_range<I0 + 1, I1>(rs, vo, sbase, d);
  }
}
template <class F, int... I> DI void static_for_impl(F&& f, std::integer_sequence<int, I...>) { (f(std::integral_constant<int, I>{}), ...); }
template <int N, class F> DI void static_for(F&& f) { static_for_impl(f, std::make_integer_sequence<int, N>{}); }
DI u32 cnt_off(int idx) { return CTRL + idx * CSTR; }
DI u32 lane0_off() { return (threadIdx.x & 63) == 0 ? 0u : 0x80000000u; }
DI void add1(rsrc_t rs, u32 l0, u32 coff) { (void)__builtin_amdgcn_raw_ptr_buffer_atomic_add_i32(1, rs, l0, coff, 0); }
template <int N> DI void wsignal(rsrc_t rs, u32 l0, u32 coff) {
  SB();
  asm volatile("s_waitcnt vmcnt(%0)" ::"n"(N) : "memory");
  add1(rs, l0, coff);
  SB();
}
DI u32 peek(rsrc_t rs, u32 l0, u32 coff) { return __builtin_amdgcn_raw_ptr_buffer_atomic_add_i32(0, rs, l0, coff, 0); }
DI void wfinish(rsrc_t rs, u32 l0, u32 coff, u32 n, u32 pv) {
  if (__builtin_amdgcn_readfirstlane(pv) < n) {
#pragma unroll 1
    for (u32 spins = 0;; ++spins) {
      if (__builtin_amdgcn_readfirstlane(peek(rs, l0, coff)) >= n) break;
      if (spins > (1u << 16)) { __builtin_amdgcn_raw_buffer_store_b32(1u, rs, 0, cnt_off(C_TMO), SC1); break; }
    }
  }
  asm volatile("" ::: "memory");
}
DI void wwait(rsrc_t rs, u32 l0, u32 coff, u32 n) { wfinish(rs, l0, coff, n, peek(rs, l0, coff)); }
DI void apeek(rsrc_t rs, u32 l0, u32 coff, u32& p) {
  SB();
  asm volatile("v_mov_b32 %0, 0\n\tbuffer_atomic_add %0, %1, %2, %3 offen sc0 ; APEEK" : "=&v"(p) : "v"(l0), "s"(rs), "s"(coff) : "memory");
  SB();
}
#define PCHK(P, W) "s_waitcnt vmcnt(" W ")\n\tv_readfirstlane_b32 %3, " P "\n\ts_cmp_ge_u32 %3, %8\n\ts_cbranch_scc1 .Lpd%=\n\tv_mov_b32 " P ", 0\n\tbuffer_atomic_add " P ", %5, %6, %7 offen sc0\n\t"
template <int N0, int N1, int N2> DI void pfinish(rsrc_t rs, u32 l0, u32 coff, u32 n, u32 (&p)[3]) {
  u32 t, cnt;
  SB();
  asm volatile(PCHK("%0", "%9") PCHK("%1", "%10") PCHK("%2", "%11") "s_movk_i32 %4, 0x7fff\n.Lpl%=:\n\t" PCHK("%0", "2") PCHK("%1", "2") PCHK("%2", "2")
               "s_sub_u32 %4, %4, 1\n\ts_cbranch_scc0 .Lpl%=\n.Lpd%=:"
               : "+v"(p[0]), "+v"(p[1]), "+v"(p[2]), "=&s"(t), "=&s"(cnt)
               : "v"(l0), "s"(rs), "s"(coff), "n"(n), "n"(N0), "n"(N1 + 1), "n"(N2 + 2)
               : "memory", "scc");
  SB();
}
DI void pretire(u32 (&p)[3]) { asm volatile("; PRETIRE %0 %1 %2" ::"v"(p[0]), "v"(p[1]), "v"(p[2])); }
#define NAP(n) do { SB(); __builtin_amdgcn_s_sleep(n); SB(); } while (0)
DI void poll3(rsrc_t rs, u32 vo, u32 need) {
#pragma unroll 1
  for (u32 spins = 0;; ++spins) {
    const u32 v = peek(rs, vo, 0);
    if ((__builtin_amdgcn_ballot_w64(v >= need) & 7ull) == 7ull) break;
    if (spins > (1u << 16)) { __builtin_amdgcn_raw_buffer_store_b32(1u, rs, 0, cnt_off(C_TMO), SC1); break; }
  }
  asm volatile("" ::: "memory");
}
template <int CTRL_> DI float dpp_add(float v) {
  return v + __builtin_bit_cast(float, __builtin_amdgcn_update_dpp(0, __builtin_bit_cast(int, v), CTRL_, 0xf, 0xf, true));
}

constexpr int SCR = 131072;
constexpr int PK_OFF = SCR, PK_STR = 144, VV_OFF = PK_OFF + 128 * PK_STR, VV_STR = 80, KF_OFF = VV_OFF + 128 * VV_STR;
constexpr int XS_OFF = 98304;
constexpr int RED_OFF = SCR;
constexpr int LDS_BYTES = 163840;
static_assert(KF_OFF + 4 * 544 <= LDS_BYTES, "lds");

DI void lds_put1(LAS unsigned char* p, const f32x16& a, int q) {
  *(LAS f32x4*)(p + q * 1024) = f32x4{a[4 * q], a[4 * q + 1], a[4 * q + 2], a[4 * q + 3]};
}

extern __shared__ __attribute__((aligned(16))) unsigned char smem[];

struct Sch { int put2[4], put3[4], bar1, rd[8], fu[8], put1[4], bar2, rd1, f1[4]; };
constexpr Sch sch_of(int KL) {
  return KL >= 14 ? Sch{{0, 1, 2, 3}, {4, 5, 6, 7}, 9, {9, 9, 9, 10, 10, 10, 11, 11}, {12, 13, 14, 19, 21, 22, 23, 24}, {15, 16, 17, 18}, 20, 20, {25, 26, 27, 27}}
                  : Sch{{0, 0, 1, 1}, {2, 2, 3, 3}, 5, {5, 5, 5, 5, 6, 6, 6, 6}, {7, 7, 8, 8, 11, 12, 12, 13}, {9, 9, 10, 10}, 11, 11, {14, 14, 15, 15}};
}
struct FrO {
  static constexpr int NS = 16, KL = 8;
  static constexpr bool own(int i) { return i >= 8; }
  static constexpr int ks(int i) { return i & 7; }
  static constexpr u32 off(int i) { return (i & 7) * 1024; }
  static constexpr int lstep(int q, int win) { return q - win; }
};
struct FrH {
  static constexpr int NS = 56, KL = 14;
  static constexpr int part(int i) { return i < 28 ? i / 14 : 2 + ((i / 7) & 1); }
  static constexpr bool own(int i) { return i < 28 ? ((i / 7) & 1) : i >= 42; }
  static constexpr int ks(int i) { return part(i) * 7 + i % 7; }
  static constexpr u32 off(int i) { return part(i) * 28672 + (i % 7) * 1024; }
  static constexpr int lstep(int q, int win) { return q >= 35 && q - win < T3CHK ? T3CHK : q - win; }
};
template <class Fr> DI v4u ld_fr(rsrc_t rs, u32 l16, u32 a_own, u32 a_oth, int i) { return ldf<SC1>(rs, l16, Fr::own(i) ? a_own : a_oth, Fr::off(i)); }
template <class Fr, int WIN, int PRE, class Pre, class Mid>
DI void splitk_phase(rsrc_t rs, const v4u (&wf)[Fr::NS / 2][2], v4u (&af)[WIN], u32 a_own, u32 a_oth, LAS unsigned char* L, int w, u32 l16, float scale,
                     v4u (&res)[2], Pre&& pre_loop, Mid&& mid) {
  const f32x16 zero16 = {0.f, 0.f, 0.f, 0.f, 0.f, 0.f, 0.f, 0.f, 0.f, 0.f, 0.f, 0.f, 0.f, 0.f, 0.f, 0.f};
  constexpr int NS = Fr::NS, KP = Fr::KL, E0 = NS - KP;
  constexpr Sch sc = sch_of(KP);
  static_assert(WIN >= KP, "window");
  auto ld = [&](int i) { return ld_fr<Fr>(rs, l16, a_own, a_oth, i); };
#pragma unroll
  for (int i = PRE; i < WIN; ++i) { af[i] = ld(i); SB(); }
  LAS unsigned char* r2 = L + opqv(RED_OFF + l16 + (w ^ 2) * 8192);
  LAS unsigned char* r3 = L + opqv(RED_OFF + l16 + (w ^ 3) * 8192 + 4096);
  LAS unsigned char* r0 = L + opqv(RED_OFF + l16 + w * 8192);
  LAS unsigned char* r1 = L + opqv(RED_OFF + l16 + (w ^ 1) * 8192);
  pre_loop();
  SB();
  f32x16 a0 = zero16, a1 = zero16, a2 = zero16, a3 = zero16;
  float u[16];
  static_for<E0>([&](auto I) {
    constexpr int i = I, ks = Fr::ks(i), e = i - (E0 - 4);
    const v4u bb = af[i % WIN];
    if constexpr (!Fr::own(i)) a2 = mfma32(wf[ks][0], bb, a2);
    else a1 = mfma32(wf[ks][1], bb, a1);
    SB();
    mid(I);
    static_for<NS - WIN>([&](auto Q) { if constexpr (Fr::lstep(Q + WIN, WIN) == i) { af[Q % WIN] = ld(Q + WIN); SB(); } });
    SB();
    if constexpr (!Fr::own(i)) a3 = mfma32(wf[ks][1], bb, a3);
    else a0 = mfma32(wf[ks][0], bb, a0);
    SB();
    if constexpr (e >= 0) {
      const u32 ra = res[e >> 1][2 * (e & 1)], rb = res[e >> 1][2 * (e & 1) + 1];
      u[4 * e] = bflo(ra); u[4 * e + 1] = bfhi(ra); u[4 * e + 2] = bflo(rb); u[4 * e + 3] = bfhi(rb);
#pragma unroll
      for (int m = 0; m < 4; ++m) PIN(u[4 * e + m]);
    }
    SB();
  });
  f32x4 p[8];
  auto acc4 = [&](int q, const f32x4& x) {
#pragma unroll
    for (int m = 0; m < 4; ++m) { const float xv = x[m]; u[4 * q + m] = __builtin_fmaf(xv, scale, u[4 * q + m]); PIN(u[4 * q + m]); }
  };
  static_for<2 * KP>([&](auto G) {
    constexpr int g = G, k = g % KP, ks = Fr::ks(E0 + k);
    if constexpr (g < KP) a1 = mfma32(wf[ks][1], af[(E0 + k) % WIN], a1);
    else a0 = mfma32(wf[ks][0], af[(E0 + k) % WIN], a0);
    SB();
    if constexpr (g == sc.bar1 || g == sc.bar2) BAR();
    static_for<8>([&](auto Q) { if constexpr (sc.rd[Q] == g) p[Q] = *(LAS f32x4*)(r0 + Q * 1024); });
    if constexpr (g == sc.rd1) static_for<4>([&](auto Q) { p[Q] = *(LAS f32x4*)(r1 + Q * 1024); });
    SB();
    static_for<4>([&](auto Q) { if constexpr (sc.put2[Q] == g) lds_put1(r2, a2, Q); });
    static_for<4>([&](auto Q) { if constexpr (sc.put3[Q] == g) lds_put1(r3, a3, Q); });
    static_for<4>([&](auto Q) { if constexpr (sc.put1[Q] == g) lds_put1(r0, a1, Q); });
    SB();
    static_for<8>([&](auto Q) { if constexpr (sc.fu[Q] == g) acc4(Q & 3, p[Q]); });
    static_for<4>([&](auto Q) { if constexpr (sc.f1[Q] == g) acc4(Q, p[Q]); });
    SB();
    mid(std::integral_constant<int, E0 + g>{});
    SB();
  });
#pragma unroll
  for (int q = 0; q < 4; ++q) {
    acc4(q, f32x4{a0[4 * q], a0[4 * q + 1], a0[4 * q + 2], a0[4 * q + 3]});
    res[q >> 1][2 * (q & 1)] = pk(u[4 * q], u[4 * q + 1]);
    res[q >> 1][2 * (q & 1) + 1] = pk(u[4 * q + 2], u[4 * q + 3]);
  }
}
DI void resid_store(rsrc_t rs, const v4u (&res)[2], u32 l16, u32 sto, u32 xoff, u32 soff) {
#pragma unroll
  for (int s = 0; s < 2; ++s) st128<SC1>(res[s], rs, l16 + s * 1024, xoff);
  SB();
  float s0 = 0.f, q0 = 0.f, s1 = 0.f, q1 = 0.f;
#pragma unroll
  for (int m = 0; m < 4; ++m) {
    s0 = dot2(res[0][m], ONES2, s0); q0 = dot2(res[0][m], res[0][m], q0);
    s1 = dot2(res[1][m], ONES2, s1); q1 = dot2(res[1][m], res[1][m], q1);
  }
  st64<SC1>(__builtin_bit_cast(v2u, f32x2{sum32(s0 + s1), sum32(q0 + q1)}), rs, sto, soff);
}

DI void stats_fin(const f32x4 (&sp)[4], float& nmean, float& rstd) {
  float t[4];
#pragma unroll
  for (int m = 0; m < 4; ++m) { t[m] = sp[0][m] + sp[1][m]; PIN(t[m]); }
#pragma unroll
  for (int i = 2; i < 4; ++i)
#pragma unroll
    for (int m = 0; m < 4; ++m) { t[m] += sp[i][m]; PIN(t[m]); }
  const float s = mix16(t[0], t[2]), q = mix16(t[1], t[3]);
  nmean = sum32(s) * (-1.f / 512.f);
  const float var = fmaxf(sum32(q) * (1.f / 512.f) - nmean * nmean, 0.f);
  rstd = __builtin_amdgcn_rsqf(var + 1e-5f);
  PIN(nmean);
  PIN(rstd);
}

template <class Hook> DI void dual_softmax(const f32x16& p, float c, u32 (&o)[16], Hook&& hook) {
  float mx = p[0], mn = p[0];
#pragma unroll
  for (int i = 1; i < 16; ++i) { mx = fmaxf(mx, p[i]); mn = fminf(mn, p[i]); }
  float ap = -mx * c, an = mn * c;
  PIN(ap);
  PIN(an);
  SB();
  hook(std::integral_constant<int, 0>{});
  SB();
  f32x2 ep[8], en[8];
#pragma unroll
  for (int i = 0; i < 8; ++i) {
    const f32x2 pv = {p[2 * i], p[2 * i + 1]};
    ep[i] = pv * c + ap;
    en[i] = pv * (-c) + an;
    PIN(ep[i]);
    PIN(en[i]);
  }
  SB();
  hook(std::integral_constant<int, 1>{});
  SB();
  f32x2 sa, sc;
  static_for<8>([&](auto I) {
    constexpr int i = I;
    ep[i] = f32x2{__builtin_amdgcn_exp2f(ep[i].x), __builtin_amdgcn_exp2f(ep[i].y)};
    en[i] = f32x2{__builtin_amdgcn_exp2f(en[i].x), __builtin_amdgcn_exp2f(en[i].y)};
    PIN(ep[i]);
    PIN(en[i]);
    if constexpr (i == 2) { sa = ep[0] + ep[1]; sc = en[0] + en[1]; PIN(sa); PIN(sc); }
    if constexpr (i > 2) { sa += ep[i - 1]; sc += en[i - 1]; PIN(sa); PIN(sc); }
    SB();
    hook(std::integral_constant<int, 2 + i>{});
    SB();
  });
  sa += ep[7];
  sc += en[7];
  const float rp = __builtin_amdgcn_rcpf(sa.x + sa.y), rn = __builtin_amdgcn_rcpf(sc.x + sc.y);
#pragma unroll
  for (int i = 0; i < 8; ++i) { ep[i] *= rp; PIN(ep[i]); }
#pragma unroll
  for (int i = 0; i < 8; ++i) { en[i] *= rn; PIN(en[i]); }
  SB();
#pragma unroll
  for (int i = 0; i < 8; ++i) { o[i] = pk(ep[i].x, ep[i].y); o[8 + i] = pk(en[i].x, en[i].y); }
}
constexpr int p3_g0(int v) { return (v * 58) / 48; }
constexpr bool p3_w2gap(int gi) { return (gi * W2B) / 8 != ((gi + 1) * W2B) / 8; }
constexpr int p3_pre(int layer, int g) {
  int n = layer == 0 && p3_w2gap(g);
  for (int q = 0; q < 6; ++q) n += p3_g0(8 * q + 7) + 5 == g;
  return n;
}
constexpr int p3_post(int g) { return (g == HS0) + (g == HS1) + (g == PK0); }
constexpr int p3_younger(int layer, int t, int gw) {
  const int gs = p3_g0(16 * t + 15) + 5;
  int n = p3_post(gs);
  for (int g = gs + 1; g < gw; ++g) n += p3_pre(layer, g) + p3_post(g);
  return n + (gw < 64 ? p3_pre(layer, gw) : 0);
}
static_assert(p3_g0(32) >= 35 && p3_g0(47) + 5 < 64 && p3_g0(15) + 5 < HS0 && p3_g0(31) + 5 < HS1 && HS0 != HS1, "P3 schedule");

__global__ void __launch_bounds__(256, 1)
k_main(const uint16_t* __restrict__ x, const uint16_t* __restrict__ wqkv, const uint16_t* __restrict__ wphi,
       const uint16_t* __restrict__ wo_, const uint16_t* __restrict__ w1, const uint16_t* __restrict__ w2,
       unsigned char* ws, unsigned char* out) {
  LAS unsigned char* L = (LAS unsigned char*)smem;
  const int b = blockIdx.x, tid = threadIdx.x, lane = tid & 63, w = __builtin_amdgcn_readfirstlane(tid >> 6);
  const int xcd = b & 7, j = b >> 3;
  const int rt = 4 * (xcd >> 1) + (j >> 3), hp = 8 * (xcd & 1) + (j & 7), tt = 4 * rt + w;
  const bool p3on = (j & 7) < 7;
  const int fb = p3on ? 7 * (xcd & 1) + (j & 7) : 0;
  const int rt2 = 4 * xcd + (j >> 3), cb = j & 7;
  const int tte = 2 * rt2 + (w >> 1), n0 = 64 * cb + 32 * (w & 1);
  const rsrc_t rs = mkrsrc(ws, WS_END);
  const f32x16 zero16 = {0.f, 0.f, 0.f, 0.f, 0.f, 0.f, 0.f, 0.f, 0.f, 0.f, 0.f, 0.f, 0.f, 0.f, 0.f, 0.f};
  STAMP(63);
  v4u res[2];
  float csq[3], cs1[4];
  v4u wph[2];
  u32 tag_lo, tag_hi, tws = 0;
  const u32 l0 = lane0_off();
  const u32 nlh = !p3on ? 0u : (fb == 3 || fb == 10) ? 2u : 1u;
  v4u xr[32];
  const uint16_t* xo = x;
  asm volatile("" : "+s"(xo));
  const rsrc_t rsx = mkrsrc(xo, S * D * 2);
  auto trfrag = [&](LAS unsigned char* p, int str) { const v2u t0 = trread(p), t1 = trread(p + str); return v4u{t0.x, t0.y, t1.x, t1.y}; };
  const u32 sv = ((lane >> 2) & 1) == ((lane >> 4) & 1) ? ONES2 : 0u;
  v4u selm = {sv, sv, sv, sv};
  if (b >= 248 && w < 2) {
#pragma unroll
    for (int i = 0; i < NCNT * CSTR / 16384; ++i) st128<SC1>(v4u{0u, 0u, 0u, 0u}, rs, lane * 16, CTRL + ((b - 248) * 2 + w) * (NCNT * CSTR / 16) + i * 1024);
  }
  const v2u ep = ld64<SC1>(rs, 0, EPOCH_OFF);
  SB();
  int kind = 4, nb = 0, kq = 0, ld = 512;
  const uint16_t* W = x;
  if (w == 0) {
    if (b < 192) { kind = 0; W = wqkv; ld = 1536; nb = b >> 3; kq = b & 7; }
  } else if (w >= 2) {
    const int hw = w & 1;
    if (b < 112) { kind = 1; W = w1; ld = 1792; nb = b >> 2; kq = (b & 3) * 2 + hw; }
    else if (b < 144) { kind = 2; W = wo_; nb = (b - 112) >> 2; kq = (b & 3) * 2 + hw; }
    else { kind = 3; W = w2; nb = (b - 144) / 14; kq = ((b - 144) - 14 * nb) * 2 + hw; }
  }
  LAS unsigned char* img = L + SCR + w * 8192;
  {
    const rsrc_t rsu = mkrsrc(W, kind < 4 ? (kind == 3 ? 1792 : 512) * ld * 2 : 0);
    const u32 vu = (lane >> 3) * ld * 2 + (((lane & 7) ^ ((lane >> 2) & 4)) * 16);
#pragma unroll
    for (int i = 0; i < 8; ++i) __builtin_amdgcn_raw_ptr_buffer_load_lds(rsu, (LAS u32*)(img + i * 1024), 16, vu, (64 * kq + 8 * i) * ld * 2 + nb * 128, 0, 0);
  }
  SB();
  const u32 vx = (lane >> 3) * 1024 + (lane & 7) * 16;
  const u32 rot = ROT ? j & 7 : 0;
  auto xld = [&](auto I) { constexpr int i = I; xr[i] = ld128<0>(rsx, vx, tt * 32768 + (i & 3) * 8192 + (((i >> 2) ^ rot) * 128)); SB(); };
  static_for<XA>([&](auto I) { xld(I); });
  u32 obase, cstr, kstr, kstr2, csb, ovo, cvo, ua;
  {
    const int t32 = 2 * nb, sec = t32 >> 4, tile = sec == 0 ? 2 : sec - 1;
    obase = opq(kind == 0 ? WQKV_T + (t32 & 15) * 98304 + tile * 32768 + kq * 4096 : kind == 1 ? W1_T + t32 * 32768 + kq * 4096
                : kind == 2 ? WO_T + (nb * 32 + kq * 4) * 2048 : W2_T + (nb * 112 + (kq & 1) * 56 + (kq >> 1) * 2) * 2048);
    cstr = opq(kind == 0 ? 98304 : kind == 1 ? 32768 : 1024);
    kstr = opq(kind < 2 ? 1024 : 2048);
    kstr2 = opq(kind == 3 ? 57344 : 2 * kstr);
    csb = opq(kind == 0 ? CS_QKV + (((t32 & 15) * 3 + tile) * 8 + kq) * 128 : CS_W1 + (t32 * 8 + kq) * 128);
    ovo = opqv(kind < 4 ? lane * 16 : 0x80000000u);
    cvo = opqv(kind < 2 ? (lane >> 5) * (kind == 0 ? 3072 : 1024) + (lane & 31) * 4 : 0x80000000u);
    const u32 c16 = lane & 15, Q = ((lane >> 2) & 4) + (lane & 3);
    const u32 Cc = kind == 0 ? 4 * (Q & 1) + (Q >> 1) : (Q & 4) + 2 * (Q & 1) + ((Q >> 1) & 1);
    ua = opqv((8 * (lane >> 5) + (c16 >> 2)) * 128 + (c16 >> 3) * 64 + Cc * 8);
  }
  const u32 jadd = opq(cnt_off(C_J + (b < 112 ? 16 + (b >> 3) : b < 144 ? (b - 112) >> 2 : (b - 144) / 14)));
  auto unit = [&]() {
    v4u o[8];
#pragma unroll
    for (int i = 0; i < 8; ++i) o[i] = trfrag(img + (ua ^ ((i >> 2) * 64)) + (i & 3) * 2048, 512);
    SB();
    f32x4 cs[2] = {{0.f, 0.f, 0.f, 0.f}, {0.f, 0.f, 0.f, 0.f}};
#pragma unroll
    for (int i = 0; i < 8; ++i) {
      st128<SC1>(o[i], rs, ovo, obase + (i >> 2) * cstr + ((i >> 1) & 1) * kstr2 + (i & 1) * kstr);
      cs[i >> 2] = mfma16(selm, o[i], cs[i >> 2]);
    }
    const float c0 = cs[0][0], c1 = cs[1][0];
    __builtin_amdgcn_raw_buffer_store_b32(__builtin_bit_cast(u32, lane < 32 ? c0 : c1), rs, cvo, csb, SC1);
    SB();
  };
  SB();
  const u32 tvo = opqv((((b >= 248 && w < 2) || kind == 0) && lane < 16) ? lane * 8 : 0x80000000u);
  const u32 tso = opq(b >= 248 ? INIT_OFF + (((b & 7) * 2 + w) & 15) * CSTR : FLAG_OFF + b * CSTR);
  SB();
  v4u w2f[28][2];

  static_for<2>([&](auto LAYER) __attribute__((always_inline)) {
    constexpr int layer = LAYER;
    constexpr int sb = layer * 29;
    v4u wo[8][2];
    const u32 wb = opq(W2_T + cb * 229376 + w * 14336);
    const u32 wsw = (w & 1) * 1024;
    u32 pvx[3];
#define IC(n) std::integral_constant<int, n>{}
    {
      const u32 ln = opqv(lane), l16 = ln * 16, r = ln & 31, hf = ln >> 5;
      if constexpr (layer) STAMP(sb + 0);
      constexpr int DX = DXV;
      v4u xf[32];
      const u32 xb = XT + (2 * layer) * XT_SZ + tt * 32768;
      if constexpr (layer > 0 && XMODE == 1) apeek(rs, l0, cnt_off(C_X + tt), pvx[0]);
      SB();
      f32x4 sp[4];
      const u32 stb = ST + (2 * layer) * ST_SZ + tt * 4096;
      const u32 wqb = opq(WQKV_T + hp * 98304 + 65536 + w * 8192);
      const u32 g4 = ln >> 4, c16 = ln & 15;
      LAS unsigned char* a_v = L + opqv(VV_OFF + (32 * w + r) * VV_STR + hf * 32);
      LAS unsigned char* a_pk = L + opqv(PK_OFF + (32 * w + r) * PK_STR + hf * 64);
      LAS unsigned char* a_pa = L + opqv(PK_OFF + (8 * g4 + (c16 >> 2)) * PK_STR + w * 32 + (ln & 3) * 8);
      LAS unsigned char* a_pb = L + opqv(VV_OFF + (8 * g4 + (c16 >> 2)) * VV_STR + (w >> 1) * 32 + (ln & 3) * 8);
      const u32 o_kv = opqv((g4 >> 1) * 256 + c16 * 16 + (g4 & 1) * 8), o_z = opqv(hf ? 0x80000000u : r * 4);
      u32 jv = 0;
      constexpr int wok = layer ? WOK : 0, NPF = 16 - wok + (layer ? 24 : 32);
      constexpr int s_q1 = 0, s_q2 = s_q1 + NQ1, s_od = s_q2 + NQ2, s_op = s_od + ODR, n_op = layer ? OPL1 : OPL0, s_p2 = s_op + n_op, s_xd = NPF - XDR;
      static_assert(s_p2 <= s_xd && s_xd - s_p2 <= 12 && s_od <= NPF - 8 && s_op >= 16 - wok, "prefetch schedule");
      LAS v4u* WL = (LAS v4u*)(L + l16);
      LAS v4u* WQ = (LAS v4u*)(L + opqv(l16 + 65536));
      const u32 wob = opq(WO_T + (cb * 4 + w) * 16384);
      auto wold = [&](auto I) {
        constexpr int i = I;
        wo[i >> 1][i & 1] = ld128<SC1>(rs, l16 + ((i >> 1) & 1) * 2048, wob + (i >> 2) * 4096 + ((i & 1) ? 1024 - wsw : wsw));
      };
      f32x16 aK = zero16, aV = zero16, aQ;
      float rstd, nmean, c;
      const u32 jvo = opqv(ln == 0 ? cnt_off(C_J + cb) : ln == 1 ? cnt_off(C_J + 16 + fb) : 0x80000000u);
      float cp[3][4];
      v4u wk[3], wv[3];
      SB();
      if constexpr (layer == 0) {
        LAS unsigned char* sw[2];
        LAS unsigned char* sr[4];
#pragma unroll
        for (int i = 0; i < 2; ++i) sw[i] = L + opqv(XS_OFF + w * 8192 + (ln >> 3) * 128 + (((ln & 7) ^ (4 * i + (ln >> 4))) * 16));
#pragma unroll
        for (int f = 0; f < 4; ++f) sr[f] = L + opqv(XS_OFF + w * 8192 + r * 128 + (((2 * f + hf) ^ ((r >> 1) & 7)) * 16));
        const u32 fu = ln >> 1, fvo = opqv(ln >= 12 ? 0x80000000u : FLAG_OFF + ((fu < 4 ? (1 + (w >> 1)) * 64 + (w & 1) * 4 + fu : 2 * w + fu - 4) + (hp >> 1) * 8) * CSTR + (ln & 1) * 4);
        PIN(selm);
        SB();
        if (w == 0) {
          asm volatile("s_waitcnt vmcnt(%0)" ::"n"(XA) : "memory");
          STAMP(58);
          unit();
          STAMP(59);
        }
        if (XBAR) asm volatile("s_barrier" ::: "memory");
        {
          const unsigned long long e = ((unsigned long long)__builtin_amdgcn_readfirstlane(ep.y) << 32) | __builtin_amdgcn_readfirstlane(ep.x);
          unsigned long long t = e * 0x9E3779B97F4A7C15ull + 0x632BE59BD9B4E019ull;
          if (t == e) t = ~e;
          tag_lo = (u32)t;
          tag_hi = (u32)(t >> 32);
        }
        const u32 fneed = (ln & 1) ? tag_hi : tag_lo;
        f32x4 sa = {0.f, 0.f, 0.f, 0.f};
        f32x16 gr = zero16;
        u32 fv;
        v2u tg;
        SB();
        auto vm = [&](auto N) {
          constexpr int n = N, xi = XA + n, di = n - 8 * FCK;
          if constexpr (xi < 32) xld(IC(xi));
          if constexpr (xi == 32) __builtin_amdgcn_raw_ptr_buffer_load_lds(mkrsrc(wphi, 16384), (LAS u32*)(L + XS_OFF + w * 8192 + 4096), 16, l16, hp * 1024, 0, 0);
          if constexpr (xi == 33) tws = ld32<0>(rs, (ln + 64 * w) * TLBW, 0);
          if constexpr (xi == XB) {
            if (w == 0) asm volatile("s_waitcnt vmcnt(%0)" ::"n"(XB - XA + 1) : "memory");
            st64<SC1>(v2u{tag_lo, tag_hi}, rs, tvo, tso);
            STAMP(60);
          }
          if constexpr (di >= 0 && di < 16)
            dma1<(di & 3)>(rs, l16, WQKV_T + hp * 98304 + w * 16384 + (di >> 2) * 4096, L + (w >> 1) * 32768 + ((((w & 1) * 4 + (di >> 2)) ^ rot) * 4096));
          if constexpr (di == 16) tg = ld64<SC1>(rs, c16 * CSTR, INIT_OFF);
          SB();
        };
        static_assert(XA + 8 * FCK > 33 && XB < 32 && FCK <= 7 && UB >= FCK + 3 && XA >= 4, "start schedule");
        static_for<11>([&](auto B) {
          constexpr int blk = B;
          if constexpr (blk == UB) { if (w >= 2) unit(); }
          if constexpr (blk == FPK) { fv = peek(rs, fvo, 0); SB(); }
          if constexpr (blk == FCK) {
            STAMP(0);
            if ((__builtin_amdgcn_ballot_w64(fv == fneed) & 0xfffull) != 0xfffull) {
#pragma unroll 1
              for (u32 spins = 0; spins < (1u << 16); ++spins)
                if ((__builtin_amdgcn_ballot_w64(peek(rs, fvo, 0) == fneed) & 0xfffull) == 0xfffull) break;
            }
            asm volatile("" ::: "memory");
            STAMP(61);
          }
          if constexpr (blk < 10) static_for<4>([&](auto I) {
            constexpr int i = I;
            if constexpr (blk > 1) gr = mfma32(xf[4 * blk - 8 + i], xf[4 * blk - 8 + i], gr);
            SB();
            vm(IC(8 * blk + 2 * i));
            if constexpr (blk < 8) *(LAS v4u*)(sw[i & 1] + i * 1024) = xr[4 * blk + i];
            SB();
            if constexpr (blk > 1) { sa = mfma16(selm, xf[4 * blk - 8 + i], sa); asm volatile("" : "+a"(sa)); }
            SB();
            vm(IC(8 * blk + 2 * i + 1));
          });
          if constexpr (blk < 8) static_for<4>([&](auto F) { xf[4 * blk + F] = *(LAS v4u*)(sr[F]); SB(); });
        });
        asm volatile("" ::"v"(tws));
        {
          const u32 Qc = 4 * (g4 & 1) + (c16 & 3);
          LAS unsigned char* a_ph = L + opqv(XS_OFF + w * 8192 + 4096 + hf * 512 + (c16 >> 2) * 32 + (Qc >> 1) * 8);
#pragma unroll
          for (int s = 0; s < 2; ++s) wph[s] = trfrag(a_ph + s * 256, 128);
        }
        const float sa0 = sa[0];
        nmean = sa0 * (-1.f / 512.f);
        auto sel = [](bool cnd, float a0, float a1) { PIN(a0); PIN(a1); return cnd ? a1 : a0; };
        auto g4f = [&](int i) { return sel(r & 2, sel(r & 1, gr[4 * i], gr[4 * i + 1]), sel(r & 1, gr[4 * i + 2], gr[4 * i + 3])); };
        const float gd = sel(r & 16, sel(r & 8, g4f(0), g4f(1)), sel(r & 8, g4f(2), g4f(3)));
        const bool dl = ((r >> 2) & 1) == hf;
        const float dg = dl ? gd : 0.f;
        rstd = __builtin_amdgcn_rsqf(fmaxf(sum32(dg) * (1.f / 512.f) - nmean * nmean, 0.f) + 1e-5f);
        c = rstd * LOG2E;
#pragma unroll
        for (int s = 0; s < 2; ++s)
#pragma unroll
          for (int m = 0; m < 4; ++m) wph[s][m] = dl ? wph[s][m] : 0u;
        PIN(wph[0]);
        PIN(wph[1]);
        PIN(c);
        PIN(rstd);
        PIN(nmean);
        SB();
        if constexpr (UB > FCK) { if (w >= 2) asm volatile("s_waitcnt vmcnt(9)" ::: "memory"); else DRAIN(); }
        else DRAIN();
        if (w == 0 && !__all(tg.x == tag_lo && tg.y == tag_hi)) {
#pragma unroll 1
          for (u32 spins = 0; spins < (1u << 16); ++spins) {
            asm volatile("" ::: "memory");
            const v2u t = ld64<SC1>(rs, c16 * CSTR, INIT_OFF);
            if (__all(t.x == tag_lo && t.y == tag_hi)) break;
          }
        }
        BAR();
#pragma unroll
        for (int i = 0; i < 2; ++i) { wk[i] = WL[i * 64]; wv[i] = WL[(32 + i) * 64]; }
        if constexpr (UB <= FCK) add1(rs, w >= 2 ? l0 : 0x80000000u, jadd);
        SB();
      } else {
        if constexpr (XMODE == 0) { NAP(PS3); pvx[0] = peek(rs, l0, cnt_off(C_X + tt)); SB(); }
        if constexpr (XMODE == 1) { NAP(PS1); apeek(rs, l0, cnt_off(C_X + tt), pvx[1]); NAP(PS2); apeek(rs, l0, cnt_off(C_X + tt), pvx[2]); SB(); }
        asm volatile("s_waitcnt vmcnt(%0)" ::"n"(XMODE == 1 ? 4 : XMODE == 0 ? 2 : 1) : "memory");
        BAR();
#pragma unroll
        for (int i = 0; i < 2; ++i) { wk[i] = WL[i * 64]; wv[i] = WL[(32 + i) * 64]; }
        SB();
        if constexpr (XMODE == 1) pfinish<2, 1, 0>(rs, l0, cnt_off(C_X + tt), 16, pvx);
        else if constexpr (XMODE == 0) wfinish(rs, l0, cnt_off(C_X + tt), 16, pvx[0]);
        else { NAP(PS3); wwait(rs, l0, cnt_off(C_X + tt), 16); }
        STAMP(sb + 1);
#pragma unroll
        for (int ks = 0; ks < DX; ++ks) { xf[ks] = ldf<SC1>(rs, l16, xb, ks * 1024); SB(); }
      }
      STAMP(sb + 26);
      static_for<32>([&](auto KSI) {
        constexpr int ks = KSI, q0 = layer ? 20 : 8;
        aK = mfma32(wk[ks % 3], xf[ks], aK);
        SB();
        if constexpr (ks + 2 < 32) wk[(ks + 2) % 3] = WL[(ks + 2) * 64];
        if constexpr (layer > 0 && ks + DX < 32) xf[ks + DX] = ldf<SC1>(rs, l16, xb, (ks + DX) * 1024);
        if constexpr (ks >= q0 && ks < q0 + 8) dma1<((ks - q0) & 3)>(rs, l16, wqb + ((ks - q0) >> 2) * 4096, L + 65536 + (((2 * w + ((ks - q0) >> 2)) ^ (layer ? 0 : rot)) * 4096));
        if constexpr (ks >= 28 && ks < 28 + wok) wold(IC(ks - 28));
        if constexpr (layer == 0 && ks < 6) cp[ks >> 1][2 * (ks & 1)] = __builtin_bit_cast(float, ld32<SC1>(rs, ln * 4 + (ks & 1) * 512 + (ks >> 1) * 1024, CS_QKV + hp * 3072));
        if constexpr (ks == 23) { SB(); aV = mfma32f_pad<5>(csq[1], nmean, aV); }
        SB();
        aV = mfma32(wv[ks % 3], xf[ks], aV);
        SB();
        if constexpr (ks + 2 < 32) wv[(ks + 2) % 3] = WL[(32 + ks + 2) * 64];
        if constexpr (layer == 0 && ks < 6) cp[ks >> 1][2 * (ks & 1) + 1] = __builtin_bit_cast(float, ld32<SC1>(rs, ln * 4 + (ks & 1) * 512 + 256 + (ks >> 1) * 1024, CS_QKV + hp * 3072));
        if constexpr (layer == 0 && (ks == 18 || ks == 22)) res[(ks - 18) >> 2] = ld128<0>(rsx, r * 1024 + hf * 16 + ((ks - 18) >> 2) * 32, tte * 32768 + n0 * 2);
        if constexpr (layer == 0 && ks == 19) {
#pragma unroll
          for (int t = 0; t < 3; ++t) { csq[t] = (cp[t][0] + cp[t][1]) + (cp[t][2] + cp[t][3]); PIN(csq[t]); }
        }
        if constexpr (layer == 0 && ks == 19 && UB > FCK) add1(rs, w >= 2 ? l0 : 0x80000000u, jadd);
        if constexpr (layer == 0 && ks == 31 && JPK < 0) jv = peek(rs, w != 0 ? 0x80000000u : jvo, 0);
        if constexpr (layer > 0) {
          if constexpr (ks < 4) sp[ks] = __builtin_bit_cast(f32x4, ld128<SC1>(rs, l16 + ks * 1024, stb));
          if constexpr (ks == 1 && XMODE == 1) pretire(pvx);
          if constexpr (ks == 17) stats_fin(sp, nmean, rstd);
          if constexpr (ks == 18) { c = rstd * LOG2E; PIN(c); }
        }
        if constexpr (ks == 20) { SB(); aK = mfma32f_pad<5>(csq[0], nmean, aK); }
        SB();
      });
      STAMP(sb + 2);
      const u32 w1b = opq(W1_T + fb * 131072);
      auto w1dma = [&](auto I) {
        constexpr int i = I;
        if constexpr (i < 16) dma1<i>(rs, l16, w1b + w * 16384, L + w * 16384);
        else if constexpr (i < 24) dma1<i - 16>(rs, l16, w1b + 98304 + w * 8192, L + 98304 + w * 8192);
        else dma1<i - 24>(rs, l16, w1b + 65536 + w * 8192, L + 65536 + w * 8192);
      };
      auto pf = [&](auto N) {
        constexpr int n = N + wok;
        if constexpr (n < 16) wold(IC(n));
        else w1dma(IC(n - 16 + (layer && n >= 32 ? 8 : 0)));
        SB();
      };
      {
        const v4u k0 = pack8(aK, 0), k1 = pack8(aK, 1);
        f32x16 p = mfma32(wph[0], k0, zero16);
        p = mfma32(wph[1], k1, p);
        SB();
#pragma unroll
        for (int i = 0; i < 16; ++i) { aV[i] *= rstd; PIN(aV[i]); }
        SB();
        *(LAS v4u*)(a_v) = pack8(aV, 0);
        *(LAS v4u*)(a_v + 16) = pack8(aV, 1);
        SB();
        u32 o[16];
        dual_softmax(p, c, o, [&](auto I) { if constexpr (layer == 0 && I == JPK) jv = peek(rs, w != 0 ? 0x80000000u : jvo, 0); });
#pragma unroll
        for (int i = 0; i < 4; ++i) *(LAS v4u*)(a_pk + i * 16) = v4u{o[4 * i], o[4 * i + 1], o[4 * i + 2], o[4 * i + 3]};
      }
      SB();
      aQ = mfma32f(csq[2], nmean, zero16);
      STAMP(sb + 3);
      asm volatile("s_waitcnt vmcnt(%0)" ::"n"(wok) : "memory");
      if constexpr (layer == 0) {
        const u32 need = ln == 0 ? 36u : ln == 1 ? 16u : 0u;
        if (w == 0 && (__builtin_amdgcn_ballot_w64(jv >= need) & 7ull) != 7ull) poll3(rs, jvo, need);
      }
      BAR();
      const u32 pbase = KVP + layer * KVP_SZ + (4 * hp + w) * KVP_HS;
      v4u wq[4];
      auto qfrag = [&](int ks) { return WQ[ks * 64]; };
      {
        f32x4 kv = {0.f, 0.f, 0.f, 0.f}, zz = {0.f, 0.f, 0.f, 0.f};
        const v4u ones = {ONES2, ONES2, ONES2, ONES2};
        v2u a0[4], a1[4], b0[4], b1[4];
#pragma unroll
        for (int ks = 0; ks < 4; ++ks) {
          a0[ks] = trread(a_pa + (32 * ks) * PK_STR); a1[ks] = trread(a_pa + (32 * ks + 4) * PK_STR);
          b0[ks] = trread(a_pb + (32 * ks) * VV_STR); b1[ks] = trread(a_pb + (32 * ks + 4) * VV_STR);
          SB();
        }
#pragma unroll
        for (int ks = 0; ks < 4; ++ks) {
          const v4u A = {a0[ks].x, a0[ks].y, a1[ks].x, a1[ks].y}, B = {b0[ks].x, b0[ks].y, b1[ks].x, b1[ks].y};
          kv = mfma16(A, B, kv);
          zz = mfma16(ones, A, zz);
          asm volatile("" : "+a"(zz));
          SB();
        }
#pragma unroll
        for (int ks = 0; ks < 4; ++ks) wq[ks] = qfrag(ks);
        SB();
        st64<SC1>(v2u{pk(kv[0], kv[1]), pk(kv[2], kv[3])}, rs, o_kv, pbase + rt * 512);
        const float z0 = zz[0];
        __builtin_amdgcn_raw_buffer_store_b32(__builtin_bit_cast(u32, z0), rs, o_z, pbase + 8192 + rt * 128, SC1);
      }
      SB();
      STAMP(sb + 4);
      auto qstep = [&](auto I) {
        constexpr int i = I;
        aQ = mfma32(wq[i & 3], xf[i], aQ);
        SB();
        if constexpr (i + 4 < 32) wq[i & 3] = qfrag(i + 4);
      };
      LAS unsigned char *a_kz, *a_kw, *a_kr;
      u32 o_st, o_zl;
      v4u sel;
      static_for<18>([&](auto I) {
        qstep(I);
        if constexpr ((I * NQ1 + 17) / 18 != ((I + 1) * NQ1 + 17) / 18) pf(IC(s_q1 + (I * NQ1 + 17) / 18));
        const u32 l2 = opqv(l16) >> 4, g2 = l2 >> 4, c2 = l2 & 15;
        if constexpr (I == 1) o_zl = opqv(g2 * 128 + c2 * 4);
        if constexpr (I == 3) a_kz = L + opqv(KF_OFF + w * 544 + (c2 >> 3) * 272 + 256 + (c2 & 7) * 2);
        if constexpr (I == 5) a_kw = L + opqv(KF_OFF + w * 544 + (g2 >> 1) * 272 + (g2 & 1) * 8 + rho32(c2 & 7, c2 >> 3) * 16);
        if constexpr (I == 7) a_kr = L + opqv(KF_OFF + (l2 >> 5) * 272 + ((l2 & 31) < 16 ? (l2 & 31) : 16) * 16);
        if constexpr (I == 9) o_st = opqv(l2 >= 32 ? 0x80000000u : c2 * 16 + g2 * 8);
        if constexpr (I == 11 || I == 13 || I == 15 || I == 17) {
          constexpr int m = (I - 11) / 2;
          sel[m] = opqv((c2 >> 1) == 4 * (g2 & 1) + m ? 0x3f80u << (16 * (c2 & 1)) : 0u);
        }
        SB();
      });
      const u32 ckv = cnt_off(C_KV + layer * 64 + hp * 4 + w);
      wsignal<NQ1>(rs, l0, ckv);
      STAMP(sb + 5);
      u32 pvk[3];
      static_for<14>([&](auto I) {
        qstep(IC(18 + I));
        if constexpr (I == KVPK1 && KMODE == 1) apeek(rs, l0, ckv, pvk[0]);
        if constexpr (I == KVPK1 && KMODE == 0) pvk[0] = peek(rs, l0, ckv);
        if constexpr ((I * NQ2 + 13) / 14 != ((I + 1) * NQ2 + 13) / 14) pf(IC(s_q2 + (I * NQ2 + 13) / 14));
        SB();
      });
      if constexpr (KMODE == 1) apeek(rs, l0, ckv, pvk[1]);
      SB();
      const v4u q0 = pack8(aQ, 0), q1 = pack8(aQ, 1);
      f32x16 pq = mfma32(wph[0], q0, zero16);
      pq = mfma32(wph[1], q1, pq);
      SB();
      v4u phq[4];
      v4u v[8];
      f32x4 z;
      {
        u32 o[16];
        dual_softmax(pq, c, o, [&](auto I) {
          constexpr int i = I;
          if constexpr (i == 0 && KMODE == 1) apeek(rs, l0, ckv, pvk[2]);
          if constexpr (i == KVCHK) {
            STAMP(sb + 6);
            if constexpr (KMODE == 1) pfinish<2 + NQ2 - (KVPK1 * NQ2 + 13) / 14, 1, 0>(rs, l0, ckv, 16, pvk);
            else wfinish(rs, l0, ckv, 16, pvk[0]);
            STAMP(sb + 7);
#pragma unroll
            for (int k = 0; k < 4; ++k) z[k] = __builtin_bit_cast(float, ld32<SC1>(rs, o_zl + k * 512, pbase + 8192));
            SB();
          }
          static_for<8>([&](auto K) {
            constexpr int k = K, at = KVCHK + (k + 1) / KVLD;
            if constexpr ((at < 9 ? at : 9) == i) { v[k] = ldf<SC1>(rs, l16, pbase, k * 1024); SB(); }
          });
        });
#pragma unroll
        for (int m = 0; m < 4; ++m) {
          auto sw = __builtin_amdgcn_permlane32_swap(o[m], o[4 + m], false, false);
          phq[0][m] = sw[0];
          phq[2][m] = sw[1];
          auto sx = __builtin_amdgcn_permlane32_swap(o[8 + m], o[12 + m], false, false);
          phq[1][m] = sx[0];
          phq[3][m] = sx[1];
        }
#pragma unroll
        for (int m = 0; m < 4; ++m) PIN(phq[m]);
      }
      SB();
      {
        const float zs = sum32(sum16((z[0] + z[1]) + (z[2] + z[3])));
        *(LAS unsigned short*)(a_kz) = (unsigned short)pk(zs, zs);
        if constexpr (KMODE == 1) pretire(pvk);
        SB();
        f32x4 sv = {0.f, 0.f, 0.f, 0.f};
#pragma unroll
        for (int i = 0; i < 8; ++i) { sv = mfma16(sel, v[i], sv); SB(); }
        *(LAS v2u*)(a_kw) = v2u{pk(sv[0], sv[1]), pk(sv[2], sv[3])};
      }
      BAR();
      STAMP(sb + 8);
      {
        v4u kvf[4];
#pragma unroll
        for (int i = 0; i < 4; ++i) kvf[i] = *(LAS v4u*)(a_kr + i * 544);
        SB();
        f32x16 nd[2];
        nd[0] = mfma32(kvf[0], phq[0], zero16);
        nd[0] = mfma32(kvf[1], phq[1], nd[0]);
        nd[1] = mfma32(kvf[2], phq[2], zero16);
        nd[1] = mfma32(kvf[3], phq[3], nd[1]);
        SB();
#pragma unroll
        for (int h = 0; h < 2; ++h) {
          const float inv = __builtin_amdgcn_rcpf(nd[h][8] + 1e-6f);
          v4u ob;
#pragma unroll
          for (int m = 0; m < 4; ++m) ob[m] = pk(nd[h][2 * m] * inv, nd[h][2 * m + 1] * inv);
          st128<SC1>(ob, rs, l16 + h * 1024, OB + layer * XT_SZ + tt * 32768 + (2 * hp) * 1024);
          SB();
        }
      }
      STAMP(sb + 9);
      static_for<ODR>([&](auto I) { pf(IC(s_od + I)); });
      wsignal<ODR>(rs, l0, cnt_off(C_O + layer * 128 + (tt >> 1) * 4 + (hp >> 2)));
      STAMP(sb + 10);
      const u32 co = cnt_off(C_O + layer * 128 + rt2 * 4 + w);
      u32 pvo[3];
      BAR();
      if constexpr (OMODE == 1) {
        apeek(rs, l0, co, pvo[0]);
        NAP(PS2);
        apeek(rs, l0, co, pvo[1]);
        NAP(PS2);
        apeek(rs, l0, co, pvo[2]);
        static_for<n_op>([&](auto I) { pf(IC(s_op + I)); });
        pfinish<2 + n_op, 1 + n_op, n_op>(rs, l0, co, 8, pvo);
      } else {
        NAP(PS4);
        pvo[0] = peek(rs, l0, co);
        SB();
        static_for<n_op>([&](auto I) { pf(IC(s_op + I)); });
        wfinish(rs, l0, co, 8, pvo[0]);
      }
      STAMP(sb + 11);
      const u32 ab = OB + layer * XT_SZ + (8 * w) * 1024;
      v4u af[DXV];
      splitk_phase<FrO, DXV, 0>(rs, wo, af, ab + tte * 32768, ab + (tte ^ 1) * 32768, L, w, l16, 1.f, res, [&]() {}, [&](auto I) {
        if constexpr (I == 1 && OMODE == 1) pretire(pvo);
        if constexpr (I >= 4 && s_p2 + I - 4 < s_xd) pf(IC(s_p2 + I - 4));
      });
      STAMP(sb + 12);
      resid_store(rs, res, l16, o_st, XT + (2 * layer + 1) * XT_SZ + tte * 32768 + (n0 >> 3) * 512, ST + (2 * layer + 1) * ST_SZ + tte * 4096 + (n0 >> 5) * 256);
      STAMP(sb + 13);
      static_for<XDR>([&](auto I) { pf(IC(s_xd + I)); });
      wsignal<XDR>(rs, l0, cnt_off(C_X1 + layer * 64 + tte));
      STAMP(sb + 14);
    }
    u32 pv1;
    const u32 lh = opqv(opqv(lane) < nlh ? lane * CSTR : 0x80000000u);
    v4u af[WINH];
    const u32 ch0 = cnt_off(C_H + layer * 512 + rt2 * 4 + w);
    const u32 sh0 = cnt_off(C_H + layer * 512 + (tt >> 1) * 4 + (2 * fb) / 7);
    const u32 hown = HB + layer * HB_SZ + (7 * w) * 1024 + tte * 114688, hoth = HB + layer * HB_SZ + (7 * w) * 1024 + (tte ^ 1) * 114688;
    {
      const u32 ln = opqv(lane), l16 = ln * 16, r = ln & 31, hf = ln >> 5;
      auto w2ld = [&](auto I) {
        constexpr int i = I, k = (i >> 1) % 7;
        w2f[i >> 1][i & 1] = ld128<SC1>(rs, l16 + (k & 1) * 2048, wb + (i / 14) * 57344 + (k >> 1) * 4096 + ((i & 1) ? 1024 - wsw : wsw));
      };
      auto hld = [&](int i) { if (i < HPRE) { af[i] = ld_fr<FrH>(rs, l16, hown, hoth, i); SB(); } };
      if (p3on) {
        static_for<3>([&](auto J) {
          if constexpr (XMODE == 1) apeek(rs, l0, cnt_off(C_X1 + layer * 64 + tt), pvx[J]);
          if constexpr (XMODE == 0 && J == (layer ? 0 : 1)) { if constexpr (layer) NAP(PS3 + 1); pvx[0] = peek(rs, l0, cnt_off(C_X1 + layer * 64 + tt)); }
          SB();
          if constexpr (layer == 0) static_for<W2D / 3>([&](auto I) { w2ld(IC(J * (W2D / 3) + I)); SB(); });
          else if constexpr (J < 2 && XMODE == 1) NAP(PS2);
        });
        constexpr int DX = DXV;
        v4u xf[32];
        const u32 xb = XT + (2 * layer + 1) * XT_SZ + tt * 32768;
        f32x4 sp[4];
        const u32 stb = ST + (2 * layer + 1) * ST_SZ + tt * 4096;
        LAS v4u* WL = (LAS v4u*)(L + l16);
        LAS v4u* WH = (LAS v4u*)(L + opqv(l16 + 65536));
        SB();
        asm volatile("s_waitcnt vmcnt(%0)" ::"n"((layer == 0 ? W2D : 0) + (XMODE == 1 ? 4 : XMODE == 0 ? 2 : 1)) : "memory");
        BAR();
        v4u wa[3], wc[3], wq[4];
#pragma unroll
        for (int i = 0; i < 2; ++i) { wa[i] = WL[i * 64]; wc[i] = WL[(32 + i) * 64]; }
        SB();
        STAMP(sb + 15);
        if constexpr (XMODE == 0) wfinish(rs, l0, cnt_off(C_X1 + layer * 64 + tt), 16, pvx[0]);
        else if constexpr (XMODE == 2) { if constexpr (layer) NAP(PS3); wwait(rs, l0, cnt_off(C_X1 + layer * 64 + tt), 16); }
        else pfinish<2 + (layer ? 0 : W2D), 1 + (layer ? 0 : 2 * W2D / 3), (layer ? 0 : W2D / 3)>(rs, l0, cnt_off(C_X1 + layer * 64 + tt), 16, pvx);
        STAMP(sb + 16);
#pragma unroll
        for (int ks = 0; ks < DX; ++ks) { xf[ks] = ldf<SC1>(rs, l16, xb, ks * 1024); SB(); }
        f32x16 acc[4];
        acc[0] = zero16;
        acc[1] = zero16;
        float nmean, nc, cp[4][4];
        SB();
        static_for<32>([&](auto KSI) {
          constexpr int ks = KSI;
          acc[0] = mfma32(wa[ks % 3], xf[ks], acc[0]);
          SB();
          if constexpr (ks + 2 < 32) wa[(ks + 2) % 3] = WL[(ks + 2) * 64];
          else wq[2 * (ks - 30)] = WH[(2 * (ks - 30)) * 64];
          if constexpr (ks + DX < 32) xf[ks + DX] = ldf<SC1>(rs, l16, xb, (ks + DX) * 1024);
          if constexpr (layer == 0 && ks >= 20) w2ld(IC(W2D + ks - 20));
          if constexpr (ks == 24) { SB(); acc[1] = mfma32f_pad<5>(cs1[1], nmean, acc[1]); }
          SB();
          acc[1] = mfma32(wc[ks % 3], xf[ks], acc[1]);
          SB();
          if constexpr (ks + 2 < 32) wc[(ks + 2) % 3] = WL[(32 + ks + 2) * 64];
          else wq[2 * (ks - 30) + 1] = WH[(2 * (ks - 30) + 1) * 64];
          if constexpr (ks < 4) sp[ks] = __builtin_bit_cast(f32x4, ld128<SC1>(rs, l16 + ks * 1024, stb));
          if constexpr (layer == 0 && ks >= 4 && ks < 8) {
#pragma unroll
            for (int i = 0; i < 4; ++i) cp[ks - 4][i] = __builtin_bit_cast(float, ld32<SC1>(rs, ln * 4 + i * 256 + (ks - 4) * 1024, CS_W1 + fb * 4096));
          }
          if constexpr (ks == 1 && XMODE == 1) pretire(pvx);
          if constexpr (ks == 17) stats_fin(sp, nmean, nc);
          if constexpr (ks == 18) { nc *= -LOG2E; PIN(nc); }
          if constexpr (layer == 0 && ks == 20) {
#pragma unroll
            for (int t = 0; t < 4; ++t) cs1[t] = (cp[t][0] + cp[t][1]) + (cp[t][2] + cp[t][3]);
          }
          if constexpr (ks == 21) { SB(); acc[0] = mfma32f_pad<5>(cs1[0], nmean, acc[0]); }
          if constexpr (ks == 26) acc[2] = mfma32f(cs1[2], nmean, zero16);
          if constexpr (ks == 28) acc[3] = mfma32f(cs1[3], nmean, zero16);
          SB();
        });
        STAMP(sb + 17);
        const u32 hb = HB + layer * HB_SZ + tt * 114688 + fb * 2048;
        float tv[64], ev[64];
        v4u ho[8];
        u32 pv0;
        auto stage = [&](auto V, auto ST_) {
          constexpr int v = V, st = ST_;
          if constexpr (st == 0) { tv[v] = acc[v >> 4][v & 15] * nc; PIN(tv[v]); }
          else if constexpr (st == 1) { ev[v] = __builtin_amdgcn_exp2f(tv[v]); PIN(ev[v]); }
          else if constexpr (st == 2) { ev[v] = 1.f + ev[v]; PIN(ev[v]); }
          else if constexpr (st == 3) { ev[v] = __builtin_amdgcn_rcpf(ev[v]); PIN(ev[v]); }
          else if constexpr (st == 4) { tv[v] = tv[v] * ev[v]; PIN(tv[v]); }
          else if constexpr (v & 1) {
            u32 hh = pk(tv[v - 1], tv[v]);
            PIN(hh);
            ho[v >> 3][(v >> 1) & 3] = hh;
            if constexpr ((v & 7) == 7) st128<SC1>(ho[v >> 3], rs, l16 + ((v >> 3) & 1) * 1024, hb + (v >> 4) * 28672);
          }
        };
        static_for<64>([&](auto GI) {
          constexpr int gi = GI, t = 2 + (gi >> 5), ks = gi & 31;
          acc[t] = mfma32(wq[gi & 3], xf[ks], acc[t]);
          SB();
          if constexpr (gi + 4 < 64) wq[gi & 3] = WH[(gi + 4) * 64];
          if constexpr (layer == 0 && p3_w2gap(gi)) w2ld(IC(W2D + 12 + (gi * W2B) / 8));
          SB();
          static_for<48>([&](auto V) {
            constexpr int d = gi - p3_g0(V);
            if constexpr (d >= 0 && d < 6) stage(V, IC(d));
          });
          if constexpr (gi == HS0) wsignal<p3_younger(layer, 0, HS0)>(rs, lh, sh0);
          if constexpr (gi == HS1) wsignal<p3_younger(layer, 1, HS1)>(rs, lh, sh0 + 128 * CSTR);
          if constexpr (gi == PK0) pv0 = peek(rs, l0, ch0);
          SB();
        });
        STAMP(sb + 18);
        constexpr int W2E = 56 - W2D - 12 - 8 * W2B;
        static_assert(W2E >= 0, "w2 schedule");
        {
          f32x2 t2[8], e2[8];
#pragma unroll
          for (int m = 0; m < 8; ++m) { t2[m] = f32x2{acc[3][2 * m], acc[3][2 * m + 1]} * nc; PIN(t2[m]); }
          SB();
#pragma unroll
          for (int m = 0; m < 8; ++m) { e2[m] = f32x2{__builtin_amdgcn_exp2f(t2[m].x), __builtin_amdgcn_exp2f(t2[m].y)}; PIN(e2[m]); }
          SB();
          if constexpr (HPRE > 0) wfinish(rs, l0, ch0, 8, pv0);
          STAMP(sb + 21);
          hld(0);
#pragma unroll
          for (int m = 0; m < 8; ++m) { e2[m] = e2[m] + 1.f; PIN(e2[m]); }
          SB();
#pragma unroll
          for (int m = 0; m < 8; ++m) { e2[m] = f32x2{__builtin_amdgcn_rcpf(e2[m].x), __builtin_amdgcn_rcpf(e2[m].y)}; PIN(e2[m]); if (m == 1 || m == 5) { SB(); hld(1 + m / 4); } }
          SB();
#pragma unroll
          for (int m = 0; m < 8; ++m) { t2[m] = t2[m] * e2[m]; PIN(t2[m]); }
          SB();
#pragma unroll
          for (int i = 3; i < HPRE; ++i) hld(i);
#pragma unroll
          for (int s = 0; s < 2; ++s)
            st128<SC1>(v4u{pk(t2[4 * s].x, t2[4 * s].y), pk(t2[4 * s + 1].x, t2[4 * s + 1].y), pk(t2[4 * s + 2].x, t2[4 * s + 2].y), pk(t2[4 * s + 3].x, t2[4 * s + 3].y)},
                       rs, l16 + s * 1024, hb + 3 * 28672);
        }
        SB();
        pv1 = peek(rs, l0, ch0 + 128 * CSTR);
        wsignal<p3_younger(layer, 2, 64) + HPRE + 3>(rs, lh, sh0 + 256 * CSTR);
        if constexpr (layer == 0) static_for<W2E>([&](auto E) { w2ld(IC(W2D + 12 + 8 * W2B + E)); });
        SB();
        STAMP(sb + 19);
        if constexpr (HPRE == 0) wfinish(rs, l0, ch0, 8, pv0);
      } else {
        if constexpr (layer == 0) static_for<56>([&](auto E) { w2ld(E); });
        DRAIN();
        BAR();
        wwait(rs, l0, ch0, 8);
#pragma unroll
        for (int i = 0; i < HPRE; ++i) hld(i);
        pv1 = 0;
      }
    }
    {
      const u32 ln = opqv(lane), l16 = ln * 16, r = ln & 31, hf = ln >> 5;
      const u32 wqb = opq(WQKV_T + hp * 98304 + w * 16384);
      u32 pv2, pv3;
      splitk_phase<FrH, WINH, HPRE>(rs, w2f, af, hown, hoth, L, w, l16, -LN2, res,
        [&]() {
          if constexpr (T3SIG < 0) wsignal<2 + WINH - HPRE>(rs, lh, sh0 + 384 * CSTR);
          STAMP(sb + 22); wfinish(rs, l0, ch0 + 128 * CSTR, 8, pv1); STAMP(sb + 20); },
        [&](auto I) {
          constexpr int i = I, g = i - 42;
          if constexpr (i == T3SIG) wsignal<2 + WINH - HPRE + T3SIG + (PK2 < T3SIG)>(rs, lh, sh0 + 384 * CSTR);
          if constexpr (i == PK2) pv2 = peek(rs, l0, ch0 + 256 * CSTR);
          if constexpr (i == PK3) pv3 = peek(rs, l0, ch0 + 384 * CSTR);
          if constexpr (i == 28 - WINH) wfinish(rs, l0, ch0 + 256 * CSTR, 8, pv2);
          if constexpr (i == T3CHK) { STAMP(sb + 27); wfinish(rs, l0, ch0 + 384 * CSTR, 8, pv3); STAMP(sb + 28); }
          if constexpr (KWPOS == 0) {
            if constexpr (layer == 0 && g >= 10 && g <= 14) dma1<g - 10>(rs, l16, wqb, L + w * 16384);
            if constexpr (layer == 0 && g >= 19 && g <= 21) dma1<g - 14>(rs, l16, wqb, L + w * 16384);
          } else {
            if constexpr (layer == 0 && g >= 10 && g < 26) dma1<g - 10>(rs, l16, wqb, L + w * 16384);
          }
        });
      STAMP(sb + 23);
      if constexpr (layer == 0) {
        resid_store(rs, res, l16, hf ? 0x80000000u : (r & 15) * 16 + (r >> 4) * 8, XT + 2 * XT_SZ + tte * 32768 + (n0 >> 3) * 512, ST + 2 * ST_SZ + tte * 4096 + (n0 >> 5) * 256);
        STAMP(sb + 24);
        if constexpr (KWPOS == 0) dma_range<8, 16>(rs, l16, wqb, L + w * 16384);
        wsignal<KWPOS == 0 ? 8 : 0>(rs, l0, cnt_off(C_X + tte));
        STAMP(sb + 25);
      } else {
        const rsrc_t rso = mkrsrc(out, S * D * 2);
#pragma unroll
        for (int s = 0; s < 2; ++s) st128<OAUX>(res[s], rso, r * 1024 + hf * 16 + s * 32, tte * 32768 + n0 * 2);
        if (b == 255 && tid < 16) st64<SC1>(v2u{tag_lo, tag_hi}, rs, tid * 8, EPOCH_OFF);
        STAMP(sb + 24);
      }
    }
  });
}

extern "C" void kernel_launch(void* const* d_in, const int* in_sizes, int n_in, void* d_out, int out_size, void* d_ws,
                              size_t ws_size, hipStream_t stream) {
  (void)hipFuncSetAttribute((const void*)k_main, hipFuncAttributeMaxDynamicSharedMemorySize, LDS_BYTES);
  k_main<<<256, 256, LDS_BYTES, stream>>>((const uint16_t*)d_in[0], (const uint16_t*)d_in[1], (const uint16_t*)d_in[2],
                                          (const uint16_t*)d_in[3], (const uint16_t*)d_in[5], (const uint16_t*)d_in[6],
                                          (unsigned char*)d_ws, (unsigned char*)d_out);
}
```

```cpp
#define TLBW 65536
#define TLBD 1
#include <hip/hip_runtime.h>
#include <hip/hip_bf16.h>
#include <stdint.h>
#include <utility>

typedef unsigned int u32;
typedef u32 v4u __attribute__((ext_vector_type(4)));
typedef u32 v2u __attribute__((ext_vector_type(2)));
typedef short s16x4 __attribute__((ext_vector_type(4)));
typedef __bf16 bf16x8 __attribute__((ext_vector_type(8)));
typedef __bf16 bf16x2 __attribute__((ext_vector_type(2)));
typedef float f32x2 __attribute__((ext_vector_type(2)));
typedef float f32x4 __attribute__((ext_vector_type(4)));
typedef float f32x16 __attribute__((ext_vector_type(16)));
#define LAS __attribute__((address_space(3)))
#define GAS __attribute__((address_space(1)))
#define DI __device__ __forceinline__
typedef __amdgpu_buffer_rsrc_t rsrc_t;

#define DXV 12
#define WINH 14
#define HS0 36
#define HS1 56
#define PK0 52
#define HPRE 0
#define PK2 2
#define T3SIG -1
#define PK3 10
#define T3CHK 24
#define KVCHK 1
#define KVLD 2
#define ODR 8
#define OAUX 0
#define PS1 3
#define PS2 4
#define XMODE 0
#define KMODE 0
#define NQ1 14
#define NQ2 7
#define OPL0 4
#define OPL1 4
#define XDR 4
#define OMODE 1
#define PS4 8
#define KWPOS 1
#define PS3 4
#define WOK 2
#define CPF 1
#define CPW 1
#define ROT 1
#define XBAR 1
#define JPK 0
#define XA 8
#define XB 20
#define UB 10
#define FPK 4
#define FCK 6
#define KVPK1 6
#define W2D 12
#define W2B 4
constexpr int S = 2048, D = 512;
constexpr u32 CTRL = 0;
constexpr u32 WQKV_T = 262144;
constexpr u32 WPHI_T = WQKV_T + 16 * 98304;
constexpr u32 WO_T = WPHI_T + 32768;
constexpr u32 W1_T = WO_T + 524288;
constexpr u32 W2_T = W1_T + 14 * 131072;
constexpr u32 CS_QKV = W2_T + 8 * 4 * 28 * 2048;
constexpr u32 CS_W1 = CS_QKV + 65536;
constexpr u32 XT = CS_W1 + 65536;
constexpr u32 XT_SZ = 2097152;
constexpr u32 ST = XT + 4 * XT_SZ;
constexpr u32 ST_SZ = 262144;
constexpr u32 OB = ST + 4 * ST_SZ;
constexpr u32 HB = OB + 2 * XT_SZ;
constexpr u32 HB_SZ = 64 * 114688;
constexpr u32 KVP = HB + 2 * HB_SZ;
constexpr u32 KVP_HS = 16 * 512 + 2048;
constexpr u32 KVP_SZ = 64 * KVP_HS;
constexpr u32 WS_END = KVP + 2 * KVP_SZ;
#define CSTR 128
enum { C_X = 0, C_KV = 64, C_O = 192, C_X1 = 448, C_H = 576, C_P = 1600, C_J = 1632, C_TMO = 1663, NCNT = 1664 };
constexpr u32 INIT_OFF = CTRL + NCNT * CSTR, EPOCH_OFF = INIT_OFF + 16 * CSTR, FLAG_OFF = EPOCH_OFF + CSTR;
static_assert(FLAG_OFF + 192 * CSTR <= WQKV_T, "control area");

#define STAMP(i)
constexpr float LOG2E = 1.4426950408889634f;
constexpr float LN2 = 0.6931471805599453f;

DI u32 pk(float a, float b) {
  f32x2 v = {a, b};
  bf16x2 r = __builtin_convertvector(v, bf16x2);
  return __builtin_bit_cast(u32, r);
}
DI float bflo(u32 u) { return __builtin_bit_cast(float, u << 16); }
DI float bfhi(u32 u) { return __builtin_bit_cast(float, u & 0xffff0000u); }
DI f32x16 mfma32(v4u a, v4u b, f32x16 c) {
  f32x16 d = __builtin_amdgcn_mfma_f32_32x32x16_bf16(__builtin_bit_cast(bf16x8, a), __builtin_bit_cast(bf16x8, b), c, 0, 0, 0);
  asm volatile("" : "+a"(d));
  return d;
}
DI f32x4 mfma16(v4u a, v4u b, f32x4 c) {
  return __builtin_amdgcn_mfma_f32_16x16x32_bf16(__builtin_bit_cast(bf16x8, a), __builtin_bit_cast(bf16x8, b), c, 0, 0, 0);
}
DI f32x16 mfma32f(float a, float b, f32x16 c) {
  f32x16 d = __builtin_amdgcn_mfma_f32_32x32x2f32(a, b, c, 0, 0, 0);
  asm volatile("" : "+a"(d));
  return d;
}
template <int N> DI f32x16 mfma32f_pad(float a, float b, f32x16 c) {
  asm volatile("s_nop %1" : "+a"(c) : "n"(N));
  return mfma32f(a, b, c);
}
DI float dot2(u32 a, u32 b, float c) {
  return __builtin_amdgcn_fdot2_f32_bf16(__builtin_bit_cast(bf16x2, a), __builtin_bit_cast(bf16x2, b), c, false);
}
template <int AUX> DI v4u ld128(rsrc_t rs, u32 voff, u32 soff) { return __builtin_amdgcn_raw_buffer_load_b128(rs, voff, soff, AUX); }
template <int AUX> DI v2u ld64(rsrc_t rs, u32 voff, u32 soff) { return __builtin_amdgcn_raw_buffer_load_b64(rs, voff, soff, AUX); }
template <int AUX> DI u32 ld32(rsrc_t rs, u32 voff, u32 soff) { return __builtin_amdgcn_raw_buffer_load_b32(rs, voff, soff, AUX); }
template <int AUX> DI void st128(v4u v, rsrc_t rs, u32 voff, u32 soff) {
  __builtin_amdgcn_raw_buffer_store_b128(v, rs, voff, soff, AUX);
  asm volatile("s_nop 1" ::"v"(v) : "memory");
}
template <int AUX> DI void st64(v2u v, rsrc_t rs, u32 voff, u32 soff) { __builtin_amdgcn_raw_buffer_store_b64(v, rs, voff, soff, AUX); }
DI rsrc_t mkrsrc(const void* p, u32 bytes) { return __builtin_amdgcn_make_buffer_rsrc((void*)p, 0, (int)bytes, 0x00020000); }
DI float sum32(float v) {
  u32 u = __builtin_bit_cast(u32, v);
  auto r = __builtin_amdgcn_permlane32_swap(u, u, false, false);
  const u32 r0 = r[0], r1 = r[1];
  return __builtin_bit_cast(float, r0) + __builtin_bit_cast(float, r1);
}
DI float sum16(float v) {
  u32 u = __builtin_bit_cast(u32, v);
  auto r = __builtin_amdgcn_permlane16_swap(u, u, false, false);
  const u32 r0 = r[0], r1 = r[1];
  return __builtin_bit_cast(float, r0) + __builtin_bit_cast(float, r1);
}
DI float mix16(float a, float b) {
  auto r = __builtin_amdgcn_permlane16_swap(__builtin_bit_cast(u32, a), __builtin_bit_cast(u32, b), false, false);
  const u32 r0 = r[0], r1 = r[1];
  return __builtin_bit_cast(float, r0) + __builtin_bit_cast(float, r1);
}
DI v2u trread(LAS unsigned char* __restrict__ p) {
  return __builtin_bit_cast(v2u, __builtin_amdgcn_ds_read_tr16_b64_v4i16((LAS s16x4*)p));
}
DI int rho32(int reg, int hf) { return (reg & 3) + 8 * (reg >> 2) + 4 * hf; }
constexpr u32 ONES2 = 0x3f803f80u;

DI v4u pack8(const f32x16& a, int s) {
  v4u r;
#pragma unroll
  for (int m = 0; m < 4; ++m) r[m] = pk(a[8 * s + 2 * m], a[8 * s + 2 * m + 1]);
  return r;
}

#define SB() __builtin_amdgcn_sched_barrier(0)
#define PIN(x) asm volatile("" : "+v"(x))
#define BAR() asm volatile("s_waitcnt lgkmcnt(0)\n\ts_barrier" ::: "memory")
#define DRAIN() asm volatile("s_waitcnt vmcnt(0)" ::: "memory")
#define RLX __ATOMIC_RELAXED, __HIP_MEMORY_SCOPE_AGENT
constexpr int SC1 = 16;

DI u32 opq(u32 x) { asm volatile("" : "+s"(x)); return x; }
DI u32 opqv(u32 x) { asm volatile("" : "+v"(x)); return x; }
template <int AUX> DI v4u ldf(rsrc_t rs, u32 vo, u32 sbase, u32 off) { return ld128<AUX>(rs, vo + (off & 4095u), sbase + (off & ~4095u)); }
template <int I> DI void dma1(rsrc_t rs, u32 vo, u32 sbase, LAS unsigned char* d) {
  __builtin_amdgcn_raw_ptr_buffer_load_lds(rs, (LAS u32*)(d + (I >> 2) * 4096), 16, vo, sbase + (I >> 2) * 4096, (I & 3) * 1024, SC1);
}
template <int I0, int I1> DI void dma_range(rsrc_t rs, u32 vo, u32 sbase, LAS unsigned char* d) {
  if constexpr (I0 < I1) {
    dma1<I0>(rs, vo, sbase, d);
    dma_range<I0 + 1, I1>(rs, vo, sbase, d);
  }
}
template <class F, int... I> DI void static_for_impl(F&& f, std::integer_sequence<int, I...>) { (f(std::integral_constant<int, I>{}), ...); }
template <int N, class F> DI void static_for(F&& f) { static_for_impl(f, std::make_integer_sequence<int, N>{}); }
DI u32 cnt_off(int idx) { return CTRL + idx * CSTR; }
DI u32 lane0_off() { return (threadIdx.x & 63) == 0 ? 0u : 0x80000000u; }
DI void add1(rsrc_t rs, u32 l0, u32 coff) { (void)__builtin_amdgcn_raw_ptr_buffer_atomic_add_i32(1, rs, l0, coff, 0); }
template <int N> DI void wsignal(rsrc_t rs, u32 l0, u32 coff) {
  SB();
  asm volatile("s_waitcnt vmcnt(%0)" ::"n"(N) : "memory");
  add1(rs, l0, coff);
  SB();
}
DI u32 peek(rsrc_t rs, u32 l0, u32 coff) { return __builtin_amdgcn_raw_ptr_buffer_atomic_add_i32(0, rs, l0, coff, 0); }
DI void wfinish(rsrc_t rs, u32 l0, u32 coff, u32 n, u32 pv) {
  if (__builtin_amdgcn_readfirstlane(pv) < n) {
#pragma unroll 1
    for (u32 spins = 0;; ++spins) {
      if (__builtin_amdgcn_readfirstlane(peek(rs, l0, coff)) >= n) break;
      if (spins > (1u << 16)) { __builtin_amdgcn_raw_buffer_store_b32(1u, rs, 0, cnt_off(C_TMO), SC1); break; }
    }
  }
  asm volatile("" ::: "memory");
}
DI void wwait(rsrc_t rs, u32 l0, u32 coff, u32 n) { wfinish(rs, l0, coff, n, peek(rs, l0, coff)); }
DI void apeek(rsrc_t rs, u32 l0, u32 coff, u32& p) {
  SB();
  asm volatile("v_mov_b32 %0, 0\n\tbuffer_atomic_add %0, %1, %2, %3 offen sc0 ; APEEK" : "=&v"(p) : "v"(l0), "s"(rs), "s"(coff) : "memory");
  SB();
}
#define PCHK(P, W) "s_waitcnt vmcnt(" W ")\n\tv_readfirstlane_b32 %3, " P "\n\ts_cmp_ge_u32 %3, %8\n\ts_cbranch_scc1 .Lpd%=\n\tv_mov_b32 " P ", 0\n\tbuffer_atomic_add " P ", %5, %6, %7 offen sc0\n\t"
template <int N0, int N1, int N2> DI void pfinish(rsrc_t rs, u32 l0, u32 coff, u32 n, u32 (&p)[3]) {
  u32 t, cnt;
  SB();
  asm volatile(PCHK("%0", "%9") PCHK("%1", "%10") PCHK("%2", "%11") "s_movk_i32 %4, 0x7fff\n.Lpl%=:\n\t" PCHK("%0", "2") PCHK("%1", "2") PCHK("%2", "2")
               "s_sub_u32 %4, %4, 1\n\ts_cbranch_scc0 .Lpl%=\n.Lpd%=:"
               : "+v"(p[0]), "+v"(p[1]), "+v"(p[2]), "=&s"(t), "=&s"(cnt)
               : "v"(l0), "s"(rs), "s"(coff), "n"(n), "n"(N0), "n"(N1 + 1), "n"(N2 + 2)
               : "memory", "scc");
  SB();
}
DI void pretire(u32 (&p)[3]) { asm volatile("; PRETIRE %0 %1 %2" ::"v"(p[0]), "v"(p[1]), "v"(p[2])); }
#define NAP(n) do { SB(); __builtin_amdgcn_s_sleep(n); SB(); } while (0)
DI void poll3(rsrc_t rs, u32 vo, u32 need) {
#pragma unroll 1
  for (u32 spins = 0;; ++spins) {
    const u32 v = peek(rs, vo, 0);
    if ((__builtin_amdgcn_ballot_w64(v >= need) & 7ull) == 7ull) break;
    if (spins > (1u << 16)) { __builtin_amdgcn_raw_buffer_store_b32(1u, rs, 0, cnt_off(C_TMO), SC1); break; }
  }
  asm volatile("" ::: "memory");
}
template <int CTRL_> DI float dpp_add(float v) {
  return v + __builtin_bit_cast(float, __builtin_amdgcn_update_dpp(0, __builtin_bit_cast(int, v), CTRL_, 0xf, 0xf, true));
}

constexpr int SCR = 131072;
constexpr int PK_OFF = SCR, PK_STR = 144, VV_OFF = PK_OFF + 128 * PK_STR, VV_STR = 80, KF_OFF = VV_OFF + 128 * VV_STR;
constexpr int XS_OFF = 98304;
constexpr int RED_OFF = SCR;
constexpr int LDS_BYTES = 163840;
static_assert(KF_OFF + 4 * 544 <= LDS_BYTES, "lds");

DI void lds_put1(LAS unsigned char* p, const f32x16& a, int q) {
  *(LAS f32x4*)(p + q * 1024) = f32x4{a[4 * q], a[4 * q + 1], a[4 * q + 2], a[4 * q + 3]};
}

extern __shared__ __attribute__((aligned(16))) unsigned char smem[];

struct Sch { int put2[4], put3[4], bar1, rd[8], fu[8], put1[4], bar2, rd1, f1[4]; };
constexpr Sch sch_of(int KL) {
  return KL >= 14 ? Sch{{0, 1, 2, 3}, {4, 5, 6, 7}, 9, {9, 9, 9, 10, 10, 10, 11, 11}, {12, 13, 14, 19, 21, 22, 23, 24}, {15, 16, 17, 18}, 20, 20, {25, 26, 27, 27}}
                  : Sch{{0, 0, 1, 1}, {2, 2, 3, 3}, 5, {5, 5, 5, 5, 6, 6, 6, 6}, {7, 7, 8, 8, 11, 12, 12, 13}, {9, 9, 10, 10}, 11, 11, {14, 14, 15, 15}};
}
struct FrO {
  static constexpr int NS = 16, KL = 8;
  static constexpr bool own(int i) { return i >= 8; }
  static constexpr int ks(int i) { return i & 7; }
  static constexpr u32 off(int i) { return (i & 7) * 1024; }
  static constexpr int lstep(int q, int win) { return q - win; }
};
struct FrH {
  static constexpr int NS = 56, KL = 14;
  static constexpr int part(int i) { return i < 28 ? i / 14 : 2 + ((i / 7) & 1); }
  static constexpr bool own(int i) { return i < 28 ? ((i / 7) & 1) : i >= 42; }
  static constexpr int ks(int i) { return part(i) * 7 + i % 7; }
  static constexpr u32 off(int i) { return part(i) * 28672 + (i % 7) * 1024; }
  static constexpr int lstep(int q, int win) { return q >= 35 && q - win < T3CHK ? T3CHK : q - win; }
};
template <class Fr> DI v4u ld_fr(rsrc_t rs, u32 l16, u32 a_own, u32 a_oth, int i) { return ldf<SC1>(rs, l16, Fr::own(i) ? a_own : a_oth, Fr::off(i)); }
template <class Fr, int WIN, int PRE, class Pre, class Mid>
DI void splitk_phase(rsrc_t rs, const v4u (&wf)[Fr::NS / 2][2], v4u (&af)[WIN], u32 a_own, u32 a_oth, LAS unsigned char* L, int w, u32 l16, float scale,
                     v4u (&res)[2], Pre&& pre_loop, Mid&& mid) {
  const f32x16 zero16 = {0.f, 0.f, 0.f, 0.f, 0.f, 0.f, 0.f, 0.f, 0.f, 0.f, 0.f, 0.f, 0.f, 0.f, 0.f, 0.f};
  constexpr int NS = Fr::NS, KP = Fr::KL, E0 = NS - KP;
  constexpr Sch sc = sch_of(KP);
  static_assert(WIN >= KP, "window");
  auto ld = [&](int i) { return ld_fr<Fr>(rs, l16, a_own, a_oth, i); };
#pragma unroll
  for (int i = PRE; i < WIN; ++i) { af[i] = ld(i); SB(); }
  LAS unsigned char* r2 = L + opqv(RED_OFF + l16 + (w ^ 2) * 8192);
  LAS unsigned char* r3 = L + opqv(RED_OFF + l16 + (w ^ 3) * 8192 + 4096);
  LAS unsigned char* r0 = L + opqv(RED_OFF + l16 + w * 8192);
  LAS unsigned char* r1 = L + opqv(RED_OFF + l16 + (w ^ 1) * 8192);
  pre_loop();
  SB();
  f32x16 a0 = zero16, a1 = zero16, a2 = zero16, a3 = zero16;
  float u[16];
  static_for<E0>([&](auto I) {
    constexpr int i = I, ks = Fr::ks(i), e = i - (E0 - 4);
    const v4u bb = af[i % WIN];
    if constexpr (!Fr::own(i)) a2 = mfma32(wf[ks][0], bb, a2);
    else a1 = mfma32(wf[ks][1], bb, a1);
    SB();
    mid(I);
    static_for<NS - WIN>([&](auto Q) { if constexpr (Fr::lstep(Q + WIN, WIN) == i) { af[Q % WIN] = ld(Q + WIN); SB(); } });
    SB();
    if constexpr (!Fr::own(i)) a3 = mfma32(wf[ks][1], bb, a3);
    else a0 = mfma32(wf[ks][0], bb, a0);
    SB();
    if constexpr (e >= 0) {
      const u32 ra = res[e >> 1][2 * (e & 1)], rb = res[e >> 1][2 * (e & 1) + 1];
      u[4 * e] = bflo(ra); u[4 * e + 1] = bfhi(ra); u[4 * e + 2] = bflo(rb); u[4 * e + 3] = bfhi(rb);
#pragma unroll
      for (int m = 0; m < 4; ++m) PIN(u[4 * e + m]);
    }
    SB();
  });
  f32x4 p[8];
  auto acc4 = [&](int q, const f32x4& x) {
#pragma unroll
    for (int m = 0; m < 4; ++m) { const float xv = x[m]; u[4 * q + m] = __builtin_fmaf(xv, scale, u[4 * q + m]); PIN(u[4 * q + m]); }
  };
  static_for<2 * KP>([&](auto G) {
    constexpr int g = G, k = g % KP, ks = Fr::ks(E0 + k);
    if constexpr (g < KP) a1 = mfma32(wf[ks][1], af[(E0 + k) % WIN], a1);
    else a0 = mfma32(wf[ks][0], af[(E0 + k) % WIN], a0);
    SB();
    if constexpr (g == sc.bar1 || g == sc.bar2) BAR();
    static_for<8>([&](auto Q) { if constexpr (sc.rd[Q] == g) p[Q] = *(LAS f32x4*)(r0 + Q * 1024); });
    if constexpr (g == sc.rd1) static_for<4>([&](auto Q) { p[Q] = *(LAS f32x4*)(r1 + Q * 1024); });
    SB();
    static_for<4>([&](auto Q) { if constexpr (sc.put2[Q] == g) lds_put1(r2, a2, Q); });
    static_for<4>([&](auto Q) { if constexpr (sc.put3[Q] == g) lds_put1(r3, a3, Q); });
    static_for<4>([&](auto Q) { if constexpr (sc.put1[Q] == g) lds_put1(r0, a1, Q); });
    SB();
    static_for<8>([&](auto Q) { if constexpr (sc.fu[Q] == g) acc4(Q & 3, p[Q]); });
    static_for<4>([&](auto Q) { if constexpr (sc.f1[Q] == g) acc4(Q, p[Q]); });
    SB();
    mid(std::integral_constant<int, E0 + g>{});
    SB();
  });
#pragma unroll
  for (int q = 0; q < 4; ++q) {
    acc4(q, f32x4{a0[4 * q], a0[4 * q + 1], a0[4 * q + 2], a0[4 * q + 3]});
    res[q >> 1][2 * (q & 1)] = pk(u[4 * q], u[4 * q + 1]);
    res[q >> 1][2 * (q & 1) + 1] = pk(u[4 * q + 2], u[4 * q + 3]);
  }
}
DI void resid_store(rsrc_t rs, const v4u (&res)[2], u32 l16, u32 sto, u32 xoff, u32 soff) {
#pragma unroll
  for (int s = 0; s < 2; ++s) st128<SC1>(res[s], rs, l16 + s * 1024, xoff);
  SB();
  float s0 = 0.f, q0 = 0.f, s1 = 0.f, q1 = 0.f;
#pragma unroll
  for (int m = 0; m < 4; ++m) {
    s0 = dot2(res[0][m], ONES2, s0); q0 = dot2(res[0][m], res[0][m], q0);
    s1 = dot2(res[1][m], ONES2, s1); q1 = dot2(res[1][m], res[1][m], q1);
  }
  st64<SC1>(__builtin_bit_cast(v2u, f32x2{sum32(s0 + s1), sum32(q0 + q1)}), rs, sto, soff);
}

DI void stats_fin(const f32x4 (&sp)[4], float& nmean, float& rstd) {
  float t[4];
#pragma unroll
  for (int m = 0; m < 4; ++m) { t[m] = sp[0][m] + sp[1][m]; PIN(t[m]); }
#pragma unroll
  for (int i = 2; i < 4; ++i)
#pragma unroll
    for (int m = 0; m < 4; ++m) { t[m] += sp[i][m]; PIN(t[m]); }
  const float s = mix16(t[0], t[2]), q = mix16(t[1], t[3]);
  nmean = sum32(s) * (-1.f / 512.f);
  const float var = fmaxf(sum32(q) * (1.f / 512.f) - nmean * nmean, 0.f);
  rstd = __builtin_amdgcn_rsqf(var + 1e-5f);
  PIN(nmean);
  PIN(rstd);
}

template <class Hook> DI void dual_softmax(const f32x16& p, float c, u32 (&o)[16], Hook&& hook) {
  float mx = p[0], mn = p[0];
#pragma unroll
  for (int i = 1; i < 16; ++i) { mx = fmaxf(mx, p[i]); mn = fminf(mn, p[i]); }
  float ap = -mx * c, an = mn * c;
  PIN(ap);
  PIN(an);
  SB();
  hook(std::integral_constant<int, 0>{});
  SB();
  f32x2 ep[8], en[8];
#pragma unroll
  for (int i = 0; i < 8; ++i) {
    const f32x2 pv = {p[2 * i], p[2 * i + 1]};
    ep[i] = pv * c + ap;
    en[i] = pv * (-c) + an;
    PIN(ep[i]);
    PIN(en[i]);
  }
  SB();
  hook(std::integral_constant<int, 1>{});
  SB();
  f32x2 sa, sc;
  static_for<8>([&](auto I) {
    constexpr int i = I;
    ep[i] = f32x2{__builtin_amdgcn_exp2f(ep[i].x), __builtin_amdgcn_exp2f(ep[i].y)};
    en[i] = f32x2{__builtin_amdgcn_exp2f(en[i].x), __builtin_amdgcn_exp2f(en[i].y)};
    PIN(ep[i]);
    PIN(en[i]);
    if constexpr (i == 2) { sa = ep[0] + ep[1]; sc = en[0] + en[1]; PIN(sa); PIN(sc); }
    if constexpr (i > 2) { sa += ep[i - 1]; sc += en[i - 1]; PIN(sa); PIN(sc); }
    SB();
    hook(std::integral_constant<int, 2 + i>{});
    SB();
  });
  sa += ep[7];
  sc += en[7];
  const float rp = __builtin_amdgcn_rcpf(sa.x + sa.y), rn = __builtin_amdgcn_rcpf(sc.x + sc.y);
#pragma unroll
  for (int i = 0; i < 8; ++i) { ep[i] *= rp; PIN(ep[i]); }
#pragma unroll
  for (int i = 0; i < 8; ++i) { en[i] *= rn; PIN(en[i]); }
  SB();
#pragma unroll
  for (int i = 0; i < 8; ++i) { o[i] = pk(ep[i].x, ep[i].y); o[8 + i] = pk(en[i].x, en[i].y); }
}
constexpr int p3_g0(int v) { return (v * 58) / 48; }
constexpr bool p3_w2gap(int gi) { return (gi * W2B) / 8 != ((gi + 1) * W2B) / 8; }
constexpr int p3_pre(int layer, int g) {
  int n = layer == 0 && p3_w2gap(g);
  for (int q = 0; q < 6; ++q) n += p3_g0(8 * q + 7) + 5 == g;
  return n;
}
constexpr int p3_post(int g) { return (g == HS0) + (g == HS1) + (g == PK0); }
constexpr int p3_younger(int layer, int t, int gw) {
  const int gs = p3_g0(16 * t + 15) + 5;
  int n = p3_post(gs);
  for (int g = gs + 1; g < gw; ++g) n += p3_pre(layer, g) + p3_post(g);
  return n + (gw < 64 ? p3_pre(layer, gw) : 0);
}
static_assert(p3_g0(32) >= 35 && p3_g0(47) + 5 < 64 && p3_g0(15) + 5 < HS0 && p3_g0(31) + 5 < HS1 && HS0 != HS1, "P3 schedule");

__global__ void __launch_bounds__(256, 1)
k_main(const uint16_t* __restrict__ x, const uint16_t* __restrict__ wqkv, const uint16_t* __restrict__ wphi,
       const uint16_t* __restrict__ wo_, const uint16_t* __restrict__ w1, const uint16_t* __restrict__ w2,
       unsigned char* ws, unsigned char* out) {
  LAS unsigned char* L = (LAS unsigned char*)smem;
  const int b = blockIdx.x, tid = threadIdx.x, lane = tid & 63, w = __builtin_amdgcn_readfirstlane(tid >> 6);
  const int xcd = b & 7, j = b >> 3;
  const int rt = 4 * (xcd >> 1) + (j >> 3), hp = 8 * (xcd & 1) + (j & 7), tt = 4 * rt + w;
  const bool p3on = (j & 7) < 7;
  const int fb = p3on ? 7 * (xcd & 1) + (j & 7) : 0;
  const int rt2 = 4 * xcd + (j >> 3), cb = j & 7;
  const int tte = 2 * rt2 + (w >> 1), n0 = 64 * cb + 32 * (w & 1);
  const rsrc_t rs = mkrsrc(ws, WS_END);
  const f32x16 zero16 = {0.f, 0.f, 0.f, 0.f, 0.f, 0.f, 0.f, 0.f, 0.f, 0.f, 0.f, 0.f, 0.f, 0.f, 0.f, 0.f};
  STAMP(63);
  v4u res[2];
  float csq[3], cs1[4];
  v4u wph[2];
  u32 tag_lo, tag_hi, tws = 0;
  const u32 l0 = lane0_off();
  const u32 nlh = !p3on ? 0u : (fb == 3 || fb == 10) ? 2u : 1u;
  v4u xr[32];
  const uint16_t* xo = x;
  asm volatile("" : "+s"(xo));
  const rsrc_t rsx = mkrsrc(xo, S * D * 2);
  auto trfrag = [&](LAS unsigned char* p, int str) { const v2u t0 = trread(p), t1 = trread(p + str); return v4u{t0.x, t0.y, t1.x, t1.y}; };
  const u32 sv = ((lane >> 2) & 1) == ((lane >> 4) & 1) ? ONES2 : 0u;
  v4u selm = {sv, sv, sv, sv};
  if (b >= 248 && w < 2) {
#pragma unroll
    for (int i = 0; i < NCNT * CSTR / 16384; ++i) st128<SC1>(v4u{0u, 0u, 0u, 0u}, rs, lane * 16, CTRL + ((b - 248) * 2 + w) * (NCNT * CSTR / 16) + i * 1024);
  }
  const v2u ep = ld64<SC1>(rs, 0, EPOCH_OFF);
  SB();
  u32 cpf;
  {
    unsigned long long pc;
    u32 len;
    asm volatile("s_getpc_b64 %0\n.Lkcode0:\n\ts_mov_b32 %1, .Lkcode1-.Lkcode0" : "=s"(pc), "=s"(len));
    cpf = ld32<0>(mkrsrc((const void*)pc, len), w == CPW ? lane * 128 + (j & 7) * 8192 : 0x80000000u, 0);
  }
  SB();
  int kind = 4, nb = 0, kq = 0, ld = 512;
  const uint16_t* W = x;
  if (w == 0) {
    if (b < 192) { kind = 0; W = wqkv; ld = 1536; nb = b >> 3; kq = b & 7; }
  } else if (w >= 2) {
    const int hw = w & 1;
    if (b < 112) { kind = 1; W = w1; ld = 1792; nb = b >> 2; kq = (b & 3) * 2 + hw; }
    else if (b < 144) { kind = 2; W = wo_; nb = (b - 112) >> 2; kq = (b & 3) * 2 + hw; }
    else { kind = 3; W = w2; nb = (b - 144) / 14; kq = ((b - 144) - 14 * nb) * 2 + hw; }
  }
  LAS unsigned char* img = L + SCR + w * 8192;
  {
    const rsrc_t rsu = mkrsrc(W, kind < 4 ? (kind == 3 ? 1792 : 512) * ld * 2 : 0);
    const u32 vu = (lane >> 3) * ld * 2 + (((lane & 7) ^ ((lane >> 2) & 4)) * 16);
#pragma unroll
    for (int i = 0; i < 8; ++i) __builtin_amdgcn_raw_ptr_buffer_load_lds(rsu, (LAS u32*)(img + i * 1024), 16, vu, (64 * kq + 8 * i) * ld * 2 + nb * 128, 0, 0);
  }
  SB();
  const u32 vx = (lane >> 3) * 1024 + (lane & 7) * 16;
  const u32 rot = ROT ? j & 7 : 0;
  auto xld = [&](auto I) { constexpr int i = I; xr[i] = ld128<0>(rsx, vx, tt * 32768 + (i & 3) * 8192 + (((i >> 2) ^ rot) * 128)); SB(); };
  static_for<XA>([&](auto I) { xld(I); });
  u32 obase, cstr, kstr, kstr2, csb, ovo, cvo, ua;
  {
    const int t32 = 2 * nb, sec = t32 >> 4, tile = sec == 0 ? 2 : sec - 1;
    obase = opq(kind == 0 ? WQKV_T + (t32 & 15) * 98304 + tile * 32768 + kq * 4096 : kind == 1 ? W1_T + t32 * 32768 + kq * 4096
                : kind == 2 ? WO_T + (nb * 32 + kq * 4) * 2048 : W2_T + (nb * 112 + (kq & 1) * 56 + (kq >> 1) * 2) * 2048);
    cstr = opq(kind == 0 ? 98304 : kind == 1 ? 32768 : 1024);
    kstr = opq(kind < 2 ? 1024 : 2048);
    kstr2 = opq(kind == 3 ? 57344 : 2 * kstr);
    csb = opq(kind == 0 ? CS_QKV + (((t32 & 15) * 3 + tile) * 8 + kq) * 128 : CS_W1 + (t32 * 8 + kq) * 128);
    ovo = opqv(kind < 4 ? lane * 16 : 0x80000000u);
    cvo = opqv(kind < 2 ? (lane >> 5) * (kind == 0 ? 3072 : 1024) + (lane & 31) * 4 : 0x80000000u);
    const u32 c16 = lane & 15, Q = ((lane >> 2) & 4) + (lane & 3);
    const u32 Cc = kind == 0 ? 4 * (Q & 1) + (Q >> 1) : (Q & 4) + 2 * (Q & 1) + ((Q >> 1) & 1);
    ua = opqv((8 * (lane >> 5) + (c16 >> 2)) * 128 + (c16 >> 3) * 64 + Cc * 8);
  }
  const u32 jadd = opq(cnt_off(C_J + (b < 112 ? 16 + (b >> 3) : b < 144 ? (b - 112) >> 2 : (b - 144) / 14)));
  auto unit = [&]() {
    v4u o[8];
#pragma unroll
    for (int i = 0; i < 8; ++i) o[i] = trfrag(img + (ua ^ ((i >> 2) * 64)) + (i & 3) * 2048, 512);
    SB();
    f32x4 cs[2] = {{0.f, 0.f, 0.f, 0.f}, {0.f, 0.f, 0.f, 0.f}};
#pragma unroll
    for (int i = 0; i < 8; ++i) {
      st128<SC1>(o[i], rs, ovo, obase + (i >> 2) * cstr + ((i >> 1) & 1) * kstr2 + (i & 1) * kstr);
      cs[i >> 2] = mfma16(selm, o[i], cs[i >> 2]);
    }
    const float c0 = cs[0][0], c1 = cs[1][0];
    __builtin_amdgcn_raw_buffer_store_b32(__builtin_bit_cast(u32, lane < 32 ? c0 : c1), rs, cvo, csb, SC1);
    SB();
  };
  SB();
  const u32 tvo = opqv((((b >= 248 && w < 2) || kind == 0) && lane < 16) ? lane * 8 : 0x80000000u);
  const u32 tso = opq(b >= 248 ? INIT_OFF + (((b & 7) * 2 + w) & 15) * CSTR : FLAG_OFF + b * CSTR);
  SB();
  v4u w2f[28][2];

  static_for<2>([&](auto LAYER) __attribute__((always_inline)) {
    constexpr int layer = LAYER;
    constexpr int sb = layer * 29;
    v4u wo[8][2];
    const u32 wb = opq(W2_T + cb * 229376 + w * 14336);
    const u32 wsw = (w & 1) * 1024;
    u32 pvx[3];
#define IC(n) std::integral_constant<int, n>{}
    {
      const u32 ln = opqv(lane), l16 = ln * 16, r = ln & 31, hf = ln >> 5;
      if constexpr (layer) STAMP(sb + 0);
      constexpr int DX = DXV;
      v4u xf[32];
      const u32 xb = XT + (2 * layer) * XT_SZ + tt * 32768;
      if constexpr (layer > 0 && XMODE == 1) apeek(rs, l0, cnt_off(C_X + tt), pvx[0]);
      SB();
      f32x4 sp[4];
      const u32 stb = ST + (2 * layer) * ST_SZ + tt * 4096;
      const u32 wqb = opq(WQKV_T + hp * 98304 + 65536 + w * 8192);
      const u32 g4 = ln >> 4, c16 = ln & 15;
      LAS unsigned char* a_v = L + opqv(VV_OFF + (32 * w + r) * VV_STR + hf * 32);
      LAS unsigned char* a_pk = L + opqv(PK_OFF + (32 * w + r) * PK_STR + hf * 64);
      LAS unsigned char* a_pa = L + opqv(PK_OFF + (8 * g4 + (c16 >> 2)) * PK_STR + w * 32 + (ln & 3) * 8);
      LAS unsigned char* a_pb = L + opqv(VV_OFF + (8 * g4 + (c16 >> 2)) * VV_STR + (w >> 1) * 32 + (ln & 3) * 8);
      const u32 o_kv = opqv((g4 >> 1) * 256 + c16 * 16 + (g4 & 1) * 8), o_z = opqv(hf ? 0x80000000u : r * 4);
      u32 jv = 0;
      constexpr int wok = layer ? WOK : 0, NPF = 16 - wok + (layer ? 24 : 32);
      constexpr int s_q1 = 0, s_q2 = s_q1 + NQ1, s_od = s_q2 + NQ2, s_op = s_od + ODR, n_op = layer ? OPL1 : OPL0, s_p2 = s_op + n_op, s_xd = NPF - XDR;
      static_assert(s_p2 <= s_xd && s_xd - s_p2 <= 12 && s_od <= NPF - 8 && s_op >= 16 - wok, "prefetch schedule");
      LAS v4u* WL = (LAS v4u*)(L + l16);
      LAS v4u* WQ = (LAS v4u*)(L + opqv(l16 + 65536));
      const u32 wob = opq(WO_T + (cb * 4 + w) * 16384);
      auto wold = [&](auto I) {
        constexpr int i = I;
        wo[i >> 1][i & 1] = ld128<SC1>(rs, l16 + ((i >> 1) & 1) * 2048, wob + (i >> 2) * 4096 + ((i & 1) ? 1024 - wsw : wsw));
      };
      f32x16 aK = zero16, aV = zero16, aQ;
      float rstd, nmean, c;
      const u32 jvo = opqv(ln == 0 ? cnt_off(C_J + cb) : ln == 1 ? cnt_off(C_J + 16 + fb) : 0x80000000u);
      float cp[3][4];
      v4u wk[3], wv[3];
      SB();
      if constexpr (layer == 0) {
        LAS unsigned char* sw[2];
        LAS unsigned char* sr[4];
#pragma unroll
        for (int i = 0; i < 2; ++i) sw[i] = L + opqv(XS_OFF + w * 8192 + (ln >> 3) * 128 + (((ln & 7) ^ (4 * i + (ln >> 4))) * 16));
#pragma unroll
        for (int f = 0; f < 4; ++f) sr[f] = L + opqv(XS_OFF + w * 8192 + r * 128 + (((2 * f + hf) ^ ((r >> 1) & 7)) * 16));
        const u32 fu = ln >> 1, fvo = opqv(ln >= 12 ? 0x80000000u : FLAG_OFF + ((fu < 4 ? (1 + (w >> 1)) * 64 + (w & 1) * 4 + fu : 2 * w + fu - 4) + (hp >> 1) * 8) * CSTR + (ln & 1) * 4);
        PIN(selm);
        SB();
        if (w == 0) {
          asm volatile("s_waitcnt vmcnt(%0)" ::"n"(XA) : "memory");
          STAMP(58);
          unit();
          STAMP(59);
        }
        if (XBAR) asm volatile("s_barrier" ::: "memory");
        {
          const unsigned long long e = ((unsigned long long)__builtin_amdgcn_readfirstlane(ep.y) << 32) | __builtin_amdgcn_readfirstlane(ep.x);
          unsigned long long t = e * 0x9E3779B97F4A7C15ull + 0x632BE59BD9B4E019ull;
          if (t == e) t = ~e;
          tag_lo = (u32)t;
          tag_hi = (u32)(t >> 32);
        }
        const u32 fneed = (ln & 1) ? tag_hi : tag_lo;
        f32x4 sa = {0.f, 0.f, 0.f, 0.f};
        f32x16 gr = zero16;
        u32 fv;
        v2u tg;
        SB();
        auto vm = [&](auto N) {
          constexpr int n = N, xi = XA + n, di = n - 8 * FCK;
          if constexpr (xi < 32) xld(IC(xi));
          if constexpr (xi == 32) __builtin_amdgcn_raw_ptr_buffer_load_lds(mkrsrc(wphi, 16384), (LAS u32*)(L + XS_OFF + w * 8192 + 4096), 16, l16, hp * 1024, 0, 0);
          if constexpr (xi == 33) tws = ld32<0>(rs, TLBW ? (ln + 64 * w + (TLBD ? 256 * j : 0)) * TLBW : 0x80000000u, 0);
          if constexpr (xi == XB) {
            if (w == 0) asm volatile("s_waitcnt vmcnt(%0)" ::"n"(XB - XA + 1) : "memory");
            st64<SC1>(v2u{tag_lo, tag_hi}, rs, tvo, tso);
            STAMP(60);
          }
          if constexpr (di >= 0 && di < 16)
            dma1<(di & 3)>(rs, l16, WQKV_T + hp * 98304 + w * 16384 + (di >> 2) * 4096, L + (w >> 1) * 32768 + ((((w & 1) * 4 + (di >> 2)) ^ rot) * 4096));
          if constexpr (di == 16) tg = ld64<SC1>(rs, c16 * CSTR, INIT_OFF);
          SB();
        };
        static_assert(XA + 8 * FCK > 33 && XB < 32 && FCK <= 7 && UB >= FCK + 3 && XA >= 4, "start schedule");
        static_for<11>([&](auto B) {
          constexpr int blk = B;
          if constexpr (blk == UB) { if (w >= 2) unit(); }
          if constexpr (blk == FPK) { fv = peek(rs, fvo, 0); SB(); }
          if constexpr (blk == FCK) {
            STAMP(0);
            if ((__builtin_amdgcn_ballot_w64(fv == fneed) & 0xfffull) != 0xfffull) {
#pragma unroll 1
              for (u32 spins = 0; spins < (1u << 16); ++spins)
                if ((__builtin_amdgcn_ballot_w64(peek(rs, fvo, 0) == fneed) & 0xfffull) == 0xfffull) break;
            }
            asm volatile("" ::: "memory");
            STAMP(61);
          }
          if constexpr (blk < 10) static_for<4>([&](auto I) {
            constexpr int i = I;
            if constexpr (blk > 1) gr = mfma32(xf[4 * blk - 8 + i], xf[4 * blk - 8 + i], gr);
            SB();
            vm(IC(8 * blk + 2 * i));
            if constexpr (blk < 8) *(LAS v4u*)(sw[i & 1] + i * 1024) = xr[4 * blk + i];
            SB();
            if constexpr (blk > 1) { sa = mfma16(selm, xf[4 * blk - 8 + i], sa); asm volatile("" : "+a"(sa)); }
            SB();
            vm(IC(8 * blk + 2 * i + 1));
          });
          if constexpr (blk < 8) static_for<4>([&](auto F) { xf[4 * blk + F] = *(LAS v4u*)(sr[F]); SB(); });
        });
        asm volatile("" ::"v"(tws));
        asm volatile("" ::"v"(cpf));
        {
          const u32 Qc = 4 * (g4 & 1) + (c16 & 3);
          LAS unsigned char* a_ph = L + opqv(XS_OFF + w * 8192 + 4096 + hf * 512 + (c16 >> 2) * 32 + (Qc >> 1) * 8);
#pragma unroll
          for (int s = 0; s < 2; ++s) wph[s] = trfrag(a_ph + s * 256, 128);
        }
        const float sa0 = sa[0];
        nmean = sa0 * (-1.f / 512.f);
        auto sel = [](bool cnd, float a0, float a1) { PIN(a0); PIN(a1); return cnd ? a1 : a0; };
        auto g4f = [&](int i) { return sel(r & 2, sel(r & 1, gr[4 * i], gr[4 * i + 1]), sel(r & 1, gr[4 * i + 2], gr[4 * i + 3])); };
        const float gd = sel(r & 16, sel(r & 8, g4f(0), g4f(1)), sel(r & 8, g4f(2), g4f(3)));
        const bool dl = ((r >> 2) & 1) == hf;
        const float dg = dl ? gd : 0.f;
        rstd = __builtin_amdgcn_rsqf(fmaxf(sum32(dg) * (1.f / 512.f) - nmean * nmean, 0.f) + 1e-5f);
        c = rstd * LOG2E;
#pragma unroll
        for (int s = 0; s < 2; ++s)
#pragma unroll
          for (int m = 0; m < 4; ++m) wph[s][m] = dl ? wph[s][m] : 0u;
        PIN(wph[0]);
        PIN(wph[1]);
        PIN(c);
        PIN(rstd);
        PIN(nmean);
        SB();
        if constexpr (UB > FCK) { if (w >= 2) asm volatile("s_waitcnt vmcnt(9)" ::: "memory"); else DRAIN(); }
        else DRAIN();
        if (w == 0 && !__all(tg.x == tag_lo && tg.y == tag_hi)) {
#pragma unroll 1
          for (u32 spins = 0; spins < (1u << 16); ++spins) {
            asm volatile("" ::: "memory");
            const v2u t = ld64<SC1>(rs, c16 * CSTR, INIT_OFF);
            if (__all(t.x == tag_lo && t.y == tag_hi)) break;
          }
        }
        BAR();
#pragma unroll
        for (int i = 0; i < 2; ++i) { wk[i] = WL[i * 64]; wv[i] = WL[(32 + i) * 64]; }
        if constexpr (UB <= FCK) add1(rs, w >= 2 ? l0 : 0x80000000u, jadd);
        SB();
      } else {
        if constexpr (XMODE == 0) { NAP(PS3); pvx[0] = peek(rs, l0, cnt_off(C_X + tt)); SB(); }
        if constexpr (XMODE == 1) { NAP(PS1); apeek(rs, l0, cnt_off(C_X + tt), pvx[1]); NAP(PS2); apeek(rs, l0, cnt_off(C_X + tt), pvx[2]); SB(); }
        asm volatile("s_waitcnt vmcnt(%0)" ::"n"(XMODE == 1 ? 4 : XMODE == 0 ? 2 : 1) : "memory");
        BAR();
#pragma unroll
        for (int i = 0; i < 2; ++i) { wk[i] = WL[i * 64]; wv[i] = WL[(32 + i) * 64]; }
        SB();
        if constexpr (XMODE == 1) pfinish<2, 1, 0>(rs, l0, cnt_off(C_X + tt), 16, pvx);
        else if constexpr (XMODE == 0) wfinish(rs, l0, cnt_off(C_X + tt), 16, pvx[0]);
        else { NAP(PS3); wwait(rs, l0, cnt_off(C_X + tt), 16); }
        STAMP(sb + 1);
#pragma unroll
        for (int ks = 0; ks < DX; ++ks) { xf[ks] = ldf<SC1>(rs, l16, xb, ks * 1024); SB(); }
      }
      STAMP(sb + 26);
      static_for<32>([&](auto KSI) {
        constexpr int ks = KSI, q0 = layer ? 20 : 8;
        aK = mfma32(wk[ks % 3], xf[ks], aK);
        SB();
        if constexpr (ks + 2 < 32) wk[(ks + 2) % 3] = WL[(ks + 2) * 64];
        if constexpr (layer > 0 && ks + DX < 32) xf[ks + DX] = ldf<SC1>(rs, l16, xb, (ks + DX) * 1024);
        if constexpr (ks >= q0 && ks < q0 + 8) dma1<((ks - q0) & 3)>(rs, l16, wqb + ((ks - q0) >> 2) * 4096, L + 65536 + (((2 * w + ((ks - q0) >> 2)) ^ (layer ? 0 : rot)) * 4096));
        if constexpr (ks >= 28 && ks < 28 + wok) wold(IC(ks - 28));
        if constexpr (layer == 0 && ks < 6) cp[ks >> 1][2 * (ks & 1)] = __builtin_bit_cast(float, ld32<SC1>(rs, ln * 4 + (ks & 1) * 512 + (ks >> 1) * 1024, CS_QKV + hp * 3072));
        if constexpr (ks == 23) { SB(); aV = mfma32f_pad<5>(csq[1], nmean, aV); }
        SB();
        aV = mfma32(wv[ks % 3], xf[ks], aV);
        SB();
        if constexpr (ks + 2 < 32) wv[(ks + 2) % 3] = WL[(32 + ks + 2) * 64];
        if constexpr (layer == 0 && ks < 6) cp[ks >> 1][2 * (ks & 1) + 1] = __builtin_bit_cast(float, ld32<SC1>(rs, ln * 4 + (ks & 1) * 512 + 256 + (ks >> 1) * 1024, CS_QKV + hp * 3072));
        if constexpr (layer == 0 && (ks == 18 || ks == 22)) res[(ks - 18) >> 2] = ld128<0>(rsx, r * 1024 + hf * 16 + ((ks - 18) >> 2) * 32, tte * 32768 + n0 * 2);
        if constexpr (layer == 0 && ks == 19) {
#pragma unroll
          for (int t = 0; t < 3; ++t) { csq[t] = (cp[t][0] + cp[t][1]) + (cp[t][2] + cp[t][3]); PIN(csq[t]); }
        }
        if constexpr (layer == 0 && ks == 19 && UB > FCK) add1(rs, w >= 2 ? l0 : 0x80000000u, jadd);
        if constexpr (layer == 0 && ks == 31 && JPK < 0) jv = peek(rs, w != 0 ? 0x80000000u : jvo, 0);
        if constexpr (layer > 0) {
          if constexpr (ks < 4) sp[ks] = __builtin_bit_cast(f32x4, ld128<SC1>(rs, l16 + ks * 1024, stb));
          if constexpr (ks == 1 && XMODE == 1) pretire(pvx);
          if constexpr (ks == 17) stats_fin(sp, nmean, rstd);
          if constexpr (ks == 18) { c = rstd * LOG2E; PIN(c); }
        }
        if constexpr (ks == 20) { SB(); aK = mfma32f_pad<5>(csq[0], nmean, aK); }
        SB();
      });
      STAMP(sb + 2);
      const u32 w1b = opq(W1_T + fb * 131072);
      auto w1dma = [&](auto I) {
        constexpr int i = I;
        if constexpr (i < 16) dma1<i>(rs, l16, w1b + w * 16384, L + w * 16384);
        else if constexpr (i < 24) dma1<i - 16>(rs, l16, w1b + 98304 + w * 8192, L + 98304 + w * 8192);
        else dma1<i - 24>(rs, l16, w1b + 65536 + w * 8192, L + 65536 + w * 8192);
      };
      auto pf = [&](auto N) {
        constexpr int n = N + wok;
        if constexpr (n < 16) wold(IC(n));
        else w1dma(IC(n - 16 + (layer && n >= 32 ? 8 : 0)));
        SB();
      };
      {
        const v4u k0 = pack8(aK, 0), k1 = pack8(aK, 1);
        f32x16 p = mfma32(wph[0], k0, zero16);
        p = mfma32(wph[1], k1, p);
        SB();
#pragma unroll
        for (int i = 0; i < 16; ++i) { aV[i] *= rstd; PIN(aV[i]); }
        SB();
        *(LAS v4u*)(a_v) = pack8(aV, 0);
        *(LAS v4u*)(a_v + 16) = pack8(aV, 1);
        SB();
        u32 o[16];
        dual_softmax(p, c, o, [&](auto I) { if constexpr (layer == 0 && I == JPK) jv = peek(rs, w != 0 ? 0x80000000u : jvo, 0); });
#pragma unroll
        for (int i = 0; i < 4; ++i) *(LAS v4u*)(a_pk + i * 16) = v4u{o[4 * i], o[4 * i + 1], o[4 * i + 2], o[4 * i + 3]};
      }
      SB();
      aQ = mfma32f(csq[2], nmean, zero16);
      STAMP(sb + 3);
      asm volatile("s_waitcnt vmcnt(%0)" ::"n"(wok) : "memory");
      if constexpr (layer == 0) {
        const u32 need = ln == 0 ? 36u : ln == 1 ? 16u : 0u;
        if (w == 0 && (__builtin_amdgcn_ballot_w64(jv >= need) & 7ull) != 7ull) poll3(rs, jvo, need);
      }
      BAR();
      const u32 pbase = KVP + layer * KVP_SZ + (4 * hp + w) * KVP_HS;
      v4u wq[4];
      auto qfrag = [&](int ks) { return WQ[ks * 64]; };
      {
        f32x4 kv = {0.f, 0.f, 0.f, 0.f}, zz = {0.f, 0.f, 0.f, 0.f};
        const v4u ones = {ONES2, ONES2, ONES2, ONES2};
        v2u a0[4], a1[4], b0[4], b1[4];
#pragma unroll
        for (int ks = 0; ks < 4; ++ks) {
          a0[ks] = trread(a_pa + (32 * ks) * PK_STR); a1[ks] = trread(a_pa + (32 * ks + 4) * PK_STR);
          b0[ks] = trread(a_pb + (32 * ks) * VV_STR); b1[ks] = trread(a_pb + (32 * ks + 4) * VV_STR);
          SB();
        }
#pragma unroll
        for (int ks = 0; ks < 4; ++ks) {
          const v4u A = {a0[ks].x, a0[ks].y, a1[ks].x, a1[ks].y}, B = {b0[ks].x, b0[ks].y, b1[ks].x, b1[ks].y};
          kv = mfma16(A, B, kv);
          zz = mfma16(ones, A, zz);
          asm volatile("" : "+a"(zz));
          SB();
        }
#pragma unroll
        for (int ks = 0; ks < 4; ++ks) wq[ks] = qfrag(ks);
        SB();
        st64<SC1>(v2u{pk(kv[0], kv[1]), pk(kv[2], kv[3])}, rs, o_kv, pbase + rt * 512);
        const float z0 = zz[0];
        __builtin_amdgcn_raw_buffer_store_b32(__builtin_bit_cast(u32, z0), rs, o_z, pbase + 8192 + rt * 128, SC1);
      }
      SB();
      STAMP(sb + 4);
      auto qstep = [&](auto I) {
        constexpr int i = I;
        aQ = mfma32(wq[i & 3], xf[i], aQ);
        SB();
        if constexpr (i + 4 < 32) wq[i & 3] = qfrag(i + 4);
      };
      LAS unsigned char *a_kz, *a_kw, *a_kr;
      u32 o_st, o_zl;
      v4u sel;
      static_for<18>([&](auto I) {
        qstep(I);
        if constexpr ((I * NQ1 + 17) / 18 != ((I + 1) * NQ1 + 17) / 18) pf(IC(s_q1 + (I * NQ1 + 17) / 18));
        const u32 l2 = opqv(l16) >> 4, g2 = l2 >> 4, c2 = l2 & 15;
        if constexpr (I == 1) o_zl = opqv(g2 * 128 + c2 * 4);
        if constexpr (I == 3) a_kz = L + opqv(KF_OFF + w * 544 + (c2 >> 3) * 272 + 256 + (c2 & 7) * 2);
        if constexpr (I == 5) a_kw = L + opqv(KF_OFF + w * 544 + (g2 >> 1) * 272 + (g2 & 1) * 8 + rho32(c2 & 7, c2 >> 3) * 16);
        if constexpr (I == 7) a_kr = L + opqv(KF_OFF + (l2 >> 5) * 272 + ((l2 & 31) < 16 ? (l2 & 31) : 16) * 16);
        if constexpr (I == 9) o_st = opqv(l2 >= 32 ? 0x80000000u : c2 * 16 + g2 * 8);
        if constexpr (I == 11 || I == 13 || I == 15 || I == 17) {
          constexpr int m = (I - 11) / 2;
          sel[m] = opqv((c2 >> 1) == 4 * (g2 & 1) + m ? 0x3f80u << (16 * (c2 & 1)) : 0u);
        }
        SB();
      });
      const u32 ckv = cnt_off(C_KV + layer * 64 + hp * 4 + w);
      wsignal<NQ1>(rs, l0, ckv);
      STAMP(sb + 5);
      u32 pvk[3];
      static_for<14>([&](auto I) {
        qstep(IC(18 + I));
        if constexpr (I == KVPK1 && KMODE == 1) apeek(rs, l0, ckv, pvk[0]);
        if constexpr (I == KVPK1 && KMODE == 0) pvk[0] = peek(rs, l0, ckv);
        if constexpr ((I * NQ2 + 13) / 14 != ((I + 1) * NQ2 + 13) / 14) pf(IC(s_q2 + (I * NQ2 + 13) / 14));
        SB();
      });
      if constexpr (KMODE == 1) apeek(rs, l0, ckv, pvk[1]);
      SB();
      const v4u q0 = pack8(aQ, 0), q1 = pack8(aQ, 1);
      f32x16 pq = mfma32(wph[0], q0, zero16);
      pq = mfma32(wph[1], q1, pq);
      SB();
      v4u phq[4];
      v4u v[8];
      f32x4 z;
      {
        u32 o[16];
        dual_softmax(pq, c, o, [&](auto I) {
          constexpr int i = I;
          if constexpr (i == 0 && KMODE == 1) apeek(rs, l0, ckv, pvk[2]);
          if constexpr (i == KVCHK) {
            STAMP(sb + 6);
            if constexpr (KMODE == 1) pfinish<2 + NQ2 - (KVPK1 * NQ2 + 13) / 14, 1, 0>(rs, l0, ckv, 16, pvk);
            else wfinish(rs, l0, ckv, 16, pvk[0]);
            STAMP(sb + 7);
#pragma unroll
            for (int k = 0; k < 4; ++k) z[k] = __builtin_bit_cast(float, ld32<SC1>(rs, o_zl + k * 512, pbase + 8192));
            SB();
          }
          static_for<8>([&](auto K) {
            constexpr int k = K, at = KVCHK + (k + 1) / KVLD;
            if constexpr ((at < 9 ? at : 9) == i) { v[k] = ldf<SC1>(rs, l16, pbase, k * 1024); SB(); }
          });
        });
#pragma unroll
        for (int m = 0; m < 4; ++m) {
          auto sw = __builtin_amdgcn_permlane32_swap(o[m], o[4 + m], false, false);
          phq[0][m] = sw[0];
          phq[2][m] = sw[1];
          auto sx = __builtin_amdgcn_permlane32_swap(o[8 + m], o[12 + m], false, false);
          phq[1][m] = sx[0];
          phq[3][m] = sx[1];
        }
#pragma unroll
        for (int m = 0; m < 4; ++m) PIN(phq[m]);
      }
      SB();
      {
        const float zs = sum32(sum16((z[0] + z[1]) + (z[2] + z[3])));
        *(LAS unsigned short*)(a_kz) = (unsigned short)pk(zs, zs);
        if constexpr (KMODE == 1) pretire(pvk);
        SB();
        f32x4 sv = {0.f, 0.f, 0.f, 0.f};
#pragma unroll
        for (int i = 0; i < 8; ++i) { sv = mfma16(sel, v[i], sv); SB(); }
        *(LAS v2u*)(a_kw) = v2u{pk(sv[0], sv[1]), pk(sv[2], sv[3])};
      }
      BAR();
      STAMP(sb + 8);
      {
        v4u kvf[4];
#pragma unroll
        for (int i = 0; i < 4; ++i) kvf[i] = *(LAS v4u*)(a_kr + i * 544);
        SB();
        f32x16 nd[2];
        nd[0] = mfma32(kvf[0], phq[0], zero16);
        nd[0] = mfma32(kvf[1], phq[1], nd[0]);
        nd[1] = mfma32(kvf[2], phq[2], zero16);
        nd[1] = mfma32(kvf[3], phq[3], nd[1]);
        SB();
#pragma unroll
        for (int h = 0; h < 2; ++h) {
          const float inv = __builtin_amdgcn_rcpf(nd[h][8] + 1e-6f);
          v4u ob;
#pragma unroll
          for (int m = 0; m < 4; ++m) ob[m] = pk(nd[h][2 * m] * inv, nd[h][2 * m + 1] * inv);
          st128<SC1>(ob, rs, l16 + h * 1024, OB + layer * XT_SZ + tt * 32768 + (2 * hp) * 1024);
          SB();
        }
      }
      STAMP(sb + 9);
      static_for<ODR>([&](auto I) { pf(IC(s_od + I)); });
      wsignal<ODR>(rs, l0, cnt_off(C_O + layer * 128 + (tt >> 1) * 4 + (hp >> 2)));
      STAMP(sb + 10);
      const u32 co = cnt_off(C_O + layer * 128 + rt2 * 4 + w);
      u32 pvo[3];
      BAR();
      if constexpr (OMODE == 1) {
        apeek(rs, l0, co, pvo[0]);
        NAP(PS2);
        apeek(rs, l0, co, pvo[1]);
        NAP(PS2);
        apeek(rs, l0, co, pvo[2]);
        static_for<n_op>([&](auto I) { pf(IC(s_op + I)); });
        pfinish<2 + n_op, 1 + n_op, n_op>(rs, l0, co, 8, pvo);
      } else {
        NAP(PS4);
        pvo[0] = peek(rs, l0, co);
        SB();
        static_for<n_op>([&](auto I) { pf(IC(s_op + I)); });
        wfinish(rs, l0, co, 8, pvo[0]);
      }
      STAMP(sb + 11);
      const u32 ab = OB + layer * XT_SZ + (8 * w) * 1024;
      v4u af[DXV];
      splitk_phase<FrO, DXV, 0>(rs, wo, af, ab + tte * 32768, ab + (tte ^ 1) * 32768, L, w, l16, 1.f, res, [&]() {}, [&](auto I) {
        if constexpr (I == 1 && OMODE == 1) pretire(pvo);
        if constexpr (I >= 4 && s_p2 + I - 4 < s_xd) pf(IC(s_p2 + I - 4));
      });
      STAMP(sb + 12);
      resid_store(rs, res, l16, o_st, XT + (2 * layer + 1) * XT_SZ + tte * 32768 + (n0 >> 3) * 512, ST + (2 * layer + 1) * ST_SZ + tte * 4096 + (n0 >> 5) * 256);
      STAMP(sb + 13);
      static_for<XDR>([&](auto I) { pf(IC(s_xd + I)); });
      wsignal<XDR>(rs, l0, cnt_off(C_X1 + layer * 64 + tte));
      STAMP(sb + 14);
    }
    u32 pv1;
    const u32 lh = opqv(opqv(lane) < nlh ? lane * CSTR : 0x80000000u);
    v4u af[WINH];
    const u32 ch0 = cnt_off(C_H + layer * 512 + rt2 * 4 + w);
    const u32 sh0 = cnt_off(C_H + layer * 512 + (tt >> 1) * 4 + (2 * fb) / 7);
    const u32 hown = HB + layer * HB_SZ + (7 * w) * 1024 + tte * 114688, hoth = HB + layer * HB_SZ + (7 * w) * 1024 + (tte ^ 1) * 114688;
    {
      const u32 ln = opqv(lane), l16 = ln * 16, r = ln & 31, hf = ln >> 5;
      auto w2ld = [&](auto I) {
        constexpr int i = I, k = (i >> 1) % 7;
        w2f[i >> 1][i & 1] = ld128<SC1>(rs, l16 + (k & 1) * 2048, wb + (i / 14) * 57344 + (k >> 1) * 4096 + ((i & 1) ? 1024 - wsw : wsw));
      };
      auto hld = [&](int i) { if (i < HPRE) { af[i] = ld_fr<FrH>(rs, l16, hown, hoth, i); SB(); } };
      if (p3on) {
        static_for<3>([&](auto J) {
          if constexpr (XMODE == 1) apeek(rs, l0, cnt_off(C_X1 + layer * 64 + tt), pvx[J]);
          if constexpr (XMODE == 0 && J == (layer ? 0 : 1)) { if constexpr (layer) NAP(PS3 + 1); pvx[0] = peek(rs, l0, cnt_off(C_X1 + layer * 64 + tt)); }
          SB();
          if constexpr (layer == 0) static_for<W2D / 3>([&](auto I) { w2ld(IC(J * (W2D / 3) + I)); SB(); });
          else if constexpr (J < 2 && XMODE == 1) NAP(PS2);
        });
        constexpr int DX = DXV;
        v4u xf[32];
        const u32 xb = XT + (2 * layer + 1) * XT_SZ + tt * 32768;
        f32x4 sp[4];
        const u32 stb = ST + (2 * layer + 1) * ST_SZ + tt * 4096;
        LAS v4u* WL = (LAS v4u*)(L + l16);
        LAS v4u* WH = (LAS v4u*)(L + opqv(l16 + 65536));
        SB();
        asm volatile("s_waitcnt vmcnt(%0)" ::"n"((layer == 0 ? W2D : 0) + (XMODE == 1 ? 4 : XMODE == 0 ? 2 : 1)) : "memory");
        BAR();
        v4u wa[3], wc[3], wq[4];
#pragma unroll
        for (int i = 0; i < 2; ++i) { wa[i] = WL[i * 64]; wc[i] = WL[(32 + i) * 64]; }
        SB();
        STAMP(sb + 15);
        if constexpr (XMODE == 0) wfinish(rs, l0, cnt_off(C_X1 + layer * 64 + tt), 16, pvx[0]);
        else if constexpr (XMODE == 2) { if constexpr (layer) NAP(PS3); wwait(rs, l0, cnt_off(C_X1 + layer * 64 + tt), 16); }
        else pfinish<2 + (layer ? 0 : W2D), 1 + (layer ? 0 : 2 * W2D / 3), (layer ? 0 : W2D / 3)>(rs, l0, cnt_off(C_X1 + layer * 64 + tt), 16, pvx);
        STAMP(sb + 16);
#pragma unroll
        for (int ks = 0; ks < DX; ++ks) { xf[ks] = ldf<SC1>(rs, l16, xb, ks * 1024); SB(); }
        f32x16 acc[4];
        acc[0] = zero16;
        acc[1] = zero16;
        float nmean, nc, cp[4][4];
        SB();
        static_for<32>([&](auto KSI) {
          constexpr int ks = KSI;
          acc[0] = mfma32(wa[ks % 3], xf[ks], acc[0]);
          SB();
          if constexpr (ks + 2 < 32) wa[(ks + 2) % 3] = WL[(ks + 2) * 64];
          else wq[2 * (ks - 30)] = WH[(2 * (ks - 30)) * 64];
          if constexpr (ks + DX < 32) xf[ks + DX] = ldf<SC1>(rs, l16, xb, (ks + DX) * 1024);
          if constexpr (layer == 0 && ks >= 20) w2ld(IC(W2D + ks - 20));
          if constexpr (ks == 24) { SB(); acc[1] = mfma32f_pad<5>(cs1[1], nmean, acc[1]); }
          SB();
          acc[1] = mfma32(wc[ks % 3], xf[ks], acc[1]);
          SB();
          if constexpr (ks + 2 < 32) wc[(ks + 2) % 3] = WL[(32 + ks + 2) * 64];
          else wq[2 * (ks - 30) + 1] = WH[(2 * (ks - 30) + 1) * 64];
          if constexpr (ks < 4) sp[ks] = __builtin_bit_cast(f32x4, ld128<SC1>(rs, l16 + ks * 1024, stb));
          if constexpr (layer == 0 && ks >= 4 && ks < 8) {
#pragma unroll
            for (int i = 0; i < 4; ++i) cp[ks - 4][i] = __builtin_bit_cast(float, ld32<SC1>(rs, ln * 4 + i * 256 + (ks - 4) * 1024, CS_W1 + fb * 4096));
          }
          if constexpr (ks == 1 && XMODE == 1) pretire(pvx);
          if constexpr (ks == 17) stats_fin(sp, nmean, nc);
          if constexpr (ks == 18) { nc *= -LOG2E; PIN(nc); }
          if constexpr (layer == 0 && ks == 20) {
#pragma unroll
            for (int t = 0; t < 4; ++t) cs1[t] = (cp[t][0] + cp[t][1]) + (cp[t][2] + cp[t][3]);
          }
          if constexpr (ks == 21) { SB(); acc[0] = mfma32f_pad<5>(cs1[0], nmean, acc[0]); }
          if constexpr (ks == 26) acc[2] = mfma32f(cs1[2], nmean, zero16);
          if constexpr (ks == 28) acc[3] = mfma32f(cs1[3], nmean, zero16);
          SB();
        });
        STAMP(sb + 17);
        const u32 hb = HB + layer * HB_SZ + tt * 114688 + fb * 2048;
        float tv[64], ev[64];
        v4u ho[8];
        u32 pv0;
        auto stage = [&](auto V, auto ST_) {
          constexpr int v = V, st = ST_;
          if constexpr (st == 0) { tv[v] = acc[v >> 4][v & 15] * nc; PIN(tv[v]); }
          else if constexpr (st == 1) { ev[v] = __builtin_amdgcn_exp2f(tv[v]); PIN(ev[v]); }
          else if constexpr (st == 2) { ev[v] = 1.f + ev[v]; PIN(ev[v]); }
          else if constexpr (st == 3) { ev[v] = __builtin_amdgcn_rcpf(ev[v]); PIN(ev[v]); }
          else if constexpr (st == 4) { tv[v] = tv[v] * ev[v]; PIN(tv[v]); }
          else if constexpr (v & 1) {
            u32 hh = pk(tv[v - 1], tv[v]);
            PIN(hh);
            ho[v >> 3][(v >> 1) & 3] = hh;
            if constexpr ((v & 7) == 7) st128<SC1>(ho[v >> 3], rs, l16 + ((v >> 3) & 1) * 1024, hb + (v >> 4) * 28672);
          }
        };
        static_for<64>([&](auto GI) {
          constexpr int gi = GI, t = 2 + (gi >> 5), ks = gi & 31;
          acc[t] = mfma32(wq[gi & 3], xf[ks], acc[t]);
          SB();
          if constexpr (gi + 4 < 64) wq[gi & 3] = WH[(gi + 4) * 64];
          if constexpr (layer == 0 && p3_w2gap(gi)) w2ld(IC(W2D + 12 + (gi * W2B) / 8));
          SB();
          static_for<48>([&](auto V) {
            constexpr int d = gi - p3_g0(V);
            if constexpr (d >= 0 && d < 6) stage(V, IC(d));
          });
          if constexpr (gi == HS0) wsignal<p3_younger(layer, 0, HS0)>(rs, lh, sh0);
          if constexpr (gi == HS1) wsignal<p3_younger(layer, 1, HS1)>(rs, lh, sh0 + 128 * CSTR);
          if constexpr (gi == PK0) pv0 = peek(rs, l0, ch0);
          SB();
        });
        STAMP(sb + 18);
        constexpr int W2E = 56 - W2D - 12 - 8 * W2B;
        static_assert(W2E >= 0, "w2 schedule");
        {
          f32x2 t2[8], e2[8];
#pragma unroll
          for (int m = 0; m < 8; ++m) { t2[m] = f32x2{acc[3][2 * m], acc[3][2 * m + 1]} * nc; PIN(t2[m]); }
          SB();
#pragma unroll
          for (int m = 0; m < 8; ++m) { e2[m] = f32x2{__builtin_amdgcn_exp2f(t2[m].x), __builtin_amdgcn_exp2f(t2[m].y)}; PIN(e2[m]); }
          SB();
          if constexpr (HPRE > 0) wfinish(rs, l0, ch0, 8, pv0);
          STAMP(sb + 21);
          hld(0);
#pragma unroll
          for (int m = 0; m < 8; ++m) { e2[m] = e2[m] + 1.f; PIN(e2[m]); }
          SB();
#pragma unroll
          for (int m = 0; m < 8; ++m) { e2[m] = f32x2{__builtin_amdgcn_rcpf(e2[m].x), __builtin_amdgcn_rcpf(e2[m].y)}; PIN(e2[m]); if (m == 1 || m == 5) { SB(); hld(1 + m / 4); } }
          SB();
#pragma unroll
          for (int m = 0; m < 8; ++m) { t2[m] = t2[m] * e2[m]; PIN(t2[m]); }
          SB();
#pragma unroll
          for (int i = 3; i < HPRE; ++i) hld(i);
#pragma unroll
          for (int s = 0; s < 2; ++s)
            st128<SC1>(v4u{pk(t2[4 * s].x, t2[4 * s].y), pk(t2[4 * s + 1].x, t2[4 * s + 1].y), pk(t2[4 * s + 2].x, t2[4 * s + 2].y), pk(t2[4 * s + 3].x, t2[4 * s + 3].y)},
                       rs, l16 + s * 1024, hb + 3 * 28672);
        }
        SB();
        pv1 = peek(rs, l0, ch0 + 128 * CSTR);
        wsignal<p3_younger(layer, 2, 64) + HPRE + 3>(rs, lh, sh0 + 256 * CSTR);
        if constexpr (layer == 0) static_for<W2E>([&](auto E) { w2ld(IC(W2D + 12 + 8 * W2B + E)); });
        SB();
        STAMP(sb + 19);
        if constexpr (HPRE == 0) wfinish(rs, l0, ch0, 8, pv0);
      } else {
        if constexpr (layer == 0) static_for<56>([&](auto E) { w2ld(E); });
        DRAIN();
        BAR();
        wwait(rs, l0, ch0, 8);
#pragma unroll
        for (int i = 0; i < HPRE; ++i) hld(i);
        pv1 = 0;
      }
    }
    {
      const u32 ln = opqv(lane), l16 = ln * 16, r = ln & 31, hf = ln >> 5;
      const u32 wqb = opq(WQKV_T + hp * 98304 + w * 16384);
      u32 pv2, pv3;
      splitk_phase<FrH, WINH, HPRE>(rs, w2f, af, hown, hoth, L, w, l16, -LN2, res,
        [&]() {
          if constexpr (T3SIG < 0) wsignal<2 + WINH - HPRE>(rs, lh, sh0 + 384 * CSTR);
          STAMP(sb + 22); wfinish(rs, l0, ch0 + 128 * CSTR, 8, pv1); STAMP(sb + 20); },
        [&](auto I) {
          constexpr int i = I, g = i - 42;
          if constexpr (i == T3SIG) wsignal<2 + WINH - HPRE + T3SIG + (PK2 < T3SIG)>(rs, lh, sh0 + 384 * CSTR);
          if constexpr (i == PK2) pv2 = peek(rs, l0, ch0 + 256 * CSTR);
          if constexpr (i == PK3) pv3 = peek(rs, l0, ch0 + 384 * CSTR);
          if constexpr (i == 28 - WINH) wfinish(rs, l0, ch0 + 256 * CSTR, 8, pv2);
          if constexpr (i == T3CHK) { STAMP(sb + 27); wfinish(rs, l0, ch0 + 384 * CSTR, 8, pv3); STAMP(sb + 28); }
          if constexpr (KWPOS == 0) {
            if constexpr (layer == 0 && g >= 10 && g <= 14) dma1<g - 10>(rs, l16, wqb, L + w * 16384);
            if constexpr (layer == 0 && g >= 19 && g <= 21) dma1<g - 14>(rs, l16, wqb, L + w * 16384);
          } else {
            if constexpr (layer == 0 && g >= 10 && g < 26) dma1<g - 10>(rs, l16, wqb, L + w * 16384);
          }
        });
      STAMP(sb + 23);
      if constexpr (layer == 0) {
        resid_store(rs, res, l16, hf ? 0x80000000u : (r & 15) * 16 + (r >> 4) * 8, XT + 2 * XT_SZ + tte * 32768 + (n0 >> 3) * 512, ST + 2 * ST_SZ + tte * 4096 + (n0 >> 5) * 256);
        STAMP(sb + 24);
        if constexpr (KWPOS == 0) dma_range<8, 16>(rs, l16, wqb, L + w * 16384);
        wsignal<KWPOS == 0 ? 8 : 0>(rs, l0, cnt_off(C_X + tte));
        STAMP(sb + 25);
      } else {
        const rsrc_t rso = mkrsrc(out, S * D * 2);
#pragma unroll
        for (int s = 0; s < 2; ++s) st128<OAUX>(res[s], rso, r * 1024 + hf * 16 + s * 32, tte * 32768 + n0 * 2);
        if (b == 255 && tid < 16) st64<SC1>(v2u{tag_lo, tag_hi}, rs, tid * 8, EPOCH_OFF);
        STAMP(sb + 24);
      }
    }
  });
  asm volatile(".Lkcode1:");
}

extern "C" void kernel_launch(void* const* d_in, const int* in_sizes, int n_in, void* d_out, int out_size, void* d_ws,
                              size_t ws_size, hipStream_t stream) {
  (void)hipFuncSetAttribute((const void*)k_main, hipFuncAttributeMaxDynamicSharedMemorySize, LDS_BYTES);
  k_main<<<256, 256, LDS_BYTES, stream>>>((const uint16_t*)d_in[0], (const uint16_t*)d_in[1], (const uint16_t*)d_in[2],
                                          (const uint16_t*)d_in[3], (const uint16_t*)d_in[5], (const uint16_t*)d_in[6],
                                          (unsigned char*)d_ws, (unsigned char*)d_out);
}
```

```cpp
#include <hip/hip_runtime.h>
#include <hip/hip_bf16.h>
#include <stdint.h>
#include <utility>

typedef unsigned int u32;
typedef u32 v4u __attribute__((ext_vector_type(4)));
typedef u32 v2u __attribute__((ext_vector_type(2)));
typedef short s16x4 __attribute__((ext_vector_type(4)));
typedef __bf16 bf16x8 __attribute__((ext_vector_type(8)));
typedef __bf16 bf16x2 __attribute__((ext_vector_type(2)));
typedef float f32x2 __attribute__((ext_vector_type(2)));
typedef float f32x4 __attribute__((ext_vector_type(4)));
typedef float f32x16 __attribute__((ext_vector_type(16)));
#define LAS __attribute__((address_space(3)))
#define GAS __attribute__((address_space(1)))
#define DI __device__ __forceinline__
typedef __amdgpu_buffer_rsrc_t rsrc_t;

#define DXV 12
#define WINH 14
#define HS0 36
#define HS1 56
#define PK0 59
#define HPRE 0
#define PK2 0
#define T1A 0
#define T1B 7
#define PS5 5
#define PK3 9
#define PK2B 6
#define PK3B 16
#define T2CHK 14
#define T3CHK 24
#define KVCHK 0
#define KVLD 2
#define ODR 8
#define OAUX 0
#define PS1 3
#define PS2 4
#define XMODE 3
#define KMODE 2
#define NQ1 11
#define NQ2 10
#define OPL0 4
#define OPL1 4
#define XDR 4
#define OMODE 2
#define PS6 3
#define PS4 2
#define KWPOS 1
#define PS3 2
#define WOK 2
#define CPF 1
#define CPW 1
#define TLBD 0
#define PRB 1
#define NFLIP 0
#define ROT2 0
#define QS 11
#define ROT 1
#define XBAR 1
#define JPK 0
#define XA 8
#define XB 20
#define UB 10
#define FPK 4
#define FCK 6
#define KVPK2 14
#define KVPK1 4
#define TLBW 262144
#define W2D 12
#define W2B 4
constexpr int S = 2048, D = 512;
constexpr u32 CTRL = 0;
constexpr u32 WQKV_T = 262144;
constexpr u32 WPHI_T = WQKV_T + 16 * 98304;
constexpr u32 WO_T = WPHI_T + 32768;
constexpr u32 W1_T = WO_T + 524288;
constexpr u32 W2_T = W1_T + 14 * 131072;
constexpr u32 CS_QKV = W2_T + 8 * 4 * 28 * 2048;
constexpr u32 CS_W1 = CS_QKV + 65536;
constexpr u32 MB2 = 2097152;
constexpr u32 XT = 3 * MB2;
constexpr u32 OB = 6 * MB2;
constexpr u32 HB = 8 * MB2;
constexpr u32 ST = 16 * MB2;
constexpr u32 ST_SZ = 262144;
constexpr u32 ZP = ST + 4 * ST_SZ;
constexpr u32 KVP = 17 * MB2;
constexpr u32 WS_END = 18 * MB2;
static_assert(CS_W1 + 65536 <= XT, "workspace map");
constexpr u32 blk(u32 off) { return (off >> 13) * 16384 + (off & 8191); }
constexpr u32 CSTR = 128;
enum { C_H = 0, C_O = 1024, C_X1 = 1280, C_X = 1408, C_J = 1440, C_V = 1504, C_TMO = 1520, C_KV = 1536, NCNT = 1792 };
constexpr u32 INIT_OFF = CTRL + NCNT * CSTR, EPOCH_OFF = INIT_OFF + 16 * CSTR, FLAG_OFF = EPOCH_OFF + CSTR;
static_assert(FLAG_OFF + 192 * CSTR <= WQKV_T, "control area");

#define STAMP(i)
constexpr float LOG2E = 1.4426950408889634f;
constexpr float LN2 = 0.6931471805599453f;

DI u32 pk(float a, float b) {
  f32x2 v = {a, b};
  bf16x2 r = __builtin_convertvector(v, bf16x2);
  return __builtin_bit_cast(u32, r);
}
DI float bflo(u32 u) { return __builtin_bit_cast(float, u << 16); }
DI float bfhi(u32 u) { return __builtin_bit_cast(float, u & 0xffff0000u); }
DI f32x16 mfma32(v4u a, v4u b, f32x16 c) {
  f32x16 d = __builtin_amdgcn_mfma_f32_32x32x16_bf16(__builtin_bit_cast(bf16x8, a), __builtin_bit_cast(bf16x8, b), c, 0, 0, 0);
  asm volatile("" : "+a"(d));
  return d;
}
DI f32x4 mfma16(v4u a, v4u b, f32x4 c) {
  return __builtin_amdgcn_mfma_f32_16x16x32_bf16(__builtin_bit_cast(bf16x8, a), __builtin_bit_cast(bf16x8, b), c, 0, 0, 0);
}
DI f32x16 mfma32f(float a, float b, f32x16 c) {
  f32x16 d = __builtin_amdgcn_mfma_f32_32x32x2f32(a, b, c, 0, 0, 0);
  asm volatile("" : "+a"(d));
  return d;
}
template <int N> DI f32x16 mfma32f_pad(float a, float b, f32x16 c) {
  asm volatile("s_nop %1" : "+a"(c) : "n"(N));
  return mfma32f(a, b, c);
}
DI float dot2(u32 a, u32 b, float c) {
  return __builtin_amdgcn_fdot2_f32_bf16(__builtin_bit_cast(bf16x2, a), __builtin_bit_cast(bf16x2, b), c, false);
}
template <int AUX> DI v4u ld128(rsrc_t rs, u32 voff, u32 soff) { return __builtin_amdgcn_raw_buffer_load_b128(rs, voff, soff, AUX); }
template <int AUX> DI v2u ld64(rsrc_t rs, u32 voff, u32 soff) { return __builtin_amdgcn_raw_buffer_load_b64(rs, voff, soff, AUX); }
template <int AUX> DI u32 ld32(rsrc_t rs, u32 voff, u32 soff) { return __builtin_amdgcn_raw_buffer_load_b32(rs, voff, soff, AUX); }
template <int AUX> DI void st128(v4u v, rsrc_t rs, u32 voff, u32 soff) {
  __builtin_amdgcn_raw_buffer_store_b128(v, rs, voff, soff, AUX);
  asm volatile("s_nop 1" ::"v"(v) : "memory");
}
template <int AUX> DI void st64(v2u v, rsrc_t rs, u32 voff, u32 soff) { __builtin_amdgcn_raw_buffer_store_b64(v, rs, voff, soff, AUX); }
DI rsrc_t mkrsrc(const void* p, u32 bytes) { return __builtin_amdgcn_make_buffer_rsrc((void*)p, 0, (int)bytes, 0x00020000); }
DI float sum32(float v) {
  u32 u = __builtin_bit_cast(u32, v);
  auto r = __builtin_amdgcn_permlane32_swap(u, u, false, false);
  const u32 r0 = r[0], r1 = r[1];
  return __builtin_bit_cast(float, r0) + __builtin_bit_cast(float, r1);
}
DI float sum16(float v) {
  u32 u = __builtin_bit_cast(u32, v);
  auto r = __builtin_amdgcn_permlane16_swap(u, u, false, false);
  const u32 r0 = r[0], r1 = r[1];
  return __builtin_bit_cast(float, r0) + __builtin_bit_cast(float, r1);
}
DI float mix16(float a, float b) {
  auto r = __builtin_amdgcn_permlane16_swap(__builtin_bit_cast(u32, a), __builtin_bit_cast(u32, b), false, false);
  const u32 r0 = r[0], r1 = r[1];
  return __builtin_bit_cast(float, r0) + __builtin_bit_cast(float, r1);
}
DI v2u trread(LAS unsigned char* __restrict__ p) {
  return __builtin_bit_cast(v2u, __builtin_amdgcn_ds_read_tr16_b64_v4i16((LAS s16x4*)p));
}
DI int rho32(int reg, int hf) { return (reg & 3) + 8 * (reg >> 2) + 4 * hf; }
constexpr u32 ONES2 = 0x3f803f80u;

DI v4u pack8(const f32x16& a, int s) {
  v4u r;
#pragma unroll
  for (int m = 0; m < 4; ++m) r[m] = pk(a[8 * s + 2 * m], a[8 * s + 2 * m + 1]);
  return r;
}

#define SB() __builtin_amdgcn_sched_barrier(0)
#define PIN(x) asm volatile("" : "+v"(x))
#define BAR() asm volatile("s_waitcnt lgkmcnt(0)\n\ts_barrier" ::: "memory")
#define DRAIN() asm volatile("s_waitcnt vmcnt(0)" ::: "memory")
#define RLX __ATOMIC_RELAXED, __HIP_MEMORY_SCOPE_AGENT
constexpr int SC1 = 16;

DI u32 opq(u32 x) { asm volatile("" : "+s"(x)); return x; }
DI u32 opqv(u32 x) { asm volatile("" : "+v"(x)); return x; }
DI v4u ldr(rsrc_t rs, u32 vo, u32 sbase, u32 rx, int ks) { return ld128<16>(rs, vo + (ks & 3) * 1024, sbase + blk((ks >> 2) * 4096)); }
template <int AUX> DI v4u ldf(rsrc_t rs, u32 vo, u32 sbase, u32 off) { return ld128<AUX>(rs, vo + (off & 4095u), sbase + (off & ~4095u)); }
template <int I> DI void dma1(rsrc_t rs, u32 vo, u32 sbase, LAS unsigned char* d) {
  __builtin_amdgcn_raw_ptr_buffer_load_lds(rs, (LAS u32*)(d + (I >> 2) * 4096), 16, vo, sbase + (I >> 2) * 4096, (I & 3) * 1024, SC1);
}
template <int I0, int I1> DI void dma_range(rsrc_t rs, u32 vo, u32 sbase, LAS unsigned char* d) {
  if constexpr (I0 < I1) {
    dma1<I0>(rs, vo, sbase, d);
    dma_range<I0 + 1, I1>(rs, vo, sbase, d);
  }
}
template <class F, int... I> DI void static_for_impl(F&& f, std::integer_sequence<int, I...>) { (f(std::integral_constant<int, I>{}), ...); }
template <int N, class F> DI void static_for(F&& f) { static_for_impl(f, std::make_integer_sequence<int, N>{}); }
DI u32 cnt_off(int idx) { return CTRL + idx * CSTR; }
DI u32 lane0_off() { return (threadIdx.x & 63) == 0 ? 0u : 0x80000000u; }
DI void add1(rsrc_t rs, u32 l0, u32 coff) { (void)__builtin_amdgcn_raw_ptr_buffer_atomic_add_i32(1, rs, l0, coff, 0); }
template <int N> DI void wsignal(rsrc_t rs, u32 l0, u32 coff) {
  SB();
  asm volatile("s_waitcnt vmcnt(%0)" ::"n"(N) : "memory");
  add1(rs, l0, coff);
  SB();
}
DI u32 peek(rsrc_t rs, u32 l0, u32 coff) { return __builtin_amdgcn_raw_ptr_buffer_atomic_add_i32(0, rs, l0, coff, 0); }
DI void wfinish(rsrc_t rs, u32 l0, u32 coff, u32 n, u32 pv) {
  if (__builtin_amdgcn_readfirstlane(pv) < n) {
#pragma unroll 1
    for (u32 spins = 0;; ++spins) {
      if (__builtin_amdgcn_readfirstlane(peek(rs, l0, coff)) >= n) break;
      if (spins > (1u << 16)) { __builtin_amdgcn_raw_buffer_store_b32(1u, rs, 0, cnt_off(C_TMO), SC1); break; }
    }
  }
  asm volatile("" ::: "memory");
}
DI void wwait(rsrc_t rs, u32 l0, u32 coff, u32 n) { wfinish(rs, l0, coff, n, peek(rs, l0, coff)); }
DI void apeek(rsrc_t rs, u32 l0, u32 coff, u32& p) {
  SB();
  asm volatile("v_mov_b32 %0, 0\n\tbuffer_atomic_add %0, %1, %2, %3 offen sc0 ; APEEK" : "=&v"(p) : "v"(l0), "s"(rs), "s"(coff) : "memory");
  SB();
}
#define PCHK(P, W) "s_waitcnt vmcnt(" W ")\n\tv_readfirstlane_b32 %3, " P "\n\ts_cmp_ge_u32 %3, %8\n\ts_cbranch_scc1 .Lpd%=\n\tv_mov_b32 " P ", 0\n\tbuffer_atomic_add " P ", %5, %6, %7 offen sc0\n\t"
template <int N0, int N1, int N2> DI void pfinish(rsrc_t rs, u32 l0, u32 coff, u32 n, u32 (&p)[3]) {
  u32 t, cnt;
  SB();
  asm volatile(PCHK("%0", "%9") PCHK("%1", "%10") PCHK("%2", "%11") "s_movk_i32 %4, 0x7fff\n.Lpl%=:\n\t" PCHK("%0", "2") PCHK("%1", "2") PCHK("%2", "2")
               "s_sub_u32 %4, %4, 1\n\ts_cbranch_scc0 .Lpl%=\n.Lpd%=:"
               : "+v"(p[0]), "+v"(p[1]), "+v"(p[2]), "=&s"(t), "=&s"(cnt)
               : "v"(l0), "s"(rs), "s"(coff), "n"(n), "n"(N0), "n"(N1 + 1), "n"(N2 + 2)
               : "memory", "scc");
  SB();
}
#define PCHK2(P, W) "s_waitcnt vmcnt(" W ")\n\tv_readfirstlane_b32 %2, " P "\n\ts_cmp_ge_u32 %2, %7\n\ts_cbranch_scc1 .Lqd%=\n\tv_mov_b32 " P ", 0\n\tbuffer_atomic_add " P ", %4, %5, %6 offen sc0\n\t"
template <int N0, int N1> DI void pfinish2(rsrc_t rs, u32 l0, u32 coff, u32 n, u32 (&p)[2]) {
  u32 t, cnt;
  SB();
  asm volatile(PCHK2("%0", "%8") PCHK2("%1", "%9") "s_movk_i32 %3, 0x7fff\n.Lql%=:\n\t" PCHK2("%0", "1") PCHK2("%1", "1")
               "s_sub_u32 %3, %3, 1\n\ts_cbranch_scc0 .Lql%=\n.Lqd%=:"
               : "+v"(p[0]), "+v"(p[1]), "=&s"(t), "=&s"(cnt)
               : "v"(l0), "s"(rs), "s"(coff), "n"(n), "n"(N0), "n"(N1 + 1)
               : "memory", "scc");
  SB();
}
DI void pretire(u32 (&p)[2]) { asm volatile("; PRETIRE %0 %1" ::"v"(p[0]), "v"(p[1])); }
DI void pretire(u32 (&p)[3]) { asm volatile("; PRETIRE %0 %1 %2" ::"v"(p[0]), "v"(p[1]), "v"(p[2])); }
#define NAP(n) do { SB(); __builtin_amdgcn_s_sleep(n); SB(); } while (0)
DI void poll3(rsrc_t rs, u32 vo, u32 need) {
#pragma unroll 1
  for (u32 spins = 0;; ++spins) {
    const u32 v = peek(rs, vo, 0);
    if ((__builtin_amdgcn_ballot_w64(v >= need) & 7ull) == 7ull) break;
    if (spins > (1u << 16)) { __builtin_amdgcn_raw_buffer_store_b32(1u, rs, 0, cnt_off(C_TMO), SC1); break; }
  }
  asm volatile("" ::: "memory");
}
template <int CTRL_> DI float dpp_add(float v) {
  return v + __builtin_bit_cast(float, __builtin_amdgcn_update_dpp(0, __builtin_bit_cast(int, v), CTRL_, 0xf, 0xf, true));
}

constexpr int SCR = 131072;
constexpr int PK_OFF = SCR, PK_STR = 144, VV_OFF = PK_OFF + 128 * PK_STR, VV_STR = 80, KF_OFF = VV_OFF + 128 * VV_STR;
constexpr int XS_OFF = 98304;
constexpr int RED_OFF = SCR;
constexpr int LDS_BYTES = 163840;
static_assert(KF_OFF + 4 * 544 <= LDS_BYTES, "lds");

DI void lds_put1(LAS unsigned char* p, const f32x16& a, int q) {
  *(LAS f32x4*)(p + q * 1024) = f32x4{a[4 * q], a[4 * q + 1], a[4 * q + 2], a[4 * q + 3]};
}

extern __shared__ __attribute__((aligned(16))) unsigned char smem[];

struct Sch { int put2[4], put3[4], bar1, rd[8], fu[8], put1[4], bar2, rd1, f1[4]; };
constexpr Sch sch_of(int KL) {
  return KL >= 14 ? Sch{{0, 1, 2, 3}, {4, 5, 6, 7}, 9, {9, 9, 9, 10, 10, 10, 11, 11}, {12, 13, 14, 19, 21, 22, 23, 24}, {15, 16, 17, 18}, 20, 20, {25, 26, 27, 27}}
                  : Sch{{0, 0, 1, 1}, {2, 2, 3, 3}, 5, {5, 5, 5, 5, 6, 6, 6, 6}, {7, 7, 8, 8, 11, 12, 12, 13}, {9, 9, 10, 10}, 11, 11, {14, 14, 15, 15}};
}
struct FrO {
  static constexpr int NS = 16, KL = 8;
  static constexpr bool own(int i) { return i >= 8; }
  static constexpr int ks(int i) { return i & 7; }
  static constexpr u32 off(int i) { return (i & 7) * 1024; }
  static constexpr int lstep(int q, int win) { return q - win; }
  static constexpr int first(int win) { return win; }
};
struct FrH {
  static constexpr int NS = 56, KL = 14;
  static constexpr int part(int i) { return i < 28 ? i / 14 : 2 + ((i / 7) & 1); }
  static constexpr bool own(int i) { return i < 28 ? ((i / 7) & 1) : i >= 42; }
  static constexpr int ks(int i) { return part(i) * 7 + i % 7; }
  static constexpr u32 off(int i) { return part(i) * 65536 + (i % 7) * 1024; }
  static constexpr int lstep(int q, int win) {
    const int c = q >= 35 ? T3CHK : q >= 28 ? T2CHK : 0;
    return q - win < c ? c : q - win;
  }
  static constexpr int first(int win) { return 14; }
};
template <class Fr> DI v4u ld_fr(rsrc_t rs, u32 l16, u32 a_own, u32 a_oth, int i) { return ldf<SC1>(rs, l16, Fr::own(i) ? a_own : a_oth, Fr::off(i)); }
template <class Fr, int WIN, int PRE, class Win, class Pre, class Mid>
DI void splitk_phase(rsrc_t rs, const v4u (&wf)[Fr::NS / 2][2], v4u (&af)[WIN], u32 a_own, u32 a_oth, LAS unsigned char* L, int w, u32 l16, float scale,
                     v4u (&res)[2], Win&& win, Pre&& pre_loop, Mid&& mid) {
  const f32x16 zero16 = {0.f, 0.f, 0.f, 0.f, 0.f, 0.f, 0.f, 0.f, 0.f, 0.f, 0.f, 0.f, 0.f, 0.f, 0.f, 0.f};
  constexpr int NS = Fr::NS, KP = Fr::KL, E0 = NS - KP, F0 = Fr::first(WIN);
  constexpr Sch sc = sch_of(KP);
  static_assert(WIN >= KP, "window");
  auto ld = [&](int i) { return ld_fr<Fr>(rs, l16, a_own, a_oth, i); };
#pragma unroll
  for (int i = PRE; i < F0; ++i) { win(i); af[i] = ld(i); SB(); }
  LAS unsigned char* r2 = L + opqv(RED_OFF + l16 + (w ^ 2) * 8192);
  LAS unsigned char* r3 = L + opqv(RED_OFF + l16 + (w ^ 3) * 8192 + 4096);
  LAS unsigned char* r0 = L + opqv(RED_OFF + l16 + w * 8192);
  LAS unsigned char* r1 = L + opqv(RED_OFF + l16 + (w ^ 1) * 8192);
  pre_loop();
  SB();
  f32x16 a0 = zero16, a1 = zero16, a2 = zero16, a3 = zero16;
  float u[16];
  static_for<E0>([&](auto I) {
    constexpr int i = I, ks = Fr::ks(i), e = i - (E0 - 4);
    const v4u bb = af[i % WIN];
    if constexpr (!Fr::own(i)) a2 = mfma32(wf[ks][0], bb, a2);
    else a1 = mfma32(wf[ks][1], bb, a1);
    SB();
    mid(I);
    static_for<NS - F0>([&](auto Q) { if constexpr (Fr::lstep(Q + F0, WIN) == i) { af[(Q + F0) % WIN] = ld(Q + F0); SB(); } });
    SB();
    if constexpr (!Fr::own(i)) a3 = mfma32(wf[ks][1], bb, a3);
    else a0 = mfma32(wf[ks][0], bb, a0);
    SB();
    if constexpr (e >= 0) {
      const u32 ra = res[e >> 1][2 * (e & 1)], rb = res[e >> 1][2 * (e & 1) + 1];
      u[4 * e] = bflo(ra); u[4 * e + 1] = bfhi(ra); u[4 * e + 2] = bflo(rb); u[4 * e + 3] = bfhi(rb);
#pragma unroll
      for (int m = 0; m < 4; ++m) PIN(u[4 * e + m]);
    }
    SB();
  });
  f32x4 p[8];
  auto acc4 = [&](int q, const f32x4& x) {
#pragma unroll
    for (int m = 0; m < 4; ++m) { const float xv = x[m]; u[4 * q + m] = __builtin_fmaf(xv, scale, u[4 * q + m]); PIN(u[4 * q + m]); }
  };
  static_for<2 * KP>([&](auto G) {
    constexpr int g = G, k = g % KP, ks = Fr::ks(E0 + k);
    if constexpr (g < KP) a1 = mfma32(wf[ks][1], af[(E0 + k) % WIN], a1);
    else a0 = mfma32(wf[ks][0], af[(E0 + k) % WIN], a0);
    SB();
    if constexpr (g == sc.bar1 || g == sc.bar2) BAR();
    static_for<8>([&](auto Q) { if constexpr (sc.rd[Q] == g) p[Q] = *(LAS f32x4*)(r0 + Q * 1024); });
    if constexpr (g == sc.rd1) static_for<4>([&](auto Q) { p[Q] = *(LAS f32x4*)(r1 + Q * 1024); });
    SB();
    static_for<4>([&](auto Q) { if constexpr (sc.put2[Q] == g) lds_put1(r2, a2, Q); });
    static_for<4>([&](auto Q) { if constexpr (sc.put3[Q] == g) lds_put1(r3, a3, Q); });
    static_for<4>([&](auto Q) { if constexpr (sc.put1[Q] == g) lds_put1(r0, a1, Q); });
    SB();
    static_for<8>([&](auto Q) { if constexpr (sc.fu[Q] == g) acc4(Q & 3, p[Q]); });
    static_for<4>([&](auto Q) { if constexpr (sc.f1[Q] == g) acc4(Q, p[Q]); });
    SB();
    mid(std::integral_constant<int, E0 + g>{});
    SB();
  });
#pragma unroll
  for (int q = 0; q < 4; ++q) {
    acc4(q, f32x4{a0[4 * q], a0[4 * q + 1], a0[4 * q + 2], a0[4 * q + 3]});
    res[q >> 1][2 * (q & 1)] = pk(u[4 * q], u[4 * q + 1]);
    res[q >> 1][2 * (q & 1) + 1] = pk(u[4 * q + 2], u[4 * q + 3]);
  }
}
DI void resid_store(rsrc_t rs, const v4u (&res)[2], u32 l16, u32 sto, u32 xoff, u32 soff) {
#pragma unroll
  for (int s = 0; s < 2; ++s) st128<SC1>(res[s], rs, l16 + s * 1024, xoff);
  SB();
  float s0 = 0.f, q0 = 0.f, s1 = 0.f, q1 = 0.f;
#pragma unroll
  for (int m = 0; m < 4; ++m) {
    s0 = dot2(res[0][m], ONES2, s0); q0 = dot2(res[0][m], res[0][m], q0);
    s1 = dot2(res[1][m], ONES2, s1); q1 = dot2(res[1][m], res[1][m], q1);
  }
  st64<SC1>(__builtin_bit_cast(v2u, f32x2{sum32(s0 + s1), sum32(q0 + q1)}), rs, sto, soff);
}

DI void stats_fin(const f32x4 (&sp)[4], float& nmean, float& rstd) {
  float t[4];
#pragma unroll
  for (int m = 0; m < 4; ++m) { t[m] = sp[0][m] + sp[1][m]; PIN(t[m]); }
#pragma unroll
  for (int i = 2; i < 4; ++i)
#pragma unroll
    for (int m = 0; m < 4; ++m) { t[m] += sp[i][m]; PIN(t[m]); }
  const float s = mix16(t[0], t[2]), q = mix16(t[1], t[3]);
  nmean = sum32(s) * (-1.f / 512.f);
  const float var = fmaxf(sum32(q) * (1.f / 512.f) - nmean * nmean, 0.f);
  rstd = __builtin_amdgcn_rsqf(var + 1e-5f);
  PIN(nmean);
  PIN(rstd);
}

template <class Hook> DI void dual_softmax(const f32x16& p, float c, u32 (&o)[16], Hook&& hook) {
  float mx = p[0], mn = p[0];
#pragma unroll
  for (int i = 1; i < 16; ++i) { mx = fmaxf(mx, p[i]); mn = fminf(mn, p[i]); }
  float ap = -mx * c, an = mn * c;
  PIN(ap);
  PIN(an);
  SB();
  hook(std::integral_constant<int, 0>{});
  SB();
  f32x2 ep[8], en[8];
#pragma unroll
  for (int i = 0; i < 8; ++i) {
    const f32x2 pv = {p[2 * i], p[2 * i + 1]};
    ep[i] = pv * c + ap;
    en[i] = pv * (-c) + an;
    PIN(ep[i]);
    PIN(en[i]);
  }
  SB();
  hook(std::integral_constant<int, 1>{});
  SB();
  f32x2 sa, sc;
  static_for<8>([&](auto I) {
    constexpr int i = I;
    ep[i] = f32x2{__builtin_amdgcn_exp2f(ep[i].x), __builtin_amdgcn_exp2f(ep[i].y)};
    en[i] = f32x2{__builtin_amdgcn_exp2f(en[i].x), __builtin_amdgcn_exp2f(en[i].y)};
    PIN(ep[i]);
    PIN(en[i]);
    if constexpr (i == 2) { sa = ep[0] + ep[1]; sc = en[0] + en[1]; PIN(sa); PIN(sc); }
    if constexpr (i > 2) { sa += ep[i - 1]; sc += en[i - 1]; PIN(sa); PIN(sc); }
    SB();
    hook(std::integral_constant<int, 2 + i>{});
    SB();
  });
  sa += ep[7];
  sc += en[7];
  const float rp = __builtin_amdgcn_rcpf(sa.x + sa.y), rn = __builtin_amdgcn_rcpf(sc.x + sc.y);
#pragma unroll
  for (int i = 0; i < 8; ++i) { ep[i] *= rp; PIN(ep[i]); }
#pragma unroll
  for (int i = 0; i < 8; ++i) { en[i] *= rn; PIN(en[i]); }
  SB();
#pragma unroll
  for (int i = 0; i < 8; ++i) { o[i] = pk(ep[i].x, ep[i].y); o[8 + i] = pk(en[i].x, en[i].y); }
}
constexpr int p3_g0(int v) { return (v * 58) / 48; }
constexpr bool p3_w2gap(int gi) { return (gi * W2B) / 8 != ((gi + 1) * W2B) / 8; }
constexpr int p3_pre(int layer, int g) {
  int n = layer == 0 && p3_w2gap(g);
  for (int q = 0; q < 6; ++q) n += p3_g0(8 * q + 7) + 5 == g;
  return n;
}
constexpr int p3_post(int g) { return (g == HS0) + (g == HS1) + (g == PK0); }
constexpr int p3_younger(int layer, int t, int gw) {
  const int gs = p3_g0(16 * t + 15) + 5;
  int n = p3_post(gs);
  for (int g = gs + 1; g < gw; ++g) n += p3_pre(layer, g) + p3_post(g);
  return n + (gw < 64 ? p3_pre(layer, gw) : 0);
}
static_assert(p3_g0(32) >= 35 && p3_g0(47) + 5 < 64 && p3_g0(15) + 5 < HS0 && p3_g0(31) + 5 < HS1 && HS0 != HS1, "P3 schedule");

__global__ void __launch_bounds__(256, 1)
k_main(const uint16_t* __restrict__ x, const uint16_t* __restrict__ wqkv, const uint16_t* __restrict__ wphi,
       const uint16_t* __restrict__ wo_, const uint16_t* __restrict__ w1, const uint16_t* __restrict__ w2,
       unsigned char* ws, unsigned char* out) {
  LAS unsigned char* L = (LAS unsigned char*)smem;
  const int b = blockIdx.x, tid = threadIdx.x, lane = tid & 63, w = __builtin_amdgcn_readfirstlane(tid >> 6);
  const int xcd = b & 7, j = b >> 3;
  const int rt = 4 * (xcd >> 1) + (j >> 3), hp = 8 * (xcd & 1) + (j & 7), tt = 4 * rt + w;
  const bool p3on = (j & 7) < 7;
  const int fb = p3on ? 7 * (xcd & 1) + (j & 7) : 0;
  const int rt2 = 4 * xcd + (j >> 3), cb = j & 7;
  const int tte = 2 * rt2 + (w >> 1), n0 = 64 * cb + 32 * (w & 1);
  const rsrc_t rs = mkrsrc((unsigned char*)(((size_t)ws + MB2 - 1) & ~(size_t)(MB2 - 1)), WS_END);
  const f32x16 zero16 = {0.f, 0.f, 0.f, 0.f, 0.f, 0.f, 0.f, 0.f, 0.f, 0.f, 0.f, 0.f, 0.f, 0.f, 0.f, 0.f};
  STAMP(63);
  v4u res[2];
  float csq[3], cs1[4];
  v4u wph[2];
  u32 tag_lo, tag_hi, tws = 0;
  u32 sd;
  const u32 l0 = lane0_off();
  const u32 nlh = !p3on ? 0u : (fb == 3 || fb == 10) ? 2u : 1u;
  v4u xr[32];
  const uint16_t* xo = x;
  asm volatile("" : "+s"(xo));
  const rsrc_t rsx = mkrsrc(xo, S * D * 2);
  auto trfrag = [&](LAS unsigned char* p, int str) { const v2u t0 = trread(p), t1 = trread(p + str); return v4u{t0.x, t0.y, t1.x, t1.y}; };
  const u32 sv = ((lane >> 2) & 1) == ((lane >> 4) & 1) ? ONES2 : 0u;
  v4u selm = {sv, sv, sv, sv};
  if (b >= 248 && w < 2) {
#pragma unroll
    for (int i = 0; i < NCNT * CSTR / 16384; ++i) st128<SC1>(v4u{0u, 0u, 0u, 0u}, rs, lane * 16, CTRL + ((b - 248) * 2 + w) * (NCNT * CSTR / 16) + i * 1024);
  }
  const v2u ep = ld64<SC1>(rs, 0, EPOCH_OFF);
  SB();
  u32 cpf;
  {
    unsigned long long pc;
    u32 len;
    asm volatile("s_getpc_b64 %0\n.Lkcode0:\n\ts_mov_b32 %1, .Lkcode1-.Lkcode0" : "=s"(pc), "=s"(len));
    cpf = ld32<0>(mkrsrc((const void*)pc, len), w == CPW ? lane * 128 + (j & 7) * 8192 : 0x80000000u, 0);
  }
  SB();
  int kind = 4, nb = 0, kq = 0, ld = 512;
  const uint16_t* W = x;
  if (w == 0) {
    if (b < 192) { kind = 0; W = wqkv; ld = 1536; nb = b >> 3; kq = b & 7; }
  } else if (w >= 2) {
    const int hw = w & 1;
    if (b < 112) { kind = 1; W = w1; ld = 1792; nb = b >> 2; kq = (b & 3) * 2 + hw; }
    else if (b < 144) { kind = 2; W = wo_; nb = (b - 112) >> 2; kq = (b & 3) * 2 + hw; }
    else { kind = 3; W = w2; nb = (b - 144) / 14; kq = ((b - 144) - 14 * nb) * 2 + hw; }
  }
  LAS unsigned char* img = L + SCR + w * 8192;
  {
    const rsrc_t rsu = mkrsrc(W, kind < 4 ? (kind == 3 ? 1792 : 512) * ld * 2 : 0);
    const u32 vu = (lane >> 3) * ld * 2 + (((lane & 7) ^ ((lane >> 2) & 4)) * 16);
#pragma unroll
    for (int i = 0; i < 8; ++i) __builtin_amdgcn_raw_ptr_buffer_load_lds(rsu, (LAS u32*)(img + i * 1024), 16, vu, (64 * kq + 8 * i) * ld * 2 + nb * 128, 0, 0);
  }
  SB();
  const u32 vx = (lane >> 3) * 1024 + (lane & 7) * 16;
  const u32 rot = ROT ? j & 7 : 0;
  auto xld = [&](auto I) { constexpr int i = I; xr[i] = ld128<0>(rsx, vx, tt * 32768 + (i & 3) * 8192 + (((i >> 2) ^ rot) * 128)); SB(); };
  static_for<XA>([&](auto I) { xld(I); });
  u32 obase, cstr, kstr, kstr2, csb, ovo, cvo, ua;
  {
    const int t32 = 2 * nb, sec = t32 >> 4, tile = sec == 0 ? 2 : sec - 1;
    obase = opq(kind == 0 ? WQKV_T + (t32 & 15) * 98304 + tile * 32768 + kq * 4096 : kind == 1 ? W1_T + t32 * 32768 + kq * 4096
                : kind == 2 ? WO_T + (nb * 32 + kq * 4) * 2048 : W2_T + (nb * 112 + (kq & 1) * 56 + (kq >> 1) * 2) * 2048);
    cstr = opq(kind == 0 ? 98304 : kind == 1 ? 32768 : 1024);
    kstr = opq(kind < 2 ? 1024 : 2048);
    kstr2 = opq(kind == 3 ? 57344 : 2 * kstr);
    csb = opq(kind == 0 ? CS_QKV + (((t32 & 15) * 3 + tile) * 8 + kq) * 128 : CS_W1 + (t32 * 8 + kq) * 128);
    ovo = opqv(kind < 4 ? lane * 16 : 0x80000000u);
    cvo = opqv(kind < 2 ? (lane >> 5) * (kind == 0 ? 3072 : 1024) + (lane & 31) * 4 : 0x80000000u);
    const u32 c16 = lane & 15, Q = ((lane >> 2) & 4) + (lane & 3);
    const u32 Cc = kind == 0 ? 4 * (Q & 1) + (Q >> 1) : (Q & 4) + 2 * (Q & 1) + ((Q >> 1) & 1);
    ua = opqv((8 * (lane >> 5) + (c16 >> 2)) * 128 + (c16 >> 3) * 64 + Cc * 8);
  }
  const u32 jadd = opq(cnt_off(C_J + (b < 112 ? 16 + (b >> 3) : b < 144 ? (b - 112) >> 2 : (b - 144) / 14)));
  auto unit = [&]() {
    v4u o[8];
#pragma unroll
    for (int i = 0; i < 8; ++i) o[i] = trfrag(img + (ua ^ ((i >> 2) * 64)) + (i & 3) * 2048, 512);
    SB();
    f32x4 cs[2] = {{0.f, 0.f, 0.f, 0.f}, {0.f, 0.f, 0.f, 0.f}};
#pragma unroll
    for (int i = 0; i < 8; ++i) {
      st128<SC1>(o[i], rs, ovo, obase + (i >> 2) * cstr + ((i >> 1) & 1) * kstr2 + (i & 1) * kstr);
      cs[i >> 2] = mfma16(selm, o[i], cs[i >> 2]);
    }
    const float c0 = cs[0][0], c1 = cs[1][0];
    __builtin_amdgcn_raw_buffer_store_b32(__builtin_bit_cast(u32, lane < 32 ? c0 : c1), rs, cvo, csb, SC1);
    SB();
  };
  SB();
  const u32 tvo = opqv((((b >= 248 && w < 2) || kind == 0) && lane < 16) ? lane * 8 : 0x80000000u);
  const u32 tso = opq(b >= 248 ? INIT_OFF + (((b & 7) * 2 + w) & 15) * CSTR : FLAG_OFF + b * CSTR);
  SB();
  v4u w2f[28][2];

  static_for<2>([&](auto LAYER) __attribute__((always_inline)) {
    constexpr int layer = LAYER;
    constexpr int sb = layer * 29;
    v4u wo[8][2];
    const u32 wb = opq(W2_T + cb * 229376 + w * 14336);
    const u32 wsw = (w & 1) * 1024;
    u32 pvx[3], pvy[2];
    const u32 rx = ROT2 ? opq(rot * 4096) : 0u;
#define IC(n) std::integral_constant<int, n>{}
    {
      const u32 ln = opqv(lane), l16 = ln * 16, r = ln & 31, hf = ln >> 5;
      if constexpr (layer) STAMP(sb + 0);
      constexpr int DX = DXV;
      v4u xf[32];
      auto xtile = [&](int i, u32 t) { return XT + i * MB2 + (t & 31) * 65536 + (sd ^ ((i & 1) ? 0 : 8192)); };
      auto stile = [&](int i, u32 t) { return ST + i * ST_SZ + blk((t & 31) * 4096) + sd; };
      const u32 xb = layer ? xtile(1, tt) : 0;
      if constexpr (layer > 0 && XMODE == 1) apeek(rs, l0, (cnt_off(C_X + (tt & 31)) + sd), pvx[0]);
      SB();
      f32x4 sp[4];
      const u32 stb = layer ? stile(1, tt) : 0;
      const u32 wqb = opq(WQKV_T + hp * 98304 + 65536 + w * 8192);
      const u32 g4 = ln >> 4, c16 = ln & 15;
      LAS unsigned char* a_v = L + opqv(VV_OFF + (32 * w + r) * VV_STR + hf * 32);
      LAS unsigned char* a_pk = L + opqv(PK_OFF + (32 * w + r) * PK_STR + hf * 64);
      LAS unsigned char* a_pa = L + opqv(PK_OFF + (8 * g4 + (c16 >> 2)) * PK_STR + w * 32 + (ln & 3) * 8);
      LAS unsigned char* a_pb = L + opqv(VV_OFF + (8 * g4 + (c16 >> 2)) * VV_STR + (w >> 1) * 32 + (ln & 3) * 8);
      const u32 o_kv = opqv((g4 >> 1) * 256 + c16 * 16 + (g4 & 1) * 8), o_z = opqv(hf * 8192 + r * 4);
      u32 jv = 0, lat = 0, vv = 0;
      constexpr int wok = layer ? WOK : 0, NPF = 16 - wok + (layer ? 24 : 32);
      constexpr int s_q1 = 0, s_q2 = s_q1 + NQ1, s_od = s_q2 + NQ2, s_op = s_od + ODR, n_op = layer ? OPL1 : OPL0, s_p2 = s_op + n_op, s_xd = NPF - XDR;
      static_assert(s_p2 <= s_xd && s_xd - s_p2 <= 12 && s_od <= NPF - 8 && s_op >= 16 - wok, "prefetch schedule");
      LAS v4u* WL = (LAS v4u*)(L + l16);
      LAS v4u* WQ = (LAS v4u*)(L + opqv(l16 + 65536));
      const u32 wob = opq(WO_T + (cb * 4 + w) * 16384);
      auto wold = [&](auto I) {
        constexpr int i = I;
        wo[i >> 1][i & 1] = ld128<SC1>(rs, l16 + ((i >> 1) & 1) * 2048, wob + (i >> 2) * 4096 + ((i & 1) ? 1024 - wsw : wsw));
      };
      f32x16 aK = zero16, aV = zero16, aQ;
      float rstd, nmean, c;
      const u32 jvo = opqv(ln == 0 ? cnt_off(C_J + cb) : ln == 1 ? cnt_off(C_J + 16 + fb) : 0x80000000u);
      float cp[3][4];
      v4u wk[3], wv[3];
      SB();
      if constexpr (layer == 0) {
        LAS unsigned char* sw[2];
        LAS unsigned char* sr[4];
#pragma unroll
        for (int i = 0; i < 2; ++i) sw[i] = L + opqv(XS_OFF + w * 8192 + (ln >> 3) * 128 + (((ln & 7) ^ (4 * i + (ln >> 4))) * 16));
#pragma unroll
        for (int f = 0; f < 4; ++f) sr[f] = L + opqv(XS_OFF + w * 8192 + r * 128 + (((2 * f + hf) ^ ((r >> 1) & 7)) * 16));
        const u32 fu = ln >> 1, fvo = opqv(ln >= 12 ? 0x80000000u : FLAG_OFF + ((fu < 4 ? (1 + (w >> 1)) * 64 + (w & 1) * 4 + fu : 2 * w + fu - 4) + (hp >> 1) * 8) * CSTR + (ln & 1) * 4);
        PIN(selm);
        SB();
        if (w == 0) {
          asm volatile("s_waitcnt vmcnt(%0)" ::"n"(XA) : "memory");
          STAMP(58);
          unit();
          STAMP(59);
        }
        asm volatile("s_barrier" ::: "memory");
        {
          const unsigned long long e = ((unsigned long long)__builtin_amdgcn_readfirstlane(ep.y) << 32) | __builtin_amdgcn_readfirstlane(ep.x);
          unsigned long long t = e * 0x9E3779B97F4A7C15ull + 0x632BE59BD9B4E019ull;
          if (t == e) t = ~e;
          tag_lo = (u32)t;
          tag_hi = (u32)(t >> 32);
        }
        const u32 fneed = (ln & 1) ? tag_hi : tag_lo;
        f32x4 sa = {0.f, 0.f, 0.f, 0.f};
        f32x16 gr = zero16;
        u32 fv;
        v2u tg;
        SB();
        auto vm = [&](auto N) {
          constexpr int n = N, xi = XA + n, di = n - 8 * FCK;
          if constexpr (xi < 32) xld(IC(xi));
          if constexpr (xi == 32) __builtin_amdgcn_raw_ptr_buffer_load_lds(mkrsrc(wphi, 16384), (LAS u32*)(L + XS_OFF + w * 8192 + 4096), 16, l16, hp * 1024, 0, 0);
          if constexpr (xi == 33) tws = ld32<0>(rs, TLBW ? (ln + 64 * w + (TLBD ? 256 * j : 0)) * TLBW : 0x80000000u, 0);
          if constexpr (xi == XB) {
            if (w == 0) asm volatile("s_waitcnt vmcnt(%0)" ::"n"(XB - XA + 1) : "memory");
            st64<SC1>(v2u{tag_lo, tag_hi}, rs, tvo, tso);
            STAMP(60);
          }
          if constexpr (di >= 0 && di < 16)
            dma1<(di & 3)>(rs, l16, WQKV_T + hp * 98304 + w * 16384 + (di >> 2) * 4096, L + (w >> 1) * 32768 + ((((w & 1) * 4 + (di >> 2)) ^ rot) * 4096));
          if constexpr (di == 16) tg = ld64<SC1>(rs, c16 * CSTR, INIT_OFF);
          SB();
        };
        static_assert(XA + 8 * FCK > 33 && XB < 32 && FCK <= 7 && UB >= FCK + 3 && XA >= 4, "start schedule");
        static_for<11>([&](auto B) {
          constexpr int blk = B;
          if constexpr (blk == UB) { if (w >= 2) unit(); }
          if constexpr (blk == FPK) { fv = peek(rs, fvo, 0); SB(); }
          if constexpr (blk == FCK) {
            STAMP(0);
            if ((__builtin_amdgcn_ballot_w64(fv == fneed) & 0xfffull) != 0xfffull) {
#pragma unroll 1
              for (u32 spins = 0; spins < (1u << 16); ++spins)
                if ((__builtin_amdgcn_ballot_w64(peek(rs, fvo, 0) == fneed) & 0xfffull) == 0xfffull) break;
            }
            asm volatile("" ::: "memory");
            STAMP(61);
          }
          if constexpr (blk < 10) static_for<4>([&](auto I) {
            constexpr int i = I;
            if constexpr (blk > 1) gr = mfma32(xf[4 * blk - 8 + i], xf[4 * blk - 8 + i], gr);
            SB();
            vm(IC(8 * blk + 2 * i));
            if constexpr (blk < 8) *(LAS v4u*)(sw[i & 1] + i * 1024) = xr[4 * blk + i];
            SB();
            if constexpr (blk > 1) { sa = mfma16(selm, xf[4 * blk - 8 + i], sa); asm volatile("" : "+a"(sa)); }
            SB();
            vm(IC(8 * blk + 2 * i + 1));
          });
          if constexpr (blk < 8) static_for<4>([&](auto F) { xf[4 * blk + F] = *(LAS v4u*)(sr[F]); SB(); });
        });
        asm volatile("" ::"v"(tws));
        asm volatile("" ::"v"(cpf));
        {
          const u32 Qc = 4 * (g4 & 1) + (c16 & 3);
          LAS unsigned char* a_ph = L + opqv(XS_OFF + w * 8192 + 4096 + hf * 512 + (c16 >> 2) * 32 + (Qc >> 1) * 8);
#pragma unroll
          for (int s = 0; s < 2; ++s) wph[s] = trfrag(a_ph + s * 256, 128);
        }
        const float sa0 = sa[0];
        nmean = sa0 * (-1.f / 512.f);
        auto sel = [](bool cnd, float a0, float a1) { PIN(a0); PIN(a1); return cnd ? a1 : a0; };
        auto g4f = [&](int i) { return sel(r & 2, sel(r & 1, gr[4 * i], gr[4 * i + 1]), sel(r & 1, gr[4 * i + 2], gr[4 * i + 3])); };
        const float gd = sel(r & 16, sel(r & 8, g4f(0), g4f(1)), sel(r & 8, g4f(2), g4f(3)));
        const bool dl = ((r >> 2) & 1) == hf;
        const float dg = dl ? gd : 0.f;
        rstd = __builtin_amdgcn_rsqf(fmaxf(sum32(dg) * (1.f / 512.f) - nmean * nmean, 0.f) + 1e-5f);
        c = rstd * LOG2E;
#pragma unroll
        for (int s = 0; s < 2; ++s)
#pragma unroll
          for (int m = 0; m < 4; ++m) wph[s][m] = dl ? wph[s][m] : 0u;
        PIN(wph[0]);
        PIN(wph[1]);
        PIN(c);
        PIN(rstd);
        PIN(nmean);
        SB();
        if constexpr (UB > FCK) { if (w >= 2) asm volatile("s_waitcnt vmcnt(9)" ::: "memory"); else DRAIN(); }
        else DRAIN();
        if (w == 0 && !__all(tg.x == tag_lo && tg.y == tag_hi)) {
#pragma unroll 1
          for (u32 spins = 0; spins < (1u << 16); ++spins) {
            asm volatile("" ::: "memory");
            const v2u t = ld64<SC1>(rs, c16 * CSTR, INIT_OFF);
            if (__all(t.x == tag_lo && t.y == tag_hi)) break;
          }
        }
        BAR();
#pragma unroll
        for (int i = 0; i < 2; ++i) { wk[i] = WL[i * 64]; wv[i] = WL[(32 + i) * 64]; }
        if constexpr (UB <= FCK) add1(rs, w >= 2 ? l0 : 0x80000000u, jadd);
        SB();
      } else {
        if constexpr (XMODE == 0) { NAP(PS3); pvx[0] = peek(rs, l0, (cnt_off(C_X + (tt & 31)) + sd)); SB(); }
        if constexpr (XMODE == 3) { NAP(PS3); apeek(rs, l0, (cnt_off(C_X + (tt & 31)) + sd), pvy[0]); NAP(PS5); apeek(rs, l0, (cnt_off(C_X + (tt & 31)) + sd), pvy[1]); }
        if constexpr (XMODE == 1) { NAP(PS1); apeek(rs, l0, (cnt_off(C_X + (tt & 31)) + sd), pvx[1]); NAP(PS2); apeek(rs, l0, (cnt_off(C_X + (tt & 31)) + sd), pvx[2]); SB(); }
        asm volatile("s_waitcnt vmcnt(%0)" ::"n"(XMODE == 1 ? 4 : XMODE == 3 ? 3 : XMODE == 0 ? 2 : 1) : "memory");
        BAR();
#pragma unroll
        for (int i = 0; i < 2; ++i) { wk[i] = WL[i * 64]; wv[i] = WL[(32 + i) * 64]; }
        SB();
        if constexpr (XMODE == 1) pfinish<2, 1, 0>(rs, l0, (cnt_off(C_X + (tt & 31)) + sd), 16, pvx);
        else if constexpr (XMODE == 3) pfinish2<1, 0>(rs, l0, (cnt_off(C_X + (tt & 31)) + sd), 16, pvy);
        else if constexpr (XMODE == 0) wfinish(rs, l0, (cnt_off(C_X + (tt & 31)) + sd), 16, pvx[0]);
        else { NAP(PS3); wwait(rs, l0, (cnt_off(C_X + (tt & 31)) + sd), 16); }
        STAMP(sb + 1);
#pragma unroll
        for (int ks = 0; ks < DX; ++ks) { xf[ks] = ldr(rs, l16, xb, rx, ks); SB(); }
      }
      STAMP(sb + 26);
      static_for<32>([&](auto KSI) {
        constexpr int ks = KSI, q0 = layer ? 20 : 8;
        aK = mfma32(wk[ks % 3], xf[ks], aK);
        SB();
        if constexpr (ks + 2 < 32) wk[(ks + 2) % 3] = WL[(ks + 2) * 64];
        if constexpr (layer > 0 && ks + DX < 32) xf[ks + DX] = ldr(rs, l16, xb, rx, ks + DX);
        if constexpr (ks >= q0 && ks < q0 + 8) dma1<((ks - q0) & 3)>(rs, l16, wqb + ((ks - q0) >> 2) * 4096, L + 65536 + (((2 * w + ((ks - q0) >> 2)) ^ (layer && !ROT2 ? 0 : rot)) * 4096));
        if constexpr (ks >= 28 && ks < 28 + wok) wold(IC(ks - 28));
        if constexpr (layer == 0 && ks < 6) cp[ks >> 1][2 * (ks & 1)] = __builtin_bit_cast(float, ld32<SC1>(rs, ln * 4 + (ks & 1) * 512 + (ks >> 1) * 1024, CS_QKV + hp * 3072));
        if constexpr (ks == 23) { SB(); aV = mfma32f_pad<5>(csq[1], nmean, aV); }
        SB();
        aV = mfma32(wv[ks % 3], xf[ks], aV);
        SB();
        if constexpr (ks + 2 < 32) wv[(ks + 2) % 3] = WL[(32 + ks + 2) * 64];
        if constexpr (layer == 0 && ks < 6) cp[ks >> 1][2 * (ks & 1) + 1] = __builtin_bit_cast(float, ld32<SC1>(rs, ln * 4 + (ks & 1) * 512 + 256 + (ks >> 1) * 1024, CS_QKV + hp * 3072));
        if constexpr (layer == 0 && (ks == 18 || ks == 22)) res[(ks - 18) >> 2] = ld128<0>(rsx, r * 1024 + hf * 16 + ((ks - 18) >> 2) * 32, tte * 32768 + n0 * 2);
        if constexpr (layer == 0 && ks == 19) {
#pragma unroll
          for (int t = 0; t < 3; ++t) { csq[t] = (cp[t][0] + cp[t][1]) + (cp[t][2] + cp[t][3]); PIN(csq[t]); }
        }
        if constexpr (layer == 0 && ks == 19 && UB > FCK) add1(rs, w >= 2 ? l0 : 0x80000000u, jadd);
        if constexpr (layer > 0) {
          if constexpr (ks < 4) sp[ks] = __builtin_bit_cast(f32x4, ld128<SC1>(rs, l16 + ks * 1024, stb));
          if constexpr (ks == 1 && XMODE == 1) pretire(pvx);
          if constexpr (ks == 1 && XMODE == 3) pretire(pvy);
          if constexpr (ks == 17) stats_fin(sp, nmean, rstd);
          if constexpr (ks == 18) { c = rstd * LOG2E; PIN(c); }
        }
        if constexpr (ks == 20) { SB(); aK = mfma32f_pad<5>(csq[0], nmean, aK); }
        SB();
      });
      STAMP(sb + 2);
      const u32 w1b = opq(W1_T + fb * 131072);
      auto w1dma = [&](auto I) {
        constexpr int i = I;
        const u32 o = i < 16 ? w * 16384 + (i >> 2) * 4096 : (i < 24 ? 98304 : 65536) + w * 8192 + ((i >> 2) & 1) * 4096;
        dma1<(i & 3)>(rs, l16, w1b + o, L + (o ^ rx));
      };
      auto pf = [&](auto N) {
        constexpr int n = N + wok;
        if constexpr (n < 16) wold(IC(n));
        else w1dma(IC(n - 16 + (layer && n >= 32 ? 8 : 0)));
        SB();
      };
      {
        const v4u k0 = pack8(aK, 0), k1 = pack8(aK, 1);
        f32x16 p = mfma32(wph[0], k0, zero16);
        p = mfma32(wph[1], k1, p);
        SB();
        if constexpr (layer == 0 && PRB == 0) { vv = peek(rs, l0, cnt_off(C_H + (b >> 5) * 128 + (w & 1) * 64 + (b & 31) * 2 + (w >> 1))); SB(); }
#pragma unroll
        for (int i = 0; i < 16; ++i) { aV[i] *= rstd; PIN(aV[i]); }
        SB();
        *(LAS v4u*)(a_v) = pack8(aV, 0);
        *(LAS v4u*)(a_v + 16) = pack8(aV, 1);
        SB();
        if constexpr (layer == 0 && PRB == 1) { vv = peek(rs, l0, cnt_off(C_H + (b >> 5) * 128 + (w & 1) * 64 + (b & 31) * 2 + (w >> 1))); SB(); }
        u32 o[16];
        dual_softmax(p, c, o, [&](auto I) {
          if constexpr (layer == 0) {
            u32 t;
            asm volatile("s_getreg_b32 %1, hwreg(HW_REG_IB_STS, 0, 4)\n\ts_cmp_gt_u32 %1, %2\n\ts_addc_u32 %0, %0, 0" : "+s"(lat), "=&s"(t) : "n"(I > JPK ? 1 : 0) : "scc");
            if constexpr (I == JPK) jv = peek(rs, w != 0 ? 0x80000000u : jvo, 0);
          }
        });
        if constexpr (layer == 0) *(LAS u32*)(L + XS_OFF + w * 4) = lat;
#pragma unroll
        for (int i = 0; i < 4; ++i) *(LAS v4u*)(a_pk + i * 16) = v4u{o[4 * i], o[4 * i + 1], o[4 * i + 2], o[4 * i + 3]};
      }
      SB();
      aQ = mfma32f(csq[2], nmean, zero16);
      STAMP(sb + 3);
      asm volatile("s_waitcnt vmcnt(%0)" ::"n"(wok) : "memory");
      if constexpr (layer == 0) {
        asm volatile("" ::"v"(vv));
        const u32 need = ln == 0 ? 36u : ln == 1 ? 16u : 0u;
        if (w == 0 && (__builtin_amdgcn_ballot_w64(jv >= need) & 7ull) != 7ull) poll3(rs, jvo, need);
      }
      BAR();
      v4u lt;
      if constexpr (layer == 0) { lt = *(LAS v4u*)(L + XS_OFF); sd = 0; }
      const u32 pbase = KVP + (layer * 64 + 4 * hp + w) * 16384, zbase = ZP + (layer * 16 + hp) * 16384 + w * 2048;
      v4u wq[4];
      auto qfrag = [&](int ks) { return WQ[ks * 64]; };
      {
        f32x4 kv = {0.f, 0.f, 0.f, 0.f}, zz = {0.f, 0.f, 0.f, 0.f};
        const v4u ones = {ONES2, ONES2, ONES2, ONES2};
        v2u a0[4], a1[4], b0[4], b1[4];
#pragma unroll
        for (int ks = 0; ks < 4; ++ks) {
          a0[ks] = trread(a_pa + (32 * ks) * PK_STR); a1[ks] = trread(a_pa + (32 * ks + 4) * PK_STR);
          b0[ks] = trread(a_pb + (32 * ks) * VV_STR); b1[ks] = trread(a_pb + (32 * ks + 4) * VV_STR);
          SB();
        }
#pragma unroll
        for (int ks = 0; ks < 4; ++ks) {
          const v4u A = {a0[ks].x, a0[ks].y, a1[ks].x, a1[ks].y}, B = {b0[ks].x, b0[ks].y, b1[ks].x, b1[ks].y};
          kv = mfma16(A, B, kv);
          zz = mfma16(ones, A, zz);
          asm volatile("" : "+a"(zz));
          SB();
        }
#pragma unroll
        for (int ks = 0; ks < 4; ++ks) wq[ks] = qfrag(ks);
        SB();
        const v2u kvb = {pk(kv[0], kv[1]), pk(kv[2], kv[3])};
        st64<SC1>(kvb, rs, o_kv, pbase + rt * 512 + sd);
        st64<SC1>(kvb, rs, o_kv, pbase + rt * 512 + (sd ^ 8192));
        const float z0 = zz[0];
        __builtin_amdgcn_raw_buffer_store_b32(__builtin_bit_cast(u32, z0), rs, o_z, zbase + rt * 128, SC1);
      }
      SB();
      STAMP(sb + 4);
      auto qstep = [&](auto I) {
        constexpr int i = I;
        aQ = mfma32(wq[i & 3], xf[i], aQ);
        SB();
        if constexpr (i + 4 < 32) wq[i & 3] = qfrag(i + 4);
      };
      LAS unsigned char *a_kz, *a_kw, *a_kr;
      u32 o_st, o_zl, l2k;
      v4u sel;
      if constexpr (layer == 0) {
        const int df = __builtin_amdgcn_readfirstlane((int)(lt[0] + lt[2]) - (int)(lt[1] + lt[3]));
        sd = df > 0 ? 8192 : 0;
        const u32 vote = df == 0 ? 1 : (((df > 0) ^ (xcd >> 2)) & 1) * 2;
        (void)__builtin_amdgcn_raw_ptr_buffer_atomic_add_i32(1 + (vote << 16), rs, (w == 0 && j < 4 && ln < 16) ? ln * CSTR : 0x80000000u, cnt_off(C_V), 0);
        SB();
      }
      const u32 skv = cnt_off(C_KV + layer * 128 + hp * 4 + w), ckv = skv + sd;
      u32 pvk[2];
      static_for<32>([&](auto I) {
        constexpr int i2 = I - QS, n2 = 32 - QS;
        if constexpr (I == QS) { wsignal<NQ1 + (layer ? 0 : 1)>(rs, l2k, skv); STAMP(sb + 5); }
        qstep(I);
        if constexpr (I < QS) { if constexpr ((I * NQ1 + QS - 1) / QS != ((I + 1) * NQ1 + QS - 1) / QS) pf(IC(s_q1 + (I * NQ1 + QS - 1) / QS)); }
        else {
          if constexpr (i2 == KVPK1 && KMODE == 0) pvk[0] = peek(rs, l0, ckv);
          if constexpr ((i2 == KVPK1 || i2 == KVPK2) && KMODE == 2) apeek(rs, l0, ckv, pvk[i2 == KVPK2]);
          if constexpr ((i2 * NQ2 + n2 - 1) / n2 != ((i2 + 1) * NQ2 + n2 - 1) / n2) pf(IC(s_q2 + (i2 * NQ2 + n2 - 1) / n2));
        }
        const u32 l2 = opqv(l16) >> 4, g2 = l2 >> 4, c2 = l2 & 15;
        if constexpr (I == 2) l2k = opqv(l2 < 2 ? l2 * 8192 : 0x80000000u);
        if constexpr (I == 1) o_zl = opqv(g2 * 128 + c2 * 4);
        if constexpr (I == 3) a_kz = L + opqv(KF_OFF + w * 544 + (c2 >> 3) * 272 + 256 + (c2 & 7) * 2);
        if constexpr (I == 5) a_kw = L + opqv(KF_OFF + w * 544 + (g2 >> 1) * 272 + (g2 & 1) * 8 + rho32(c2 & 7, c2 >> 3) * 16);
        if constexpr (I == 7) a_kr = L + opqv(KF_OFF + (l2 >> 5) * 272 + ((l2 & 31) < 16 ? (l2 & 31) : 16) * 16);
        if constexpr (I == 9) o_st = opqv(l2 >= 32 ? 0x80000000u : c2 * 16 + g2 * 8);
        if constexpr (I == 11 || I == 13 || I == 15 || I == 17) {
          constexpr int m = (I - 11) / 2;
          sel[m] = opqv((c2 >> 1) == 4 * (g2 & 1) + m ? 0x3f80u << (16 * (c2 & 1)) : 0u);
        }
        SB();
      });
      const v4u q0 = pack8(aQ, 0), q1 = pack8(aQ, 1);
      f32x16 pq = mfma32(wph[0], q0, zero16);
      pq = mfma32(wph[1], q1, pq);
      SB();
      v4u phq[4];
      v4u v[8];
      f32x4 z;
      {
        u32 o[16];
        dual_softmax(pq, c, o, [&](auto I) {
          constexpr int i = I;
          if constexpr (i == KVCHK) {
            STAMP(sb + 6);
            if constexpr (KMODE == 0) wfinish(rs, l0, ckv, 16, pvk[0]);
            else pfinish2<1 + NQ2 - (KVPK1 * NQ2 + 31 - QS) / (32 - QS), NQ2 - (KVPK2 * NQ2 + 31 - QS) / (32 - QS)>(rs, l0, ckv, 16, pvk);
            STAMP(sb + 7);
#pragma unroll
            for (int k = 0; k < 4; ++k) z[k] = __builtin_bit_cast(float, ld32<SC1>(rs, o_zl + k * 512, zbase + sd));
            SB();
          }
          static_for<8>([&](auto K) {
            constexpr int k = K, at = KVCHK + (k + 1) / KVLD;
            if constexpr ((at < 9 ? at : 9) == i) { {
              v[k] = ldf<SC1>(rs, l16, pbase + (sd ^ 8192), k * 1024);
              SB();
              if constexpr (layer == 0 && k == 7) { vv = peek(rs, w == 0 ? l0 : 0x80000000u, cnt_off(C_V + (b & 15))); SB(); }
            } }
          });
        });
#pragma unroll
        for (int m = 0; m < 4; ++m) {
          auto sw = __builtin_amdgcn_permlane32_swap(o[m], o[4 + m], false, false);
          phq[0][m] = sw[0];
          phq[2][m] = sw[1];
          auto sx = __builtin_amdgcn_permlane32_swap(o[8 + m], o[12 + m], false, false);
          phq[1][m] = sx[0];
          phq[3][m] = sx[1];
        }
#pragma unroll
        for (int m = 0; m < 4; ++m) PIN(phq[m]);
      }
      SB();
      {
        const float zs = sum32(sum16((z[0] + z[1]) + (z[2] + z[3])));
        *(LAS unsigned short*)(a_kz) = (unsigned short)pk(zs, zs);
        if constexpr (KMODE == 2) pretire(pvk);
        SB();
        f32x4 sv = {0.f, 0.f, 0.f, 0.f};
#pragma unroll
        for (int i = 0; i < 8; ++i) { sv = mfma16(sel, v[i], sv); SB(); }
        *(LAS v2u*)(a_kw) = v2u{pk(sv[0], sv[1]), pk(sv[2], sv[3])};
      }
      if constexpr (layer == 0) {
        if (w == 0) {
#pragma unroll 1
          for (u32 spins = 0; spins < (1u << 16) && (__builtin_amdgcn_readfirstlane(vv) & 0xffffu) < 32u; ++spins) vv = peek(rs, l0, cnt_off(C_V + (b & 15)));
          if (ln == 0) *(LAS u32*)(L + XS_OFF + 16) = vv;
        }
      }
      BAR();
      if constexpr (layer == 0) vv = *(LAS u32*)(L + XS_OFF + 16);
      STAMP(sb + 8);
      {
        v4u kvf[4];
#pragma unroll
        for (int i = 0; i < 4; ++i) kvf[i] = *(LAS v4u*)(a_kr + i * 544);
        SB();
        f32x16 nd[2];
        nd[0] = mfma32(kvf[0], phq[0], zero16);
        nd[0] = mfma32(kvf[1], phq[1], nd[0]);
        nd[1] = mfma32(kvf[2], phq[2], zero16);
        nd[1] = mfma32(kvf[3], phq[3], nd[1]);
        SB();
        if constexpr (layer == 0) {
          sd = opq(((((u32)__builtin_amdgcn_readfirstlane(vv) >> 16 > 32u ? 1u : 0u) ^ (xcd >> 2) ^ NFLIP) & 1u) * 8192);
        }
#pragma unroll
        for (int h = 0; h < 2; ++h) {
          const float inv = __builtin_amdgcn_rcpf(nd[h][8] + 1e-6f);
          v4u ob;
#pragma unroll
          for (int m = 0; m < 4; ++m) ob[m] = pk(nd[h][2 * m] * inv, nd[h][2 * m + 1] * inv);
          st128<SC1>(ob, rs, l16 + h * 1024, OB + layer * MB2 + (tt & 31) * 65536 + blk(hp * 2048) + (sd ^ (layer * 8192)));
          SB();
        }
      }
      STAMP(sb + 9);
      static_for<ODR>([&](auto I) { pf(IC(s_od + I)); });
      wsignal<ODR>(rs, l0, cnt_off(C_O + layer * 128 + ((tt >> 1) & 15) * 4 + (hp >> 2)) + sd);
      STAMP(sb + 10);
      const u32 co = cnt_off(C_O + layer * 128 + (rt2 & 15) * 4 + w) + sd;
      u32 pvo[3], pvz[2];
      BAR();
      if constexpr (OMODE == 1) {
        apeek(rs, l0, co, pvo[0]);
        NAP(PS2);
        apeek(rs, l0, co, pvo[1]);
        NAP(PS2);
        apeek(rs, l0, co, pvo[2]);
        static_for<n_op>([&](auto I) { pf(IC(s_op + I)); });
        pfinish<2 + n_op, 1 + n_op, n_op>(rs, l0, co, 8, pvo);
      } else if constexpr (OMODE == 2) {
        NAP(PS4);
        apeek(rs, l0, co, pvz[0]);
        static_for<n_op>([&](auto I) { pf(IC(s_op + I)); });
        NAP(PS6);
        apeek(rs, l0, co, pvz[1]);
        pfinish2<1 + n_op, 0>(rs, l0, co, 8, pvz);
      } else {
        NAP(PS4);
        pvo[0] = peek(rs, l0, co);
        SB();
        static_for<n_op>([&](auto I) { pf(IC(s_op + I)); });
        wfinish(rs, l0, co, 8, pvo[0]);
      }
      STAMP(sb + 11);
      const u32 ab = OB + layer * MB2 + w * 16384 + (sd ^ (layer * 8192));
      v4u af[DXV];
      splitk_phase<FrO, DXV, 0>(rs, wo, af, ab + (tte & 31) * 65536, ab + ((tte & 31) ^ 1) * 65536, L, w, l16, 1.f, res, [&](int) {}, [&]() {}, [&](auto I) {
        if constexpr (I == 1 && OMODE == 1) pretire(pvo);
        if constexpr (I == 1 && OMODE == 2) pretire(pvz);
        if constexpr (I >= 4 && s_p2 + I - 4 < s_xd) pf(IC(s_p2 + I - 4));
      });
      STAMP(sb + 12);
      resid_store(rs, res, l16, o_st, xtile(2 * layer, tte) + blk(n0 * 64), stile(2 * layer, tte) + (n0 >> 5) * 256);
      STAMP(sb + 13);
      static_for<XDR>([&](auto I) { pf(IC(s_xd + I)); });
      wsignal<XDR>(rs, l0, (cnt_off(C_X1 + layer * 32 + (tte & 31)) + sd));
      STAMP(sb + 14);
    }
    const u32 lh = opqv(opqv(lane) < nlh ? lane * CSTR : 0x80000000u);
    v4u af[WINH];
    const u32 ch0 = cnt_off(C_H + layer * 512 + (rt2 & 15) * 4 + w) + sd;
    const u32 sh0 = cnt_off(C_H + layer * 512 + ((tt >> 1) & 15) * 4 + (2 * fb) / 7) + sd;
    auto htile = [&](u32 t) { return HB + layer * 4 * MB2 + (t & 31) * 262144 + (sd ^ ((t & 8) * 1024)); };
    auto xtile = [&](int i, u32 t) { return XT + i * MB2 + (t & 31) * 65536 + (sd ^ ((i & 1) ? 0 : 8192)); };
    auto stile = [&](int i, u32 t) { return ST + i * ST_SZ + blk((t & 31) * 4096) + sd; };
    const u32 hown = htile(tte) + w * 16384, hoth = hown ^ 262144;
    {
      const u32 ln = opqv(lane), l16 = ln * 16, r = ln & 31, hf = ln >> 5;
      auto w2ld = [&](auto I) {
        constexpr int i = I, k = (i >> 1) % 7;
        w2f[i >> 1][i & 1] = ld128<SC1>(rs, l16 + (k & 1) * 2048, wb + (i / 14) * 57344 + (k >> 1) * 4096 + ((i & 1) ? 1024 - wsw : wsw));
      };
      auto hld = [&](int i) { if (i < HPRE) { af[i] = ld_fr<FrH>(rs, l16, hown, hoth, i); SB(); } };
      if (p3on) {
        static_for<3>([&](auto J) {
          if constexpr (XMODE == 1) apeek(rs, l0, (cnt_off(C_X1 + layer * 32 + (tt & 31)) + sd), pvx[J]);
          if constexpr (XMODE == 0 && J == (layer ? 0 : 1)) { if constexpr (layer) NAP(PS3 + 1); pvx[0] = peek(rs, l0, (cnt_off(C_X1 + layer * 32 + (tt & 31)) + sd)); }
          if constexpr (XMODE == 3 && J > 0) { if constexpr (layer) NAP(J == 1 ? PS3 + 1 : PS5); apeek(rs, l0, (cnt_off(C_X1 + layer * 32 + (tt & 31)) + sd), pvy[J - 1]); }
          SB();
          if constexpr (layer == 0) static_for<W2D / 3>([&](auto I) { w2ld(IC(J * (W2D / 3) + I)); SB(); });
          else if constexpr (J < 2 && XMODE == 1) NAP(PS2);
        });
        constexpr int DX = DXV;
        v4u xf[32];
        const u32 xb = xtile(2 * layer, tt);
        f32x4 sp[4];
        const u32 stb = stile(2 * layer, tt);
        LAS v4u* WL = (LAS v4u*)(L + l16);
        LAS v4u* WH = (LAS v4u*)(L + opqv(l16 + 65536));
        SB();
        asm volatile("s_waitcnt vmcnt(%0)" ::"n"((layer == 0 ? W2D : 0) + (XMODE == 1 ? 4 : XMODE == 3 ? 3 : XMODE == 0 ? 2 : 1)) : "memory");
        BAR();
        v4u wa[3], wc[3], wq[4];
#pragma unroll
        for (int i = 0; i < 2; ++i) { wa[i] = WL[i * 64]; wc[i] = WL[(32 + i) * 64]; }
        SB();
        STAMP(sb + 15);
        if constexpr (XMODE == 0) wfinish(rs, l0, (cnt_off(C_X1 + layer * 32 + (tt & 31)) + sd), 16, pvx[0]);
        else if constexpr (XMODE == 3) pfinish2<1 + (layer ? 0 : 2 * W2D / 3), (layer ? 0 : W2D / 3)>(rs, l0, (cnt_off(C_X1 + layer * 32 + (tt & 31)) + sd), 16, pvy);
        else if constexpr (XMODE == 2) { if constexpr (layer) NAP(PS3); wwait(rs, l0, (cnt_off(C_X1 + layer * 32 + (tt & 31)) + sd), 16); }
        else pfinish<2 + (layer ? 0 : W2D), 1 + (layer ? 0 : 2 * W2D / 3), (layer ? 0 : W2D / 3)>(rs, l0, (cnt_off(C_X1 + layer * 32 + (tt & 31)) + sd), 16, pvx);
        STAMP(sb + 16);
#pragma unroll
        for (int ks = 0; ks < DX; ++ks) { xf[ks] = ldr(rs, l16, xb, rx, ks); SB(); }
        f32x16 acc[4];
        acc[0] = zero16;
        acc[1] = zero16;
        float nmean, nc, cp[4][4];
        SB();
        static_for<32>([&](auto KSI) {
          constexpr int ks = KSI;
          acc[0] = mfma32(wa[ks % 3], xf[ks], acc[0]);
          SB();
          if constexpr (ks + 2 < 32) wa[(ks + 2) % 3] = WL[(ks + 2) * 64];
          else wq[2 * (ks - 30)] = WH[(2 * (ks - 30)) * 64];
          if constexpr (ks + DX < 32) xf[ks + DX] = ldr(rs, l16, xb, rx, ks + DX);
          if constexpr (layer == 0 && ks >= 20) w2ld(IC(W2D + ks - 20));
          if constexpr (ks == 24) { SB(); acc[1] = mfma32f_pad<5>(cs1[1], nmean, acc[1]); }
          SB();
          acc[1] = mfma32(wc[ks % 3], xf[ks], acc[1]);
          SB();
          if constexpr (ks + 2 < 32) wc[(ks + 2) % 3] = WL[(32 + ks + 2) * 64];
          else wq[2 * (ks - 30) + 1] = WH[(2 * (ks - 30) + 1) * 64];
          if constexpr (ks < 4) sp[ks] = __builtin_bit_cast(f32x4, ld128<SC1>(rs, l16 + ks * 1024, stb));
          if constexpr (layer == 0 && ks >= 4 && ks < 8) {
#pragma unroll
            for (int i = 0; i < 4; ++i) cp[ks - 4][i] = __builtin_bit_cast(float, ld32<SC1>(rs, ln * 4 + i * 256 + (ks - 4) * 1024, CS_W1 + fb * 4096));
          }
          if constexpr (ks == 1 && XMODE == 1) pretire(pvx);
          if constexpr (ks == 1 && XMODE == 3) pretire(pvy);
          if constexpr (ks == 17) stats_fin(sp, nmean, nc);
          if constexpr (ks == 18) { nc *= -LOG2E; PIN(nc); }
          if constexpr (layer == 0 && ks == 20) {
#pragma unroll
            for (int t = 0; t < 4; ++t) cs1[t] = (cp[t][0] + cp[t][1]) + (cp[t][2] + cp[t][3]);
          }
          if constexpr (ks == 21) { SB(); acc[0] = mfma32f_pad<5>(cs1[0], nmean, acc[0]); }
          if constexpr (ks == 26) acc[2] = mfma32f(cs1[2], nmean, zero16);
          if constexpr (ks == 28) acc[3] = mfma32f(cs1[3], nmean, zero16);
          SB();
        });
        STAMP(sb + 17);
        u32 hb[2];
#pragma unroll
        for (int s = 0; s < 2; ++s) hb[s] = opq(htile(tt) + ((2 * fb + s) / 7) * 16384 + ((2 * fb + s) % 7) * 1024);
        float tv[64], ev[64];
        v4u ho[8];
        u32 pv0;
        auto stage = [&](auto V, auto ST_) {
          constexpr int v = V, st = ST_;
          if constexpr (st == 0) { tv[v] = acc[v >> 4][v & 15] * nc; PIN(tv[v]); }
          else if constexpr (st == 1) { ev[v] = __builtin_amdgcn_exp2f(tv[v]); PIN(ev[v]); }
          else if constexpr (st == 2) { ev[v] = 1.f + ev[v]; PIN(ev[v]); }
          else if constexpr (st == 3) { ev[v] = __builtin_amdgcn_rcpf(ev[v]); PIN(ev[v]); }
          else if constexpr (st == 4) { tv[v] = tv[v] * ev[v]; PIN(tv[v]); }
          else if constexpr (v & 1) {
            u32 hh = pk(tv[v - 1], tv[v]);
            PIN(hh);
            ho[v >> 3][(v >> 1) & 3] = hh;
            if constexpr ((v & 7) == 7) st128<SC1>(ho[v >> 3], rs, l16, hb[(v >> 3) & 1] + (v >> 4) * 65536);
          }
        };
        static_for<64>([&](auto GI) {
          constexpr int gi = GI, t = 2 + (gi >> 5), ks = gi & 31;
          acc[t] = mfma32(wq[gi & 3], xf[ks], acc[t]);
          SB();
          if constexpr (gi + 4 < 64) wq[gi & 3] = WH[(gi + 4) * 64];
          if constexpr (layer == 0 && p3_w2gap(gi)) w2ld(IC(W2D + 12 + (gi * W2B) / 8));
          SB();
          static_for<48>([&](auto V) {
            constexpr int d = gi - p3_g0(V);
            if constexpr (d >= 0 && d < 6) stage(V, IC(d));
          });
          if constexpr (gi == HS0) wsignal<p3_younger(layer, 0, HS0)>(rs, lh, sh0);
          if constexpr (gi == HS1) wsignal<p3_younger(layer, 1, HS1)>(rs, lh, sh0 + 128 * CSTR);
          if constexpr (gi == PK0) pv0 = peek(rs, l0, ch0);
          SB();
        });
        STAMP(sb + 18);
        constexpr int W2E = 56 - W2D - 12 - 8 * W2B;
        static_assert(W2E >= 0, "w2 schedule");
        {
          f32x2 t2[8], e2[8];
#pragma unroll
          for (int m = 0; m < 8; ++m) { t2[m] = f32x2{acc[3][2 * m], acc[3][2 * m + 1]} * nc; PIN(t2[m]); }
          SB();
#pragma unroll
          for (int m = 0; m < 8; ++m) { e2[m] = f32x2{__builtin_amdgcn_exp2f(t2[m].x), __builtin_amdgcn_exp2f(t2[m].y)}; PIN(e2[m]); }
          SB();
          if constexpr (HPRE > 0) wfinish(rs, l0, ch0, 8, pv0);
          STAMP(sb + 21);
          hld(0);
#pragma unroll
          for (int m = 0; m < 8; ++m) { e2[m] = e2[m] + 1.f; PIN(e2[m]); }
          SB();
#pragma unroll
          for (int m = 0; m < 8; ++m) { e2[m] = f32x2{__builtin_amdgcn_rcpf(e2[m].x), __builtin_amdgcn_rcpf(e2[m].y)}; PIN(e2[m]); if (m == 1 || m == 5) { SB(); hld(1 + m / 4); } }
          SB();
#pragma unroll
          for (int m = 0; m < 8; ++m) { t2[m] = t2[m] * e2[m]; PIN(t2[m]); }
          SB();
#pragma unroll
          for (int i = 3; i < HPRE; ++i) hld(i);
#pragma unroll
          for (int s = 0; s < 2; ++s)
            st128<SC1>(v4u{pk(t2[4 * s].x, t2[4 * s].y), pk(t2[4 * s + 1].x, t2[4 * s + 1].y), pk(t2[4 * s + 2].x, t2[4 * s + 2].y), pk(t2[4 * s + 3].x, t2[4 * s + 3].y)},
                       rs, l16, hb[s] + 3 * 65536);
        }
        SB();
        wsignal<p3_younger(layer, 2, 64) + HPRE + 2>(rs, lh, sh0 + 256 * CSTR);
        if constexpr (layer == 0) static_for<W2E>([&](auto E) { w2ld(IC(W2D + 12 + 8 * W2B + E)); });
        SB();
        STAMP(sb + 19);
        if constexpr (HPRE == 0) wfinish(rs, l0, ch0, 8, pv0);
      } else {
        if constexpr (layer == 0) static_for<56>([&](auto E) { w2ld(E); });
        DRAIN();
        BAR();
        wwait(rs, l0, ch0, 8);
#pragma unroll
        for (int i = 0; i < HPRE; ++i) hld(i);
      }
    }
    {
      const u32 ln = opqv(lane), l16 = ln * 16, r = ln & 31, hf = ln >> 5;
      const u32 wqb = opq(WQKV_T + hp * 98304 + w * 16384);
      u32 pv1[2], pv2[2], pv3[2];
      splitk_phase<FrH, WINH, HPRE>(rs, w2f, af, hown, hoth, L, w, l16, -LN2, res,
        [&](int i) { if (i == T1A || i == T1B) apeek(rs, l0, ch0 + 128 * CSTR, pv1[i == T1B]); },
        [&]() {
          wsignal<3 + 14 - HPRE>(rs, lh, sh0 + 384 * CSTR);
          STAMP(sb + 22);
          pfinish2<14 - T1A + 2, 14 - T1B + 1>(rs, l0, ch0 + 128 * CSTR, 8, pv1);
          STAMP(sb + 20);
        },
        [&](auto I) {
          constexpr int i = I, g = i - 42;
          if constexpr (i == 15) pretire(pv1);
          constexpr auto ops = [](int a, int b) {
            int n = 0;
            for (int q = 14; q < 56; ++q) n += FrH::lstep(q, WINH) >= a && FrH::lstep(q, WINH) < b;
            for (int q : {PK2, PK2B, PK3, PK3B}) n += q > a && q < b;
            return n;
          };
          static_assert(PK2 < PK2B && PK2B < T2CHK && PK2B != PK3 && PK2 != PK3 && PK3 < PK3B && PK3B < T3CHK && T2CHK >= 28 - WINH && T3CHK >= 35 - WINH, "peek order");
          if constexpr (i == T2CHK) pfinish2<ops(PK2, i), ops(PK2B, i)>(rs, l0, ch0 + 256 * CSTR, 8, pv2);
          if constexpr (i == T3CHK) { STAMP(sb + 27); pfinish2<ops(PK3, i), ops(PK3B, i)>(rs, l0, ch0 + 384 * CSTR, 8, pv3); STAMP(sb + 28); }
          if constexpr (i == PK2 || i == PK2B) apeek(rs, l0, ch0 + 256 * CSTR, pv2[i == PK2B]);
          if constexpr (i == PK3 || i == PK3B) apeek(rs, l0, ch0 + 384 * CSTR, pv3[i == PK3B]);
          if constexpr (i == 29) pretire(pv2);
          if constexpr (i == 36) pretire(pv3);
          if constexpr (KWPOS == 0) {
            if constexpr (layer == 0 && g >= 10 && g <= 14) dma1<g - 10>(rs, l16, wqb, L + w * 16384);
            if constexpr (layer == 0 && g >= 19 && g <= 21) dma1<g - 14>(rs, l16, wqb, L + w * 16384);
          } else {
            if constexpr (layer == 0 && g >= 10 && g < 26) dma1<((g - 10) & 3)>(rs, l16, wqb + ((g - 10) >> 2) * 4096, L + ((w * 16384 + ((g - 10) >> 2) * 4096) ^ rx));
          }
        });
      STAMP(sb + 23);
      if constexpr (layer == 0) {
        resid_store(rs, res, l16, hf ? 0x80000000u : (r & 15) * 16 + (r >> 4) * 8, xtile(1, tte) + blk(n0 * 64), stile(1, tte) + (n0 >> 5) * 256);
        STAMP(sb + 24);
        if constexpr (KWPOS == 0) dma_range<8, 16>(rs, l16, wqb, L + w * 16384);
        wsignal<KWPOS == 0 ? 8 : 0>(rs, l0, (cnt_off(C_X + (tte & 31)) + sd));
        STAMP(sb + 25);
      } else {
        const rsrc_t rso = mkrsrc(out, S * D * 2);
#pragma unroll
        for (int s = 0; s < 2; ++s) st128<OAUX>(res[s], rso, r * 1024 + hf * 16 + s * 32, tte * 32768 + n0 * 2);
        if (b == 255 && tid < 16) st64<SC1>(v2u{tag_lo, tag_hi}, rs, tid * 8, EPOCH_OFF);
        STAMP(sb + 24);
      }
    }
  });
  asm volatile(".Lkcode1:");
}

extern "C" void kernel_launch(void* const* d_in, const int* in_sizes, int n_in, void* d_out, int out_size, void* d_ws,
                              size_t ws_size, hipStream_t stream) {
  (void)hipFuncSetAttribute((const void*)k_main, hipFuncAttributeMaxDynamicSharedMemorySize, LDS_BYTES);
  k_main<<<256, 256, LDS_BYTES, stream>>>((const uint16_t*)d_in[0], (const uint16_t*)d_in[1], (const uint16_t*)d_in[2],
                                          (const uint16_t*)d_in[3], (const uint16_t*)d_in[5], (const uint16_t*)d_in[6],
                                          (unsigned char*)d_ws, (unsigned char*)d_out);
}
```

```cpp
#include <hip/hip_runtime.h>
#include <hip/hip_bf16.h>
#include <stdint.h>
#include <utility>

typedef unsigned int u32;
typedef u32 v4u __attribute__((ext_vector_type(4)));
typedef u32 v2u __attribute__((ext_vector_type(2)));
typedef short s16x4 __attribute__((ext_vector_type(4)));
typedef __bf16 bf16x8 __attribute__((ext_vector_type(8)));
typedef __bf16 bf16x2 __attribute__((ext_vector_type(2)));
typedef float f32x2 __attribute__((ext_vector_type(2)));
typedef float f32x4 __attribute__((ext_vector_type(4)));
typedef float f32x16 __attribute__((ext_vector_type(16)));
#define LAS __attribute__((address_space(3)))
#define GAS __attribute__((address_space(1)))
#define DI __device__ __forceinline__
typedef __amdgpu_buffer_rsrc_t rsrc_t;

#define DXV 12
#define WINH 14
#define HS0 36
#define HS1 56
#define PK0 59
#define HPRE 0
#define PK2 0
#define T1A 0
#define T1B 7
#define PS5 5
#define PK3 9
#define PK2B 6
#define PK3B 16
#define T2CHK 14
#define T3CHK 24
#define KVCHK 0
#define KVLD 2
#define ODR 8
#define OAUX 0
#define PS1 3
#define PS2 4
#define XMODE 3
#define KMODE 2
#define NQ1 11
#define NQ2 10
#define OPL0 4
#define OPL1 4
#define XDR 4
#define OMODE 2
#define PS6 3
#define PS4 2
#define KWPOS 1
#define PS3 2
#define WOK 2
#define CPF 1
#define CPW 1
#define TLBD 0
#define VPK 18
#define PRB 1
#define NFLIP 0
#define ROT2 0
#define QS 11
#define ROT 1
#define XBAR 1
#define JPK 0
#define XA 8
#define XB 20
#define UB 10
#define FPK 4
#define FCK 6
#define KVPK2 14
#define KVPK1 4
#define TLBW 262144
#define W2D 12
#define W2B 4
constexpr int S = 2048, D = 512;
constexpr u32 CTRL = 0;
constexpr u32 WQKV_T = 262144;
constexpr u32 WPHI_T = WQKV_T + 16 * 98304;
constexpr u32 WO_T = WPHI_T + 32768;
constexpr u32 W1_T = WO_T + 524288;
constexpr u32 W2_T = W1_T + 14 * 131072;
constexpr u32 CS_QKV = W2_T + 8 * 4 * 28 * 2048;
constexpr u32 CS_W1 = CS_QKV + 65536;
constexpr u32 MB2 = 2097152;
constexpr u32 XT = 3 * MB2;
constexpr u32 OB = 6 * MB2;
constexpr u32 HB = 8 * MB2;
constexpr u32 ST = 16 * MB2;
constexpr u32 ST_SZ = 262144;
constexpr u32 ZP = ST + 4 * ST_SZ;
constexpr u32 KVP = 17 * MB2;
constexpr u32 WS_END = 18 * MB2;
static_assert(CS_W1 + 65536 <= XT, "workspace map");
constexpr u32 blk(u32 off) { return (off >> 13) * 16384 + (off & 8191); }
constexpr u32 CSTR = 128;
enum { C_H = 0, C_O = 1024, C_X1 = 1280, C_X = 1408, C_J = 1440, C_V = 1504, C_TMO = 1520, C_KV = 1536, NCNT = 1792 };
constexpr u32 INIT_OFF = CTRL + NCNT * CSTR, EPOCH_OFF = INIT_OFF + 16 * CSTR, FLAG_OFF = EPOCH_OFF + CSTR;
static_assert(FLAG_OFF + 192 * CSTR <= WQKV_T, "control area");

#define STAMP(i)
constexpr float LOG2E = 1.4426950408889634f;
constexpr float LN2 = 0.6931471805599453f;

DI u32 pk(float a, float b) {
  f32x2 v = {a, b};
  bf16x2 r = __builtin_convertvector(v, bf16x2);
  return __builtin_bit_cast(u32, r);
}
DI float bflo(u32 u) { return __builtin_bit_cast(float, u << 16); }
DI float bfhi(u32 u) { return __builtin_bit_cast(float, u & 0xffff0000u); }
DI f32x16 mfma32(v4u a, v4u b, f32x16 c) {
  f32x16 d = __builtin_amdgcn_mfma_f32_32x32x16_bf16(__builtin_bit_cast(bf16x8, a), __builtin_bit_cast(bf16x8, b), c, 0, 0, 0);
  asm volatile("" : "+a"(d));
  return d;
}
DI f32x4 mfma16(v4u a, v4u b, f32x4 c) {
  return __builtin_amdgcn_mfma_f32_16x16x32_bf16(__builtin_bit_cast(bf16x8, a), __builtin_bit_cast(bf16x8, b), c, 0, 0, 0);
}
DI f32x16 mfma32f(float a, float b, f32x16 c) {
  f32x16 d = __builtin_amdgcn_mfma_f32_32x32x2f32(a, b, c, 0, 0, 0);
  asm volatile("" : "+a"(d));
  return d;
}
template <int N> DI f32x16 mfma32f_pad(float a, float b, f32x16 c) {
  asm volatile("s_nop %1" : "+a"(c) : "n"(N));
  return mfma32f(a, b, c);
}
DI float dot2(u32 a, u32 b, float c) {
  return __builtin_amdgcn_fdot2_f32_bf16(__builtin_bit_cast(bf16x2, a), __builtin_bit_cast(bf16x2, b), c, false);
}
template <int AUX> DI v4u ld128(rsrc_t rs, u32 voff, u32 soff) { return __builtin_amdgcn_raw_buffer_load_b128(rs, voff, soff, AUX); }
template <int AUX> DI v2u ld64(rsrc_t rs, u32 voff, u32 soff) { return __builtin_amdgcn_raw_buffer_load_b64(rs, voff, soff, AUX); }
template <int AUX> DI u32 ld32(rsrc_t rs, u32 voff, u32 soff) { return __builtin_amdgcn_raw_buffer_load_b32(rs, voff, soff, AUX); }
template <int AUX> DI void st128(v4u v, rsrc_t rs, u32 voff, u32 soff) {
  __builtin_amdgcn_raw_buffer_store_b128(v, rs, voff, soff, AUX);
  asm volatile("s_nop 1" ::"v"(v) : "memory");
}
template <int AUX> DI void st64(v2u v, rsrc_t rs, u32 voff, u32 soff) { __builtin_amdgcn_raw_buffer_store_b64(v, rs, voff, soff, AUX); }
DI rsrc_t mkrsrc(const void* p, u32 bytes) { return __builtin_amdgcn_make_buffer_rsrc((void*)p, 0, (int)bytes, 0x00020000); }
DI float sum32(float v) {
  u32 u = __builtin_bit_cast(u32, v);
  auto r = __builtin_amdgcn_permlane32_swap(u, u, false, false);
  const u32 r0 = r[0], r1 = r[1];
  return __builtin_bit_cast(float, r0) + __builtin_bit_cast(float, r1);
}
DI float sum16(float v) {
  u32 u = __builtin_bit_cast(u32, v);
  auto r = __builtin_amdgcn_permlane16_swap(u, u, false, false);
  const u32 r0 = r[0], r1 = r[1];
  return __builtin_bit_cast(float, r0) + __builtin_bit_cast(float, r1);
}
DI float mix16(float a, float b) {
  auto r = __builtin_amdgcn_permlane16_swap(__builtin_bit_cast(u32, a), __builtin_bit_cast(u32, b), false, false);
  const u32 r0 = r[0], r1 = r[1];
  return __builtin_bit_cast(float, r0) + __builtin_bit_cast(float, r1);
}
DI v2u trread(LAS unsigned char* __restrict__ p) {
  return __builtin_bit_cast(v2u, __builtin_amdgcn_ds_read_tr16_b64_v4i16((LAS s16x4*)p));
}
DI int rho32(int reg, int hf) { return (reg & 3) + 8 * (reg >> 2) + 4 * hf; }
constexpr u32 ONES2 = 0x3f803f80u;

DI v4u pack8(const f32x16& a, int s) {
  v4u r;
#pragma unroll
  for (int m = 0; m < 4; ++m) r[m] = pk(a[8 * s + 2 * m], a[8 * s + 2 * m + 1]);
  return r;
}

#define SB() __builtin_amdgcn_sched_barrier(0)
#define PIN(x) asm volatile("" : "+v"(x))
#define BAR() asm volatile("s_waitcnt lgkmcnt(0)\n\ts_barrier" ::: "memory")
#define DRAIN() asm volatile("s_waitcnt vmcnt(0)" ::: "memory")
#define RLX __ATOMIC_RELAXED, __HIP_MEMORY_SCOPE_AGENT
constexpr int SC1 = 16;

DI u32 opq(u32 x) { asm volatile("" : "+s"(x)); return x; }
DI u32 opqv(u32 x) { asm volatile("" : "+v"(x)); return x; }
DI v4u ldr(rsrc_t rs, u32 vo, u32 sbase, u32 rx, int ks) { return ld128<16>(rs, vo + (ks & 3) * 1024, sbase + blk((ks >> 2) * 4096)); }
template <int AUX> DI v4u ldf(rsrc_t rs, u32 vo, u32 sbase, u32 off) { return ld128<AUX>(rs, vo + (off & 4095u), sbase + (off & ~4095u)); }
template <int I> DI void dma1(rsrc_t rs, u32 vo, u32 sbase, LAS unsigned char* d) {
  __builtin_amdgcn_raw_ptr_buffer_load_lds(rs, (LAS u32*)(d + (I >> 2) * 4096), 16, vo, sbase + (I >> 2) * 4096, (I & 3) * 1024, SC1);
}
template <int I0, int I1> DI void dma_range(rsrc_t rs, u32 vo, u32 sbase, LAS unsigned char* d) {
  if constexpr (I0 < I1) {
    dma1<I0>(rs, vo, sbase, d);
    dma_range<I0 + 1, I1>(rs, vo, sbase, d);
  }
}
template <class F, int... I> DI void static_for_impl(F&& f, std::integer_sequence<int, I...>) { (f(std::integral_constant<int, I>{}), ...); }
template <int N, class F> DI void static_for(F&& f) { static_for_impl(f, std::make_integer_sequence<int, N>{}); }
DI u32 cnt_off(int idx) { return CTRL + idx * CSTR; }
DI u32 lane0_off() { return (threadIdx.x & 63) == 0 ? 0u : 0x80000000u; }
DI void add1(rsrc_t rs, u32 l0, u32 coff) { (void)__builtin_amdgcn_raw_ptr_buffer_atomic_add_i32(1, rs, l0, coff, 0); }
template <int N> DI void wsignal(rsrc_t rs, u32 l0, u32 coff) {
  SB();
  asm volatile("s_waitcnt vmcnt(%0)" ::"n"(N) : "memory");
  add1(rs, l0, coff);
  SB();
}
DI u32 peek(rsrc_t rs, u32 l0, u32 coff) { return __builtin_amdgcn_raw_ptr_buffer_atomic_add_i32(0, rs, l0, coff, 0); }
DI void wfinish(rsrc_t rs, u32 l0, u32 coff, u32 n, u32 pv) {
  if (__builtin_amdgcn_readfirstlane(pv) < n) {
#pragma unroll 1
    for (u32 spins = 0;; ++spins) {
      if (__builtin_amdgcn_readfirstlane(peek(rs, l0, coff)) >= n) break;
      if (spins > (1u << 16)) { __builtin_amdgcn_raw_buffer_store_b32(1u, rs, 0, cnt_off(C_TMO), SC1); break; }
    }
  }
  asm volatile("" ::: "memory");
}
DI void wwait(rsrc_t rs, u32 l0, u32 coff, u32 n) { wfinish(rs, l0, coff, n, peek(rs, l0, coff)); }
DI void apeek(rsrc_t rs, u32 l0, u32 coff, u32& p) {
  SB();
  asm volatile("v_mov_b32 %0, 0\n\tbuffer_atomic_add %0, %1, %2, %3 offen sc0 ; APEEK" : "=&v"(p) : "v"(l0), "s"(rs), "s"(coff) : "memory");
  SB();
}
#define PCHK(P, W) "s_waitcnt vmcnt(" W ")\n\tv_readfirstlane_b32 %3, " P "\n\ts_cmp_ge_u32 %3, %8\n\ts_cbranch_scc1 .Lpd%=\n\tv_mov_b32 " P ", 0\n\tbuffer_atomic_add " P ", %5, %6, %7 offen sc0\n\t"
template <int N0, int N1, int N2> DI void pfinish(rsrc_t rs, u32 l0, u32 coff, u32 n, u32 (&p)[3]) {
  u32 t, cnt;
  SB();
  asm volatile(PCHK("%0", "%9") PCHK("%1", "%10") PCHK("%2", "%11") "s_movk_i32 %4, 0x7fff\n.Lpl%=:\n\t" PCHK("%0", "2") PCHK("%1", "2") PCHK("%2", "2")
               "s_sub_u32 %4, %4, 1\n\ts_cbranch_scc0 .Lpl%=\n.Lpd%=:"
               : "+v"(p[0]), "+v"(p[1]), "+v"(p[2]), "=&s"(t), "=&s"(cnt)
               : "v"(l0), "s"(rs), "s"(coff), "n"(n), "n"(N0), "n"(N1 + 1), "n"(N2 + 2)
               : "memory", "scc");
  SB();
}
#define PCHK2(P, W) "s_waitcnt vmcnt(" W ")\n\tv_readfirstlane_b32 %2, " P "\n\ts_cmp_ge_u32 %2, %7\n\ts_cbranch_scc1 .Lqd%=\n\tv_mov_b32 " P ", 0\n\tbuffer_atomic_add " P ", %4, %5, %6 offen sc0\n\t"
template <int N0, int N1> DI void pfinish2(rsrc_t rs, u32 l0, u32 coff, u32 n, u32 (&p)[2]) {
  u32 t, cnt;
  SB();
  asm volatile(PCHK2("%0", "%8") PCHK2("%1", "%9") "s_movk_i32 %3, 0x7fff\n.Lql%=:\n\t" PCHK2("%0", "1") PCHK2("%1", "1")
               "s_sub_u32 %3, %3, 1\n\ts_cbranch_scc0 .Lql%=\n.Lqd%=:"
               : "+v"(p[0]), "+v"(p[1]), "=&s"(t), "=&s"(cnt)
               : "v"(l0), "s"(rs), "s"(coff), "n"(n), "n"(N0), "n"(N1 + 1)
               : "memory", "scc");
  SB();
}
DI void pretire(u32 (&p)[2]) { asm volatile("; PRETIRE %0 %1" ::"v"(p[0]), "v"(p[1])); }
DI void pretire(u32 (&p)[3]) { asm volatile("; PRETIRE %0 %1 %2" ::"v"(p[0]), "v"(p[1]), "v"(p[2])); }
#define NAP(n) do { SB(); __builtin_amdgcn_s_sleep(n); SB(); } while (0)
DI void poll3(rsrc_t rs, u32 vo, u32 need) {
#pragma unroll 1
  for (u32 spins = 0;; ++spins) {
    const u32 v = peek(rs, vo, 0);
    if ((__builtin_amdgcn_ballot_w64(v >= need) & 7ull) == 7ull) break;
    if (spins > (1u << 16)) { __builtin_amdgcn_raw_buffer_store_b32(1u, rs, 0, cnt_off(C_TMO), SC1); break; }
  }
  asm volatile("" ::: "memory");
}
template <int CTRL_> DI float dpp_add(float v) {
  return v + __builtin_bit_cast(float, __builtin_amdgcn_update_dpp(0, __builtin_bit_cast(int, v), CTRL_, 0xf, 0xf, true));
}

constexpr int SCR = 131072;
constexpr int PK_OFF = SCR, PK_STR = 144, VV_OFF = PK_OFF + 128 * PK_STR, VV_STR = 80, KF_OFF = VV_OFF + 128 * VV_STR;
constexpr int XS_OFF = 98304;
constexpr int RED_OFF = SCR;
constexpr int LDS_BYTES = 163840;
static_assert(KF_OFF + 4 * 544 <= LDS_BYTES, "lds");

DI void lds_put1(LAS unsigned char* p, const f32x16& a, int q) {
  *(LAS f32x4*)(p + q * 1024) = f32x4{a[4 * q], a[4 * q + 1], a[4 * q + 2], a[4 * q + 3]};
}

extern __shared__ __attribute__((aligned(16))) unsigned char smem[];

struct Sch { int put2[4], put3[4], bar1, rd[8], fu[8], put1[4], bar2, rd1, f1[4]; };
constexpr Sch sch_of(int KL) {
  return KL >= 14 ? Sch{{0, 1, 2, 3}, {4, 5, 6, 7}, 9, {9, 9, 9, 10, 10, 10, 11, 11}, {12, 13, 14, 19, 21, 22, 23, 24}, {15, 16, 17, 18}, 20, 20, {25, 26, 27, 27}}
                  : Sch{{0, 0, 1, 1}, {2, 2, 3, 3}, 5, {5, 5, 5, 5, 6, 6, 6, 6}, {7, 7, 8, 8, 11, 12, 12, 13}, {9, 9, 10, 10}, 11, 11, {14, 14, 15, 15}};
}
struct FrO {
  static constexpr int NS = 16, KL = 8;
  static constexpr bool own(int i) { return i >= 8; }
  static constexpr int ks(int i) { return i & 7; }
  static constexpr u32 off(int i) { return (i & 7) * 1024; }
  static constexpr int lstep(int q, int win) { return q - win; }
  static constexpr int first(int win) { return win; }
};
struct FrH {
  static constexpr int NS = 56, KL = 14;
  static constexpr int part(int i) { return i < 28 ? i / 14 : 2 + ((i / 7) & 1); }
  static constexpr bool own(int i) { return i < 28 ? ((i / 7) & 1) : i >= 42; }
  static constexpr int ks(int i) { return part(i) * 7 + i % 7; }
  static constexpr u32 off(int i) { return part(i) * 65536 + (i % 7) * 1024; }
  static constexpr int lstep(int q, int win) {
    const int c = q >= 35 ? T3CHK : q >= 28 ? T2CHK : 0;
    return q - win < c ? c : q - win;
  }
  static constexpr int first(int win) { return 14; }
};
template <class Fr> DI v4u ld_fr(rsrc_t rs, u32 l16, u32 a_own, u32 a_oth, int i) { return ldf<SC1>(rs, l16, Fr::own(i) ? a_own : a_oth, Fr::off(i)); }
template <class Fr, int WIN, int PRE, class Win, class Pre, class Mid>
DI void splitk_phase(rsrc_t rs, const v4u (&wf)[Fr::NS / 2][2], v4u (&af)[WIN], u32 a_own, u32 a_oth, LAS unsigned char* L, int w, u32 l16, float scale,
                     v4u (&res)[2], Win&& win, Pre&& pre_loop, Mid&& mid) {
  const f32x16 zero16 = {0.f, 0.f, 0.f, 0.f, 0.f, 0.f, 0.f, 0.f, 0.f, 0.f, 0.f, 0.f, 0.f, 0.f, 0.f, 0.f};
  constexpr int NS = Fr::NS, KP = Fr::KL, E0 = NS - KP, F0 = Fr::first(WIN);
  constexpr Sch sc = sch_of(KP);
  static_assert(WIN >= KP, "window");
  auto ld = [&](int i) { return ld_fr<Fr>(rs, l16, a_own, a_oth, i); };
#pragma unroll
  for (int i = PRE; i < F0; ++i) { win(i); af[i] = ld(i); SB(); }
  LAS unsigned char* r2 = L + opqv(RED_OFF + l16 + (w ^ 2) * 8192);
  LAS unsigned char* r3 = L + opqv(RED_OFF + l16 + (w ^ 3) * 8192 + 4096);
  LAS unsigned char* r0 = L + opqv(RED_OFF + l16 + w * 8192);
  LAS unsigned char* r1 = L + opqv(RED_OFF + l16 + (w ^ 1) * 8192);
  pre_loop();
  SB();
  f32x16 a0 = zero16, a1 = zero16, a2 = zero16, a3 = zero16;
  float u[16];
  static_for<E0>([&](auto I) {
    constexpr int i = I, ks = Fr::ks(i), e = i - (E0 - 4);
    const v4u bb = af[i % WIN];
    if constexpr (!Fr::own(i)) a2 = mfma32(wf[ks][0], bb, a2);
    else a1 = mfma32(wf[ks][1], bb, a1);
    SB();
    mid(I);
    static_for<NS - F0>([&](auto Q) { if constexpr (Fr::lstep(Q + F0, WIN) == i) { af[(Q + F0) % WIN] = ld(Q + F0); SB(); } });
    SB();
    if constexpr (!Fr::own(i)) a3 = mfma32(wf[ks][1], bb, a3);
    else a0 = mfma32(wf[ks][0], bb, a0);
    SB();
    if constexpr (e >= 0) {
      const u32 ra = res[e >> 1][2 * (e & 1)], rb = res[e >> 1][2 * (e & 1) + 1];
      u[4 * e] = bflo(ra); u[4 * e + 1] = bfhi(ra); u[4 * e + 2] = bflo(rb); u[4 * e + 3] = bfhi(rb);
#pragma unroll
      for (int m = 0; m < 4; ++m) PIN(u[4 * e + m]);
    }
    SB();
  });
  f32x4 p[8];
  auto acc4 = [&](int q, const f32x4& x) {
#pragma unroll
    for (int m = 0; m < 4; ++m) { const float xv = x[m]; u[4 * q + m] = __builtin_fmaf(xv, scale, u[4 * q + m]); PIN(u[4 * q + m]); }
  };
  static_for<2 * KP>([&](auto G) {
    constexpr int g = G, k = g % KP, ks = Fr::ks(E0 + k);
    if constexpr (g < KP) a1 = mfma32(wf[ks][1], af[(E0 + k) % WIN], a1);
    else a0 = mfma32(wf[ks][0], af[(E0 + k) % WIN], a0);
    SB();
    if constexpr (g == sc.bar1 || g == sc.bar2) BAR();
    static_for<8>([&](auto Q) { if constexpr (sc.rd[Q] == g) p[Q] = *(LAS f32x4*)(r0 + Q * 1024); });
    if constexpr (g == sc.rd1) static_for<4>([&](auto Q) { p[Q] = *(LAS f32x4*)(r1 + Q * 1024); });
    SB();
    static_for<4>([&](auto Q) { if constexpr (sc.put2[Q] == g) lds_put1(r2, a2, Q); });
    static_for<4>([&](auto Q) { if constexpr (sc.put3[Q] == g) lds_put1(r3, a3, Q); });
    static_for<4>([&](auto Q) { if constexpr (sc.put1[Q] == g) lds_put1(r0, a1, Q); });
    SB();
    static_for<8>([&](auto Q) { if constexpr (sc.fu[Q] == g) acc4(Q & 3, p[Q]); });
    static_for<4>([&](auto Q) { if constexpr (sc.f1[Q] == g) acc4(Q, p[Q]); });
    SB();
    mid(std::integral_constant<int, E0 + g>{});
    SB();
  });
#pragma unroll
  for (int q = 0; q < 4; ++q) {
    acc4(q, f32x4{a0[4 * q], a0[4 * q + 1], a0[4 * q + 2], a0[4 * q + 3]});
    res[q >> 1][2 * (q & 1)] = pk(u[4 * q], u[4 * q + 1]);
    res[q >> 1][2 * (q & 1) + 1] = pk(u[4 * q + 2], u[4 * q + 3]);
  }
}
DI void resid_store(rsrc_t rs, const v4u (&res)[2], u32 l16, u32 sto, u32 xoff, u32 soff) {
#pragma unroll
  for (int s = 0; s < 2; ++s) st128<SC1>(res[s], rs, l16 + s * 1024, xoff);
  SB();
  float s0 = 0.f, q0 = 0.f, s1 = 0.f, q1 = 0.f;
#pragma unroll
  for (int m = 0; m < 4; ++m) {
    s0 = dot2(res[0][m], ONES2, s0); q0 = dot2(res[0][m], res[0][m], q0);
    s1 = dot2(res[1][m], ONES2, s1); q1 = dot2(res[1][m], res[1][m], q1);
  }
  st64<SC1>(__builtin_bit_cast(v2u, f32x2{sum32(s0 + s1), sum32(q0 + q1)}), rs, sto, soff);
}

DI void stats_fin(const f32x4 (&sp)[4], float& nmean, float& rstd) {
  float t[4];
#pragma unroll
  for (int m = 0; m < 4; ++m) { t[m] = sp[0][m] + sp[1][m]; PIN(t[m]); }
#pragma unroll
  for (int i = 2; i < 4; ++i)
#pragma unroll
    for (int m = 0; m < 4; ++m) { t[m] += sp[i][m]; PIN(t[m]); }
  const float s = mix16(t[0], t[2]), q = mix16(t[1], t[3]);
  nmean = sum32(s) * (-1.f / 512.f);
  const float var = fmaxf(sum32(q) * (1.f / 512.f) - nmean * nmean, 0.f);
  rstd = __builtin_amdgcn_rsqf(var + 1e-5f);
  PIN(nmean);
  PIN(rstd);
}

template <class Hook> DI void dual_softmax(const f32x16& p, float c, u32 (&o)[16], Hook&& hook) {
  float mx = p[0], mn = p[0];
#pragma unroll
  for (int i = 1; i < 16; ++i) { mx = fmaxf(mx, p[i]); mn = fminf(mn, p[i]); }
  float ap = -mx * c, an = mn * c;
  PIN(ap);
  PIN(an);
  SB();
  hook(std::integral_constant<int, 0>{});
  SB();
  f32x2 ep[8], en[8];
#pragma unroll
  for (int i = 0; i < 8; ++i) {
    const f32x2 pv = {p[2 * i], p[2 * i + 1]};
    ep[i] = pv * c + ap;
    en[i] = pv * (-c) + an;
    PIN(ep[i]);
    PIN(en[i]);
  }
  SB();
  hook(std::integral_constant<int, 1>{});
  SB();
  f32x2 sa, sc;
  static_for<8>([&](auto I) {
    constexpr int i = I;
    ep[i] = f32x2{__builtin_amdgcn_exp2f(ep[i].x), __builtin_amdgcn_exp2f(ep[i].y)};
    en[i] = f32x2{__builtin_amdgcn_exp2f(en[i].x), __builtin_amdgcn_exp2f(en[i].y)};
    PIN(ep[i]);
    PIN(en[i]);
    if constexpr (i == 2) { sa = ep[0] + ep[1]; sc = en[0] + en[1]; PIN(sa); PIN(sc); }
    if constexpr (i > 2) { sa += ep[i - 1]; sc += en[i - 1]; PIN(sa); PIN(sc); }
    SB();
    hook(std::integral_constant<int, 2 + i>{});
    SB();
  });
  sa += ep[7];
  sc += en[7];
  const float rp = __builtin_amdgcn_rcpf(sa.x + sa.y), rn = __builtin_amdgcn_rcpf(sc.x + sc.y);
#pragma unroll
  for (int i = 0; i < 8; ++i) { ep[i] *= rp; PIN(ep[i]); }
#pragma unroll
  for (int i = 0; i < 8; ++i) { en[i] *= rn; PIN(en[i]); }
  SB();
#pragma unroll
  for (int i = 0; i < 8; ++i) { o[i] = pk(ep[i].x, ep[i].y); o[8 + i] = pk(en[i].x, en[i].y); }
}
constexpr int p3_g0(int v) { return (v * 58) / 48; }
constexpr bool p3_w2gap(int gi) { return (gi * W2B) / 8 != ((gi + 1) * W2B) / 8; }
constexpr int p3_pre(int layer, int g) {
  int n = layer == 0 && p3_w2gap(g);
  for (int q = 0; q < 6; ++q) n += p3_g0(8 * q + 7) + 5 == g;
  return n;
}
constexpr int p3_post(int g) { return (g == HS0) + (g == HS1) + (g == PK0); }
constexpr int p3_younger(int layer, int t, int gw) {
  const int gs = p3_g0(16 * t + 15) + 5;
  int n = p3_post(gs);
  for (int g = gs + 1; g < gw; ++g) n += p3_pre(layer, g) + p3_post(g);
  return n + (gw < 64 ? p3_pre(layer, gw) : 0);
}
static_assert(p3_g0(32) >= 35 && p3_g0(47) + 5 < 64 && p3_g0(15) + 5 < HS0 && p3_g0(31) + 5 < HS1 && HS0 != HS1, "P3 schedule");

__global__ void __launch_bounds__(256, 1)
k_main(const uint16_t* __restrict__ x, const uint16_t* __restrict__ wqkv, const uint16_t* __restrict__ wphi,
       const uint16_t* __restrict__ wo_, const uint16_t* __restrict__ w1, const uint16_t* __restrict__ w2,
       unsigned char* ws, unsigned char* out) {
  LAS unsigned char* L = (LAS unsigned char*)smem;
  const int b = blockIdx.x, tid = threadIdx.x, lane = tid & 63, w = __builtin_amdgcn_readfirstlane(tid >> 6);
  const int xcd = b & 7, j = b >> 3;
  const int rt = 4 * (xcd >> 1) + (j >> 3), hp = 8 * (xcd & 1) + (j & 7), tt = 4 * rt + w;
  const bool p3on = (j & 7) < 7;
  const int fb = p3on ? 7 * (xcd & 1) + (j & 7) : 0;
  const int rt2 = 4 * xcd + (j >> 3), cb = j & 7;
  const int tte = 2 * rt2 + (w >> 1), n0 = 64 * cb + 32 * (w & 1);
  const rsrc_t rs = mkrsrc((unsigned char*)(((size_t)ws + MB2 - 1) & ~(size_t)(MB2 - 1)), WS_END);
  const f32x16 zero16 = {0.f, 0.f, 0.f, 0.f, 0.f, 0.f, 0.f, 0.f, 0.f, 0.f, 0.f, 0.f, 0.f, 0.f, 0.f, 0.f};
  STAMP(63);
  v4u res[2];
  float csq[3], cs1[4];
  v4u wph[2];
  u32 tag_lo, tag_hi, tws = 0;
  u32 sd;
  const u32 l0 = lane0_off();
  const u32 nlh = !p3on ? 0u : (fb == 3 || fb == 10) ? 2u : 1u;
  v4u xr[32];
  const uint16_t* xo = x;
  asm volatile("" : "+s"(xo));
  const rsrc_t rsx = mkrsrc(xo, S * D * 2);
  auto trfrag = [&](LAS unsigned char* p, int str) { const v2u t0 = trread(p), t1 = trread(p + str); return v4u{t0.x, t0.y, t1.x, t1.y}; };
  const u32 sv = ((lane >> 2) & 1) == ((lane >> 4) & 1) ? ONES2 : 0u;
  v4u selm = {sv, sv, sv, sv};
  if (b >= 248 && w < 2) {
#pragma unroll
    for (int i = 0; i < NCNT * CSTR / 16384; ++i) st128<SC1>(v4u{0u, 0u, 0u, 0u}, rs, lane * 16, CTRL + ((b - 248) * 2 + w) * (NCNT * CSTR / 16) + i * 1024);
  }
  const v2u ep = ld64<SC1>(rs, 0, EPOCH_OFF);
  SB();
  u32 cpf;
  {
    unsigned long long pc;
    u32 len;
    asm volatile("s_getpc_b64 %0\n.Lkcode0:\n\ts_mov_b32 %1, .Lkcode1-.Lkcode0" : "=s"(pc), "=s"(len));
    cpf = ld32<0>(mkrsrc((const void*)pc, len), w == CPW ? lane * 128 + (j & 7) * 8192 : 0x80000000u, 0);
  }
  SB();
  int kind = 4, nb = 0, kq = 0, ld = 512;
  const uint16_t* W = x;
  if (w == 0) {
    if (b < 192) { kind = 0; W = wqkv; ld = 1536; nb = b >> 3; kq = b & 7; }
  } else if (w >= 2) {
    const int hw = w & 1;
    if (b < 112) { kind = 1; W = w1; ld = 1792; nb = b >> 2; kq = (b & 3) * 2 + hw; }
    else if (b < 144) { kind = 2; W = wo_; nb = (b - 112) >> 2; kq = (b & 3) * 2 + hw; }
    else { kind = 3; W = w2; nb = (b - 144) / 14; kq = ((b - 144) - 14 * nb) * 2 + hw; }
  }
  LAS unsigned char* img = L + SCR + w * 8192;
  {
    const rsrc_t rsu = mkrsrc(W, kind < 4 ? (kind == 3 ? 1792 : 512) * ld * 2 : 0);
    const u32 vu = (lane >> 3) * ld * 2 + (((lane & 7) ^ ((lane >> 2) & 4)) * 16);
#pragma unroll
    for (int i = 0; i < 8; ++i) __builtin_amdgcn_raw_ptr_buffer_load_lds(rsu, (LAS u32*)(img + i * 1024), 16, vu, (64 * kq + 8 * i) * ld * 2 + nb * 128, 0, 0);
  }
  SB();
  const u32 vx = (lane >> 3) * 1024 + (lane & 7) * 16;
  const u32 rot = ROT ? j & 7 : 0;
  auto xld = [&](auto I) { constexpr int i = I; xr[i] = ld128<0>(rsx, vx, tt * 32768 + (i & 3) * 8192 + (((i >> 2) ^ rot) * 128)); SB(); };
  static_for<XA>([&](auto I) { xld(I); });
  u32 obase, cstr, kstr, kstr2, csb, ovo, cvo, ua;
  {
    const int t32 = 2 * nb, sec = t32 >> 4, tile = sec == 0 ? 2 : sec - 1;
    obase = opq(kind == 0 ? WQKV_T + (t32 & 15) * 98304 + tile * 32768 + kq * 4096 : kind == 1 ? W1_T + t32 * 32768 + kq * 4096
                : kind == 2 ? WO_T + (nb * 32 + kq * 4) * 2048 : W2_T + (nb * 112 + (kq & 1) * 56 + (kq >> 1) * 2) * 2048);
    cstr = opq(kind == 0 ? 98304 : kind == 1 ? 32768 : 1024);
    kstr = opq(kind < 2 ? 1024 : 2048);
    kstr2 = opq(kind == 3 ? 57344 : 2 * kstr);
    csb = opq(kind == 0 ? CS_QKV + (((t32 & 15) * 3 + tile) * 8 + kq) * 128 : CS_W1 + (t32 * 8 + kq) * 128);
    ovo = opqv(kind < 4 ? lane * 16 : 0x80000000u);
    cvo = opqv(kind < 2 ? (lane >> 5) * (kind == 0 ? 3072 : 1024) + (lane & 31) * 4 : 0x80000000u);
    const u32 c16 = lane & 15, Q = ((lane >> 2) & 4) + (lane & 3);
    const u32 Cc = kind == 0 ? 4 * (Q & 1) + (Q >> 1) : (Q & 4) + 2 * (Q & 1) + ((Q >> 1) & 1);
    ua = opqv((8 * (lane >> 5) + (c16 >> 2)) * 128 + (c16 >> 3) * 64 + Cc * 8);
  }
  const u32 jadd = opq(cnt_off(C_J + (b < 112 ? 16 + (b >> 3) : b < 144 ? (b - 112) >> 2 : (b - 144) / 14)));
  auto unit = [&]() {
    v4u o[8];
#pragma unroll
    for (int i = 0; i < 8; ++i) o[i] = trfrag(img + (ua ^ ((i >> 2) * 64)) + (i & 3) * 2048, 512);
    SB();
    f32x4 cs[2] = {{0.f, 0.f, 0.f, 0.f}, {0.f, 0.f, 0.f, 0.f}};
#pragma unroll
    for (int i = 0; i < 8; ++i) {
      st128<SC1>(o[i], rs, ovo, obase + (i >> 2) * cstr + ((i >> 1) & 1) * kstr2 + (i & 1) * kstr);
      cs[i >> 2] = mfma16(selm, o[i], cs[i >> 2]);
    }
    const float c0 = cs[0][0], c1 = cs[1][0];
    __builtin_amdgcn_raw_buffer_store_b32(__builtin_bit_cast(u32, lane < 32 ? c0 : c1), rs, cvo, csb, SC1);
    SB();
  };
  SB();
  const u32 tvo = opqv((((b >= 248 && w < 2) || kind == 0) && lane < 16) ? lane * 8 : 0x80000000u);
  const u32 tso = opq(b >= 248 ? INIT_OFF + (((b & 7) * 2 + w) & 15) * CSTR : FLAG_OFF + b * CSTR);
  SB();
  v4u w2f[28][2];

  static_for<2>([&](auto LAYER) __attribute__((always_inline)) {
    constexpr int layer = LAYER;
    constexpr int sb = layer * 29;
    v4u wo[8][2];
    const u32 wb = opq(W2_T + cb * 229376 + w * 14336);
    const u32 wsw = (w & 1) * 1024;
    u32 pvx[3], pvy[2];
    const u32 rx = ROT2 ? opq(rot * 4096) : 0u;
#define IC(n) std::integral_constant<int, n>{}
    {
      const u32 ln = opqv(lane), l16 = ln * 16, r = ln & 31, hf = ln >> 5;
      if constexpr (layer) STAMP(sb + 0);
      constexpr int DX = DXV;
      v4u xf[32];
      auto xtile = [&](int i, u32 t) { return XT + i * MB2 + (t & 31) * 65536 + (sd ^ ((i & 1) ? 0 : 8192)); };
      auto stile = [&](int i, u32 t) { return ST + i * ST_SZ + blk((t & 31) * 4096) + sd; };
      const u32 xb = layer ? xtile(1, tt) : 0;
      if constexpr (layer > 0 && XMODE == 1) apeek(rs, l0, (cnt_off(C_X + (tt & 31)) + sd), pvx[0]);
      SB();
      f32x4 sp[4];
      const u32 stb = layer ? stile(1, tt) : 0;
      const u32 wqb = opq(WQKV_T + hp * 98304 + 65536 + w * 8192);
      const u32 g4 = ln >> 4, c16 = ln & 15;
      LAS unsigned char* a_v = L + opqv(VV_OFF + (32 * w + r) * VV_STR + hf * 32);
      LAS unsigned char* a_pk = L + opqv(PK_OFF + (32 * w + r) * PK_STR + hf * 64);
      LAS unsigned char* a_pa = L + opqv(PK_OFF + (8 * g4 + (c16 >> 2)) * PK_STR + w * 32 + (ln & 3) * 8);
      LAS unsigned char* a_pb = L + opqv(VV_OFF + (8 * g4 + (c16 >> 2)) * VV_STR + (w >> 1) * 32 + (ln & 3) * 8);
      const u32 o_kv = opqv((g4 >> 1) * 256 + c16 * 16 + (g4 & 1) * 8), o_z = opqv(hf * 8192 + r * 4);
      u32 jv = 0, lat = 0, vv = 0;
      constexpr int wok = layer ? WOK : 0, NPF = 16 - wok + (layer ? 24 : 32);
      constexpr int s_q1 = 0, s_q2 = s_q1 + NQ1, s_od = s_q2 + NQ2, s_op = s_od + ODR, n_op = layer ? OPL1 : OPL0, s_p2 = s_op + n_op, s_xd = NPF - XDR;
      static_assert(s_p2 <= s_xd && s_xd - s_p2 <= 12 && s_od <= NPF - 8 && s_op >= 16 - wok, "prefetch schedule");
      LAS v4u* WL = (LAS v4u*)(L + l16);
      LAS v4u* WQ = (LAS v4u*)(L + opqv(l16 + 65536));
      const u32 wob = opq(WO_T + (cb * 4 + w) * 16384);
      auto wold = [&](auto I) {
        constexpr int i = I;
        wo[i >> 1][i & 1] = ld128<SC1>(rs, l16 + ((i >> 1) & 1) * 2048, wob + (i >> 2) * 4096 + ((i & 1) ? 1024 - wsw : wsw));
      };
      f32x16 aK = zero16, aV = zero16, aQ;
      float rstd, nmean, c;
      const u32 jvo = opqv(ln == 0 ? cnt_off(C_J + cb) : ln == 1 ? cnt_off(C_J + 16 + fb) : 0x80000000u);
      float cp[3][4];
      v4u wk[3], wv[3];
      SB();
      if constexpr (layer == 0) {
        LAS unsigned char* sw[2];
        LAS unsigned char* sr[4];
#pragma unroll
        for (int i = 0; i < 2; ++i) sw[i] = L + opqv(XS_OFF + w * 8192 + (ln >> 3) * 128 + (((ln & 7) ^ (4 * i + (ln >> 4))) * 16));
#pragma unroll
        for (int f = 0; f < 4; ++f) sr[f] = L + opqv(XS_OFF + w * 8192 + r * 128 + (((2 * f + hf) ^ ((r >> 1) & 7)) * 16));
        const u32 fu = ln >> 1, fvo = opqv(ln >= 12 ? 0x80000000u : FLAG_OFF + ((fu < 4 ? (1 + (w >> 1)) * 64 + (w & 1) * 4 + fu : 2 * w + fu - 4) + (hp >> 1) * 8) * CSTR + (ln & 1) * 4);
        PIN(selm);
        SB();
        if (w == 0) {
          asm volatile("s_waitcnt vmcnt(%0)" ::"n"(XA) : "memory");
          STAMP(58);
          unit();
          STAMP(59);
        }
        asm volatile("s_barrier" ::: "memory");
        {
          const unsigned long long e = ((unsigned long long)__builtin_amdgcn_readfirstlane(ep.y) << 32) | __builtin_amdgcn_readfirstlane(ep.x);
          unsigned long long t = e * 0x9E3779B97F4A7C15ull + 0x632BE59BD9B4E019ull;
          if (t == e) t = ~e;
          tag_lo = (u32)t;
          tag_hi = (u32)(t >> 32);
        }
        const u32 fneed = (ln & 1) ? tag_hi : tag_lo;
        f32x4 sa = {0.f, 0.f, 0.f, 0.f};
        f32x16 gr = zero16;
        u32 fv;
        v2u tg;
        SB();
        auto vm = [&](auto N) {
          constexpr int n = N, xi = XA + n, di = n - 8 * FCK;
          if constexpr (xi < 32) xld(IC(xi));
          if constexpr (xi == 32) __builtin_amdgcn_raw_ptr_buffer_load_lds(mkrsrc(wphi, 16384), (LAS u32*)(L + XS_OFF + w * 8192 + 4096), 16, l16, hp * 1024, 0, 0);
          if constexpr (xi == 33) tws = ld32<0>(rs, TLBW ? (ln + 64 * w + (TLBD ? 256 * j : 0)) * TLBW : 0x80000000u, 0);
          if constexpr (xi == XB) {
            if (w == 0) asm volatile("s_waitcnt vmcnt(%0)" ::"n"(XB - XA + 1) : "memory");
            st64<SC1>(v2u{tag_lo, tag_hi}, rs, tvo, tso);
            STAMP(60);
          }
          if constexpr (di >= 0 && di < 16)
            dma1<(di & 3)>(rs, l16, WQKV_T + hp * 98304 + w * 16384 + (di >> 2) * 4096, L + (w >> 1) * 32768 + ((((w & 1) * 4 + (di >> 2)) ^ rot) * 4096));
          if constexpr (di == 16) tg = ld64<SC1>(rs, c16 * CSTR, INIT_OFF);
          SB();
        };
        static_assert(XA + 8 * FCK > 33 && XB < 32 && FCK <= 7 && UB >= FCK + 3 && XA >= 4, "start schedule");
        static_for<11>([&](auto B) {
          constexpr int blk = B;
          if constexpr (blk == UB) { if (w >= 2) unit(); }
          if constexpr (blk == FPK) { fv = peek(rs, fvo, 0); SB(); }
          if constexpr (blk == FCK) {
            STAMP(0);
            if ((__builtin_amdgcn_ballot_w64(fv == fneed) & 0xfffull) != 0xfffull) {
#pragma unroll 1
              for (u32 spins = 0; spins < (1u << 16); ++spins)
                if ((__builtin_amdgcn_ballot_w64(peek(rs, fvo, 0) == fneed) & 0xfffull) == 0xfffull) break;
            }
            asm volatile("" ::: "memory");
            STAMP(61);
          }
          if constexpr (blk < 10) static_for<4>([&](auto I) {
            constexpr int i = I;
            if constexpr (blk > 1) gr = mfma32(xf[4 * blk - 8 + i], xf[4 * blk - 8 + i], gr);
            SB();
            vm(IC(8 * blk + 2 * i));
            if constexpr (blk < 8) *(LAS v4u*)(sw[i & 1] + i * 1024) = xr[4 * blk + i];
            SB();
            if constexpr (blk > 1) { sa = mfma16(selm, xf[4 * blk - 8 + i], sa); asm volatile("" : "+a"(sa)); }
            SB();
            vm(IC(8 * blk + 2 * i + 1));
          });
          if constexpr (blk < 8) static_for<4>([&](auto F) { xf[4 * blk + F] = *(LAS v4u*)(sr[F]); SB(); });
        });
        asm volatile("" ::"v"(tws));
        asm volatile("" ::"v"(cpf));
        {
          const u32 Qc = 4 * (g4 & 1) + (c16 & 3);
          LAS unsigned char* a_ph = L + opqv(XS_OFF + w * 8192 + 4096 + hf * 512 + (c16 >> 2) * 32 + (Qc >> 1) * 8);
#pragma unroll
          for (int s = 0; s < 2; ++s) wph[s] = trfrag(a_ph + s * 256, 128);
        }
        const float sa0 = sa[0];
        nmean = sa0 * (-1.f / 512.f);
        auto sel = [](bool cnd, float a0, float a1) { PIN(a0); PIN(a1); return cnd ? a1 : a0; };
        auto g4f = [&](int i) { return sel(r & 2, sel(r & 1, gr[4 * i], gr[4 * i + 1]), sel(r & 1, gr[4 * i + 2], gr[4 * i + 3])); };
        const float gd = sel(r & 16, sel(r & 8, g4f(0), g4f(1)), sel(r & 8, g4f(2), g4f(3)));
        const bool dl = ((r >> 2) & 1) == hf;
        const float dg = dl ? gd : 0.f;
        rstd = __builtin_amdgcn_rsqf(fmaxf(sum32(dg) * (1.f / 512.f) - nmean * nmean, 0.f) + 1e-5f);
        c = rstd * LOG2E;
#pragma unroll
        for (int s = 0; s < 2; ++s)
#pragma unroll
          for (int m = 0; m < 4; ++m) wph[s][m] = dl ? wph[s][m] : 0u;
        PIN(wph[0]);
        PIN(wph[1]);
        PIN(c);
        PIN(rstd);
        PIN(nmean);
        SB();
        if constexpr (UB > FCK) { if (w >= 2) asm volatile("s_waitcnt vmcnt(9)" ::: "memory"); else DRAIN(); }
        else DRAIN();
        if (w == 0 && !__all(tg.x == tag_lo && tg.y == tag_hi)) {
#pragma unroll 1
          for (u32 spins = 0; spins < (1u << 16); ++spins) {
            asm volatile("" ::: "memory");
            const v2u t = ld64<SC1>(rs, c16 * CSTR, INIT_OFF);
            if (__all(t.x == tag_lo && t.y == tag_hi)) break;
          }
        }
        BAR();
#pragma unroll
        for (int i = 0; i < 2; ++i) { wk[i] = WL[i * 64]; wv[i] = WL[(32 + i) * 64]; }
        if constexpr (UB <= FCK) add1(rs, w >= 2 ? l0 : 0x80000000u, jadd);
        SB();
      } else {
        if constexpr (XMODE == 0) { NAP(PS3); pvx[0] = peek(rs, l0, (cnt_off(C_X + (tt & 31)) + sd)); SB(); }
        if constexpr (XMODE == 3) { NAP(PS3); apeek(rs, l0, (cnt_off(C_X + (tt & 31)) + sd), pvy[0]); NAP(PS5); apeek(rs, l0, (cnt_off(C_X + (tt & 31)) + sd), pvy[1]); }
        if constexpr (XMODE == 1) { NAP(PS1); apeek(rs, l0, (cnt_off(C_X + (tt & 31)) + sd), pvx[1]); NAP(PS2); apeek(rs, l0, (cnt_off(C_X + (tt & 31)) + sd), pvx[2]); SB(); }
        asm volatile("s_waitcnt vmcnt(%0)" ::"n"(XMODE == 1 ? 4 : XMODE == 3 ? 3 : XMODE == 0 ? 2 : 1) : "memory");
        BAR();
#pragma unroll
        for (int i = 0; i < 2; ++i) { wk[i] = WL[i * 64]; wv[i] = WL[(32 + i) * 64]; }
        SB();
        if constexpr (XMODE == 1) pfinish<2, 1, 0>(rs, l0, (cnt_off(C_X + (tt & 31)) + sd), 16, pvx);
        else if constexpr (XMODE == 3) pfinish2<1, 0>(rs, l0, (cnt_off(C_X + (tt & 31)) + sd), 16, pvy);
        else if constexpr (XMODE == 0) wfinish(rs, l0, (cnt_off(C_X + (tt & 31)) + sd), 16, pvx[0]);
        else { NAP(PS3); wwait(rs, l0, (cnt_off(C_X + (tt & 31)) + sd), 16); }
        STAMP(sb + 1);
#pragma unroll
        for (int ks = 0; ks < DX; ++ks) { xf[ks] = ldr(rs, l16, xb, rx, ks); SB(); }
      }
      STAMP(sb + 26);
      static_for<32>([&](auto KSI) {
        constexpr int ks = KSI, q0 = layer ? 20 : 8;
        aK = mfma32(wk[ks % 3], xf[ks], aK);
        SB();
        if constexpr (ks + 2 < 32) wk[(ks + 2) % 3] = WL[(ks + 2) * 64];
        if constexpr (layer > 0 && ks + DX < 32) xf[ks + DX] = ldr(rs, l16, xb, rx, ks + DX);
        if constexpr (ks >= q0 && ks < q0 + 8) dma1<((ks - q0) & 3)>(rs, l16, wqb + ((ks - q0) >> 2) * 4096, L + 65536 + (((2 * w + ((ks - q0) >> 2)) ^ (layer && !ROT2 ? 0 : rot)) * 4096));
        if constexpr (ks >= 28 && ks < 28 + wok) wold(IC(ks - 28));
        if constexpr (layer == 0 && ks < 6) cp[ks >> 1][2 * (ks & 1)] = __builtin_bit_cast(float, ld32<SC1>(rs, ln * 4 + (ks & 1) * 512 + (ks >> 1) * 1024, CS_QKV + hp * 3072));
        if constexpr (ks == 23) { SB(); aV = mfma32f_pad<5>(csq[1], nmean, aV); }
        SB();
        aV = mfma32(wv[ks % 3], xf[ks], aV);
        SB();
        if constexpr (ks + 2 < 32) wv[(ks + 2) % 3] = WL[(32 + ks + 2) * 64];
        if constexpr (layer == 0 && ks < 6) cp[ks >> 1][2 * (ks & 1) + 1] = __builtin_bit_cast(float, ld32<SC1>(rs, ln * 4 + (ks & 1) * 512 + 256 + (ks >> 1) * 1024, CS_QKV + hp * 3072));
        if constexpr (layer == 0 && (ks == 18 || ks == 22)) res[(ks - 18) >> 2] = ld128<0>(rsx, r * 1024 + hf * 16 + ((ks - 18) >> 2) * 32, tte * 32768 + n0 * 2);
        if constexpr (layer == 0 && ks == 19) {
#pragma unroll
          for (int t = 0; t < 3; ++t) { csq[t] = (cp[t][0] + cp[t][1]) + (cp[t][2] + cp[t][3]); PIN(csq[t]); }
        }
        if constexpr (layer == 0 && ks == 19 && UB > FCK) add1(rs, w >= 2 ? l0 : 0x80000000u, jadd);
        if constexpr (layer > 0) {
          if constexpr (ks < 4) sp[ks] = __builtin_bit_cast(f32x4, ld128<SC1>(rs, l16 + ks * 1024, stb));
          if constexpr (ks == 1 && XMODE == 1) pretire(pvx);
          if constexpr (ks == 1 && XMODE == 3) pretire(pvy);
          if constexpr (ks == 17) stats_fin(sp, nmean, rstd);
          if constexpr (ks == 18) { c = rstd * LOG2E; PIN(c); }
        }
        if constexpr (ks == 20) { SB(); aK = mfma32f_pad<5>(csq[0], nmean, aK); }
        SB();
      });
      STAMP(sb + 2);
      const u32 w1b = opq(W1_T + fb * 131072);
      auto w1dma = [&](auto I) {
        constexpr int i = I;
        const u32 o = i < 16 ? w * 16384 + (i >> 2) * 4096 : (i < 24 ? 98304 : 65536) + w * 8192 + ((i >> 2) & 1) * 4096;
        dma1<(i & 3)>(rs, l16, w1b + o, L + (o ^ rx));
      };
      auto pf = [&](auto N) {
        constexpr int n = N + wok;
        if constexpr (n < 16) wold(IC(n));
        else w1dma(IC(n - 16 + (layer && n >= 32 ? 8 : 0)));
        SB();
      };
      {
        const v4u k0 = pack8(aK, 0), k1 = pack8(aK, 1);
        f32x16 p = mfma32(wph[0], k0, zero16);
        p = mfma32(wph[1], k1, p);
        SB();
        if constexpr (layer == 0 && PRB == 0) { vv = peek(rs, l0, cnt_off(C_H + (b >> 5) * 128 + (w & 1) * 64 + (b & 31) * 2 + (w >> 1))); SB(); }
#pragma unroll
        for (int i = 0; i < 16; ++i) { aV[i] *= rstd; PIN(aV[i]); }
        SB();
        *(LAS v4u*)(a_v) = pack8(aV, 0);
        *(LAS v4u*)(a_v + 16) = pack8(aV, 1);
        SB();
        if constexpr (layer == 0 && PRB == 1) { vv = peek(rs, l0, cnt_off(C_H + (b >> 5) * 128 + (w & 1) * 64 + (b & 31) * 2 + (w >> 1))); SB(); }
        u32 o[16];
        dual_softmax(p, c, o, [&](auto I) {
          if constexpr (layer == 0) {
            u32 t;
            asm volatile("s_getreg_b32 %1, hwreg(HW_REG_IB_STS, 0, 4)\n\ts_cmp_gt_u32 %1, %2\n\ts_addc_u32 %0, %0, 0" : "+s"(lat), "=&s"(t) : "n"(I > JPK ? 1 : 0) : "scc");
            if constexpr (I == JPK) jv = peek(rs, w != 0 ? 0x80000000u : jvo, 0);
          }
        });
        if constexpr (layer == 0) *(LAS u32*)(L + XS_OFF + w * 4) = lat;
#pragma unroll
        for (int i = 0; i < 4; ++i) *(LAS v4u*)(a_pk + i * 16) = v4u{o[4 * i], o[4 * i + 1], o[4 * i + 2], o[4 * i + 3]};
      }
      SB();
      aQ = mfma32f(csq[2], nmean, zero16);
      STAMP(sb + 3);
      asm volatile("s_waitcnt vmcnt(%0)" ::"n"(wok) : "memory");
      if constexpr (layer == 0) {
        asm volatile("" ::"v"(vv));
        const u32 need = ln == 0 ? 36u : ln == 1 ? 16u : 0u;
        if (w == 0 && (__builtin_amdgcn_ballot_w64(jv >= need) & 7ull) != 7ull) poll3(rs, jvo, need);
      }
      BAR();
      v4u lt;
      if constexpr (layer == 0) { lt = *(LAS v4u*)(L + XS_OFF); sd = 0; }
      const u32 pbase = KVP + (layer * 64 + 4 * hp + w) * 16384, zbase = ZP + (layer * 16 + hp) * 16384 + w * 2048;
      v4u wq[4];
      auto qfrag = [&](int ks) { return WQ[ks * 64]; };
      {
        f32x4 kv = {0.f, 0.f, 0.f, 0.f}, zz = {0.f, 0.f, 0.f, 0.f};
        const v4u ones = {ONES2, ONES2, ONES2, ONES2};
        v2u a0[4], a1[4], b0[4], b1[4];
#pragma unroll
        for (int ks = 0; ks < 4; ++ks) {
          a0[ks] = trread(a_pa + (32 * ks) * PK_STR); a1[ks] = trread(a_pa + (32 * ks + 4) * PK_STR);
          b0[ks] = trread(a_pb + (32 * ks) * VV_STR); b1[ks] = trread(a_pb + (32 * ks + 4) * VV_STR);
          SB();
        }
#pragma unroll
        for (int ks = 0; ks < 4; ++ks) {
          const v4u A = {a0[ks].x, a0[ks].y, a1[ks].x, a1[ks].y}, B = {b0[ks].x, b0[ks].y, b1[ks].x, b1[ks].y};
          kv = mfma16(A, B, kv);
          zz = mfma16(ones, A, zz);
          asm volatile("" : "+a"(zz));
          SB();
        }
#pragma unroll
        for (int ks = 0; ks < 4; ++ks) wq[ks] = qfrag(ks);
        SB();
        const v2u kvb = {pk(kv[0], kv[1]), pk(kv[2], kv[3])};
        st64<SC1>(kvb, rs, o_kv, pbase + rt * 512 + sd);
        st64<SC1>(kvb, rs, o_kv, pbase + rt * 512 + (sd ^ 8192));
        const float z0 = zz[0];
        __builtin_amdgcn_raw_buffer_store_b32(__builtin_bit_cast(u32, z0), rs, o_z, zbase + rt * 128, SC1);
      }
      SB();
      STAMP(sb + 4);
      auto qstep = [&](auto I) {
        constexpr int i = I;
        aQ = mfma32(wq[i & 3], xf[i], aQ);
        SB();
        if constexpr (i + 4 < 32) wq[i & 3] = qfrag(i + 4);
      };
      LAS unsigned char *a_kz, *a_kw, *a_kr;
      u32 o_st, o_zl, l2k;
      v4u sel;
      if constexpr (layer == 0) {
        const int df = __builtin_amdgcn_readfirstlane((int)(lt[0] + lt[2]) - (int)(lt[1] + lt[3]));
        sd = df > 0 ? 8192 : 0;
        const u32 vote = df == 0 ? 1 : (((df > 0) ^ (xcd >> 2)) & 1) * 2;
        (void)__builtin_amdgcn_raw_ptr_buffer_atomic_add_i32(1 + (vote << 16), rs, (w == 0 && j < 4 && ln < 16) ? ln * CSTR : 0x80000000u, cnt_off(C_V), 0);
        SB();
      }
      const u32 skv = cnt_off(C_KV + layer * 128 + hp * 4 + w), ckv = skv + sd;
      u32 pvk[2];
      static_for<32>([&](auto I) {
        constexpr int i2 = I - QS, n2 = 32 - QS;
        if constexpr (I == QS) { wsignal<NQ1 + (layer ? 0 : 1)>(rs, l2k, skv); STAMP(sb + 5); }
        qstep(I);
        if constexpr (I < QS) { if constexpr ((I * NQ1 + QS - 1) / QS != ((I + 1) * NQ1 + QS - 1) / QS) pf(IC(s_q1 + (I * NQ1 + QS - 1) / QS)); }
        else {
          if constexpr (layer == 0 && i2 == VPK) { vv = peek(rs, w == 0 ? l0 : 0x80000000u, cnt_off(C_V + (b & 15))); SB(); }
          if constexpr ((i2 == KVPK1 || i2 == KVPK2) && KMODE == 2) apeek(rs, l0, ckv, pvk[i2 == KVPK2]);
          if constexpr ((i2 * NQ2 + n2 - 1) / n2 != ((i2 + 1) * NQ2 + n2 - 1) / n2) pf(IC(s_q2 + (i2 * NQ2 + n2 - 1) / n2));
        }
        const u32 l2 = opqv(l16) >> 4, g2 = l2 >> 4, c2 = l2 & 15;
        if constexpr (I == 2) l2k = opqv(l2 < 2 ? l2 * 8192 : 0x80000000u);
        if constexpr (I == 1) o_zl = opqv(g2 * 128 + c2 * 4);
        if constexpr (I == 3) a_kz = L + opqv(KF_OFF + w * 544 + (c2 >> 3) * 272 + 256 + (c2 & 7) * 2);
        if constexpr (I == 5) a_kw = L + opqv(KF_OFF + w * 544 + (g2 >> 1) * 272 + (g2 & 1) * 8 + rho32(c2 & 7, c2 >> 3) * 16);
        if constexpr (I == 7) a_kr = L + opqv(KF_OFF + (l2 >> 5) * 272 + ((l2 & 31) < 16 ? (l2 & 31) : 16) * 16);
        if constexpr (I == 9) o_st = opqv(l2 >= 32 ? 0x80000000u : c2 * 16 + g2 * 8);
        if constexpr (I == 11 || I == 13 || I == 15 || I == 17) {
          constexpr int m = (I - 11) / 2;
          sel[m] = opqv((c2 >> 1) == 4 * (g2 & 1) + m ? 0x3f80u << (16 * (c2 & 1)) : 0u);
        }
        SB();
      });
      const v4u q0 = pack8(aQ, 0), q1 = pack8(aQ, 1);
      f32x16 pq = mfma32(wph[0], q0, zero16);
      pq = mfma32(wph[1], q1, pq);
      SB();
      v4u phq[4];
      v4u v[8];
      f32x4 z;
      {
        u32 o[16];
        dual_softmax(pq, c, o, [&](auto I) {
          constexpr int i = I;
          if constexpr (i == KVCHK) {
            STAMP(sb + 6);
            if constexpr (KMODE == 0) wfinish(rs, l0, ckv, 16, pvk[0]);
            else pfinish2<1 + NQ2 - (KVPK1 * NQ2 + 31 - QS) / (32 - QS) + (!layer && VPK > KVPK1), NQ2 - (KVPK2 * NQ2 + 31 - QS) / (32 - QS) + (!layer && VPK > KVPK2)>(rs, l0, ckv, 16, pvk);
            STAMP(sb + 7);
#pragma unroll
            for (int k = 0; k < 4; ++k) z[k] = __builtin_bit_cast(float, ld32<SC1>(rs, o_zl + k * 512, zbase + sd));
            SB();
          }
          static_for<8>([&](auto K) {
            constexpr int k = K, at = KVCHK + (k + 1) / KVLD;
            if constexpr ((at < 9 ? at : 9) == i) { {
              v[k] = ldf<SC1>(rs, l16, pbase + (sd ^ 8192), k * 1024);
              SB();
              if constexpr (layer == 0 && k == 7 && VPK < 0) { vv = peek(rs, w == 0 ? l0 : 0x80000000u, cnt_off(C_V + (b & 15))); SB(); }
            } }
          });
        });
#pragma unroll
        for (int m = 0; m < 4; ++m) {
          auto sw = __builtin_amdgcn_permlane32_swap(o[m], o[4 + m], false, false);
          phq[0][m] = sw[0];
          phq[2][m] = sw[1];
          auto sx = __builtin_amdgcn_permlane32_swap(o[8 + m], o[12 + m], false, false);
          phq[1][m] = sx[0];
          phq[3][m] = sx[1];
        }
#pragma unroll
        for (int m = 0; m < 4; ++m) PIN(phq[m]);
      }
      SB();
      {
        const float zs = sum32(sum16((z[0] + z[1]) + (z[2] + z[3])));
        *(LAS unsigned short*)(a_kz) = (unsigned short)pk(zs, zs);
        if constexpr (KMODE == 2) pretire(pvk);
        SB();
        f32x4 sv = {0.f, 0.f, 0.f, 0.f};
#pragma unroll
        for (int i = 0; i < 8; ++i) { sv = mfma16(sel, v[i], sv); SB(); }
        *(LAS v2u*)(a_kw) = v2u{pk(sv[0], sv[1]), pk(sv[2], sv[3])};
      }
      if constexpr (layer == 0) {
        if (w == 0) {
#pragma unroll 1
          for (u32 spins = 0; spins < (1u << 16) && (__builtin_amdgcn_readfirstlane(vv) & 0xffffu) < 32u; ++spins) vv = peek(rs, l0, cnt_off(C_V + (b & 15)));
          if (ln == 0) *(LAS u32*)(L + XS_OFF + 16) = vv;
        }
      }
      BAR();
      if constexpr (layer == 0) vv = *(LAS u32*)(L + XS_OFF + 16);
      STAMP(sb + 8);
      {
        v4u kvf[4];
#pragma unroll
        for (int i = 0; i < 4; ++i) kvf[i] = *(LAS v4u*)(a_kr + i * 544);
        SB();
        f32x16 nd[2];
        nd[0] = mfma32(kvf[0], phq[0], zero16);
        nd[0] = mfma32(kvf[1], phq[1], nd[0]);
        nd[1] = mfma32(kvf[2], phq[2], zero16);
        nd[1] = mfma32(kvf[3], phq[3], nd[1]);
        SB();
        if constexpr (layer == 0) {
          sd = opq(((((u32)__builtin_amdgcn_readfirstlane(vv) >> 16 > 32u ? 1u : 0u) ^ (xcd >> 2) ^ NFLIP) & 1u) * 8192);
        }
#pragma unroll
        for (int h = 0; h < 2; ++h) {
          const float inv = __builtin_amdgcn_rcpf(nd[h][8] + 1e-6f);
          v4u ob;
#pragma unroll
          for (int m = 0; m < 4; ++m) ob[m] = pk(nd[h][2 * m] * inv, nd[h][2 * m + 1] * inv);
          st128<SC1>(ob, rs, l16 + h * 1024, OB + layer * MB2 + (tt & 31) * 65536 + blk(hp * 2048) + (sd ^ (layer * 8192)));
          SB();
        }
      }
      STAMP(sb + 9);
      static_for<ODR>([&](auto I) { pf(IC(s_od + I)); });
      wsignal<ODR>(rs, l0, cnt_off(C_O + layer * 128 + ((tt >> 1) & 15) * 4 + (hp >> 2)) + sd);
      STAMP(sb + 10);
      const u32 co = cnt_off(C_O + layer * 128 + (rt2 & 15) * 4 + w) + sd;
      u32 pvo[3], pvz[2];
      BAR();
      if constexpr (OMODE == 1) {
        apeek(rs, l0, co, pvo[0]);
        NAP(PS2);
        apeek(rs, l0, co, pvo[1]);
        NAP(PS2);
        apeek(rs, l0, co, pvo[2]);
        static_for<n_op>([&](auto I) { pf(IC(s_op + I)); });
        pfinish<2 + n_op, 1 + n_op, n_op>(rs, l0, co, 8, pvo);
      } else if constexpr (OMODE == 2) {
        NAP(PS4);
        apeek(rs, l0, co, pvz[0]);
        static_for<n_op>([&](auto I) { pf(IC(s_op + I)); });
        NAP(PS6);
        apeek(rs, l0, co, pvz[1]);
        pfinish2<1 + n_op, 0>(rs, l0, co, 8, pvz);
      } else {
        NAP(PS4);
        pvo[0] = peek(rs, l0, co);
        SB();
        static_for<n_op>([&](auto I) { pf(IC(s_op + I)); });
        wfinish(rs, l0, co, 8, pvo[0]);
      }
      STAMP(sb + 11);
      const u32 ab = OB + layer * MB2 + w * 16384 + (sd ^ (layer * 8192));
      v4u af[DXV];
      splitk_phase<FrO, DXV, 0>(rs, wo, af, ab + (tte & 31) * 65536, ab + ((tte & 31) ^ 1) * 65536, L, w, l16, 1.f, res, [&](int) {}, [&]() {}, [&](auto I) {
        if constexpr (I == 1 && OMODE == 1) pretire(pvo);
        if constexpr (I == 1 && OMODE == 2) pretire(pvz);
        if constexpr (I >= 4 && s_p2 + I - 4 < s_xd) pf(IC(s_p2 + I - 4));
      });
      STAMP(sb + 12);
      resid_store(rs, res, l16, o_st, xtile(2 * layer, tte) + blk(n0 * 64), stile(2 * layer, tte) + (n0 >> 5) * 256);
      STAMP(sb + 13);
      static_for<XDR>([&](auto I) { pf(IC(s_xd + I)); });
      wsignal<XDR>(rs, l0, (cnt_off(C_X1 + layer * 32 + (tte & 31)) + sd));
      STAMP(sb + 14);
    }
    const u32 lh = opqv(opqv(lane) < nlh ? lane * CSTR : 0x80000000u);
    v4u af[WINH];
    const u32 ch0 = cnt_off(C_H + layer * 512 + (rt2 & 15) * 4 + w) + sd;
    const u32 sh0 = cnt_off(C_H + layer * 512 + ((tt >> 1) & 15) * 4 + (2 * fb) / 7) + sd;
    auto htile = [&](u32 t) { return HB + layer * 4 * MB2 + (t & 31) * 262144 + (sd ^ ((t & 8) * 1024)); };
    auto xtile = [&](int i, u32 t) { return XT + i * MB2 + (t & 31) * 65536 + (sd ^ ((i & 1) ? 0 : 8192)); };
    auto stile = [&](int i, u32 t) { return ST + i * ST_SZ + blk((t & 31) * 4096) + sd; };
    const u32 hown = htile(tte) + w * 16384, hoth = hown ^ 262144;
    {
      const u32 ln = opqv(lane), l16 = ln * 16, r = ln & 31, hf = ln >> 5;
      auto w2ld = [&](auto I) {
        constexpr int i = I, k = (i >> 1) % 7;
        w2f[i >> 1][i & 1] = ld128<SC1>(rs, l16 + (k & 1) * 2048, wb + (i / 14) * 57344 + (k >> 1) * 4096 + ((i & 1) ? 1024 - wsw : wsw));
      };
      auto hld = [&](int i) { if (i < HPRE) { af[i] = ld_fr<FrH>(rs, l16, hown, hoth, i); SB(); } };
      if (p3on) {
        static_for<3>([&](auto J) {
          if constexpr (XMODE == 1) apeek(rs, l0, (cnt_off(C_X1 + layer * 32 + (tt & 31)) + sd), pvx[J]);
          if constexpr (XMODE == 0 && J == (layer ? 0 : 1)) { if constexpr (layer) NAP(PS3 + 1); pvx[0] = peek(rs, l0, (cnt_off(C_X1 + layer * 32 + (tt & 31)) + sd)); }
          if constexpr (XMODE == 3 && J > 0) { if constexpr (layer) NAP(J == 1 ? PS3 + 1 : PS5); apeek(rs, l0, (cnt_off(C_X1 + layer * 32 + (tt & 31)) + sd), pvy[J - 1]); }
          SB();
          if constexpr (layer == 0) static_for<W2D / 3>([&](auto I) { w2ld(IC(J * (W2D / 3) + I)); SB(); });
          else if constexpr (J < 2 && XMODE == 1) NAP(PS2);
        });
        constexpr int DX = DXV;
        v4u xf[32];
        const u32 xb = xtile(2 * layer, tt);
        f32x4 sp[4];
        const u32 stb = stile(2 * layer, tt);
        LAS v4u* WL = (LAS v4u*)(L + l16);
        LAS v4u* WH = (LAS v4u*)(L + opqv(l16 + 65536));
        SB();
        asm volatile("s_waitcnt vmcnt(%0)" ::"n"((layer == 0 ? W2D : 0) + (XMODE == 1 ? 4 : XMODE == 3 ? 3 : XMODE == 0 ? 2 : 1)) : "memory");
        BAR();
        v4u wa[3], wc[3], wq[4];
#pragma unroll
        for (int i = 0; i < 2; ++i) { wa[i] = WL[i * 64]; wc[i] = WL[(32 + i) * 64]; }
        SB();
        STAMP(sb + 15);
        if constexpr (XMODE == 0) wfinish(rs, l0, (cnt_off(C_X1 + layer * 32 + (tt & 31)) + sd), 16, pvx[0]);
        else if constexpr (XMODE == 3) pfinish2<1 + (layer ? 0 : 2 * W2D / 3), (layer ? 0 : W2D / 3)>(rs, l0, (cnt_off(C_X1 + layer * 32 + (tt & 31)) + sd), 16, pvy);
        else if constexpr (XMODE == 2) { if constexpr (layer) NAP(PS3); wwait(rs, l0, (cnt_off(C_X1 + layer * 32 + (tt & 31)) + sd), 16); }
        else pfinish<2 + (layer ? 0 : W2D), 1 + (layer ? 0 : 2 * W2D / 3), (layer ? 0 : W2D / 3)>(rs, l0, (cnt_off(C_X1 + layer * 32 + (tt & 31)) + sd), 16, pvx);
        STAMP(sb + 16);
#pragma unroll
        for (int ks = 0; ks < DX; ++ks) { xf[ks] = ldr(rs, l16, xb, rx, ks); SB(); }
        f32x16 acc[4];
        acc[0] = zero16;
        acc[1] = zero16;
        float nmean, nc, cp[4][4];
        SB();
        static_for<32>([&](auto KSI) {
          constexpr int ks = KSI;
          acc[0] = mfma32(wa[ks % 3], xf[ks], acc[0]);
          SB();
          if constexpr (ks + 2 < 32) wa[(ks + 2) % 3] = WL[(ks + 2) * 64];
          else wq[2 * (ks - 30)] = WH[(2 * (ks - 30)) * 64];
          if constexpr (ks + DX < 32) xf[ks + DX] = ldr(rs, l16, xb, rx, ks + DX);
          if constexpr (layer == 0 && ks >= 20) w2ld(IC(W2D + ks - 20));
          if constexpr (ks == 24) { SB(); acc[1] = mfma32f_pad<5>(cs1[1], nmean, acc[1]); }
          SB();
          acc[1] = mfma32(wc[ks % 3], xf[ks], acc[1]);
          SB();
          if constexpr (ks + 2 < 32) wc[(ks + 2) % 3] = WL[(32 + ks + 2) * 64];
          else wq[2 * (ks - 30) + 1] = WH[(2 * (ks - 30) + 1) * 64];
          if constexpr (ks < 4) sp[ks] = __builtin_bit_cast(f32x4, ld128<SC1>(rs, l16 + ks * 1024, stb));
          if constexpr (layer == 0 && ks >= 4 && ks < 8) {
#pragma unroll
            for (int i = 0; i < 4; ++i) cp[ks - 4][i] = __builtin_bit_cast(float, ld32<SC1>(rs, ln * 4 + i * 256 + (ks - 4) * 1024, CS_W1 + fb * 4096));
          }
          if constexpr (ks == 1 && XMODE == 1) pretire(pvx);
          if constexpr (ks == 1 && XMODE == 3) pretire(pvy);
          if constexpr (ks == 17) stats_fin(sp, nmean, nc);
          if constexpr (ks == 18) { nc *= -LOG2E; PIN(nc); }
          if constexpr (layer == 0 && ks == 20) {
#pragma unroll
            for (int t = 0; t < 4; ++t) cs1[t] = (cp[t][0] + cp[t][1]) + (cp[t][2] + cp[t][3]);
          }
          if constexpr (ks == 21) { SB(); acc[0] = mfma32f_pad<5>(cs1[0], nmean, acc[0]); }
          if constexpr (ks == 26) acc[2] = mfma32f(cs1[2], nmean, zero16);
          if constexpr (ks == 28) acc[3] = mfma32f(cs1[3], nmean, zero16);
          SB();
        });
        STAMP(sb + 17);
        u32 hb[2];
#pragma unroll
        for (int s = 0; s < 2; ++s) hb[s] = opq(htile(tt) + ((2 * fb + s) / 7) * 16384 + ((2 * fb + s) % 7) * 1024);
        float tv[64], ev[64];
        v4u ho[8];
        u32 pv0;
        auto stage = [&](auto V, auto ST_) {
          constexpr int v = V, st = ST_;
          if constexpr (st == 0) { tv[v] = acc[v >> 4][v & 15] * nc; PIN(tv[v]); }
          else if constexpr (st == 1) { ev[v] = __builtin_amdgcn_exp2f(tv[v]); PIN(ev[v]); }
          else if constexpr (st == 2) { ev[v] = 1.f + ev[v]; PIN(ev[v]); }
          else if constexpr (st == 3) { ev[v] = __builtin_amdgcn_rcpf(ev[v]); PIN(ev[v]); }
          else if constexpr (st == 4) { tv[v] = tv[v] * ev[v]; PIN(tv[v]); }
          else if constexpr (v & 1) {
            u32 hh = pk(tv[v - 1], tv[v]);
            PIN(hh);
            ho[v >> 3][(v >> 1) & 3] = hh;
            if constexpr ((v & 7) == 7) st128<SC1>(ho[v >> 3], rs, l16, hb[(v >> 3) & 1] + (v >> 4) * 65536);
          }
        };
        static_for<64>([&](auto GI) {
          constexpr int gi = GI, t = 2 + (gi >> 5), ks = gi & 31;
          acc[t] = mfma32(wq[gi & 3], xf[ks], acc[t]);
          SB();
          if constexpr (gi + 4 < 64) wq[gi & 3] = WH[(gi + 4) * 64];
          if constexpr (layer == 0 && p3_w2gap(gi)) w2ld(IC(W2D + 12 + (gi * W2B) / 8));
          SB();
          static_for<48>([&](auto V) {
            constexpr int d = gi - p3_g0(V);
            if constexpr (d >= 0 && d < 6) stage(V, IC(d));
          });
          if constexpr (gi == HS0) wsignal<p3_younger(layer, 0, HS0)>(rs, lh, sh0);
          if constexpr (gi == HS1) wsignal<p3_younger(layer, 1, HS1)>(rs, lh, sh0 + 128 * CSTR);
          if constexpr (gi == PK0) pv0 = peek(rs, l0, ch0);
          SB();
        });
        STAMP(sb + 18);
        constexpr int W2E = 56 - W2D - 12 - 8 * W2B;
        static_assert(W2E >= 0, "w2 schedule");
        {
          f32x2 t2[8], e2[8];
#pragma unroll
          for (int m = 0; m < 8; ++m) { t2[m] = f32x2{acc[3][2 * m], acc[3][2 * m + 1]} * nc; PIN(t2[m]); }
          SB();
#pragma unroll
          for (int m = 0; m < 8; ++m) { e2[m] = f32x2{__builtin_amdgcn_exp2f(t2[m].x), __builtin_amdgcn_exp2f(t2[m].y)}; PIN(e2[m]); }
          SB();
          if constexpr (HPRE > 0) wfinish(rs, l0, ch0, 8, pv0);
          STAMP(sb + 21);
          hld(0);
#pragma unroll
          for (int m = 0; m < 8; ++m) { e2[m] = e2[m] + 1.f; PIN(e2[m]); }
          SB();
#pragma unroll
          for (int m = 0; m < 8; ++m) { e2[m] = f32x2{__builtin_amdgcn_rcpf(e2[m].x), __builtin_amdgcn_rcpf(e2[m].y)}; PIN(e2[m]); if (m == 1 || m == 5) { SB(); hld(1 + m / 4); } }
          SB();
#pragma unroll
          for (int m = 0; m < 8; ++m) { t2[m] = t2[m] * e2[m]; PIN(t2[m]); }
          SB();
#pragma unroll
          for (int i = 3; i < HPRE; ++i) hld(i);
#pragma unroll
          for (int s = 0; s < 2; ++s)
            st128<SC1>(v4u{pk(t2[4 * s].x, t2[4 * s].y), pk(t2[4 * s + 1].x, t2[4 * s + 1].y), pk(t2[4 * s + 2].x, t2[4 * s + 2].y), pk(t2[4 * s + 3].x, t2[4 * s + 3].y)},
                       rs, l16, hb[s] + 3 * 65536);
        }
        SB();
        wsignal<p3_younger(layer, 2, 64) + HPRE + 2>(rs, lh, sh0 + 256 * CSTR);
        if constexpr (layer == 0) static_for<W2E>([&](auto E) { w2ld(IC(W2D + 12 + 8 * W2B + E)); });
        SB();
        STAMP(sb + 19);
        if constexpr (HPRE == 0) wfinish(rs, l0, ch0, 8, pv0);
      } else {
        if constexpr (layer == 0) static_for<56>([&](auto E) { w2ld(E); });
        DRAIN();
        BAR();
        wwait(rs, l0, ch0, 8);
#pragma unroll
        for (int i = 0; i < HPRE; ++i) hld(i);
      }
    }
    {
      const u32 ln = opqv(lane), l16 = ln * 16, r = ln & 31, hf = ln >> 5;
      const u32 wqb = opq(WQKV_T + hp * 98304 + w * 16384);
      u32 pv1[2], pv2[2], pv3[2];
      splitk_phase<FrH, WINH, HPRE>(rs, w2f, af, hown, hoth, L, w, l16, -LN2, res,
        [&](int i) { if (i == T1A || i == T1B) apeek(rs, l0, ch0 + 128 * CSTR, pv1[i == T1B]); },
        [&]() {
          wsignal<3 + 14 - HPRE>(rs, lh, sh0 + 384 * CSTR);
          STAMP(sb + 22);
          pfinish2<14 - T1A + 2, 14 - T1B + 1>(rs, l0, ch0 + 128 * CSTR, 8, pv1);
          STAMP(sb + 20);
        },
        [&](auto I) {
          constexpr int i = I, g = i - 42;
          if constexpr (i == 15) pretire(pv1);
          constexpr auto ops = [](int a, int b) {
            int n = 0;
            for (int q = 14; q < 56; ++q) n += FrH::lstep(q, WINH) >= a && FrH::lstep(q, WINH) < b;
            for (int q : {PK2, PK2B, PK3, PK3B}) n += q > a && q < b;
            return n;
          };
          static_assert(PK2 < PK2B && PK2B < T2CHK && PK2B != PK3 && PK2 != PK3 && PK3 < PK3B && PK3B < T3CHK && T2CHK >= 28 - WINH && T3CHK >= 35 - WINH, "peek order");
          if constexpr (i == T2CHK) pfinish2<ops(PK2, i), ops(PK2B, i)>(rs, l0, ch0 + 256 * CSTR, 8, pv2);
          if constexpr (i == T3CHK) { STAMP(sb + 27); pfinish2<ops(PK3, i), ops(PK3B, i)>(rs, l0, ch0 + 384 * CSTR, 8, pv3); STAMP(sb + 28); }
          if constexpr (i == PK2 || i == PK2B) apeek(rs, l0, ch0 + 256 * CSTR, pv2[i == PK2B]);
          if constexpr (i == PK3 || i == PK3B) apeek(rs, l0, ch0 + 384 * CSTR, pv3[i == PK3B]);
          if constexpr (i == 29) pretire(pv2);
          if constexpr (i == 36) pretire(pv3);
          if constexpr (KWPOS == 0) {
            if constexpr (layer == 0 && g >= 10 && g <= 14) dma1<g - 10>(rs, l16, wqb, L + w * 16384);
            if constexpr (layer == 0 && g >= 19 && g <= 21) dma1<g - 14>(rs, l16, wqb, L + w * 16384);
          } else {
            if constexpr (layer == 0 && g >= 10 && g < 26) dma1<((g - 10) & 3)>(rs, l16, wqb + ((g - 10) >> 2) * 4096, L + ((w * 16384 + ((g - 10) >> 2) * 4096) ^ rx));
          }
        });
      STAMP(sb + 23);
      if constexpr (layer == 0) {
        resid_store(rs, res, l16, hf ? 0x80000000u : (r & 15) * 16 + (r >> 4) * 8, xtile(1, tte) + blk(n0 * 64), stile(1, tte) + (n0 >> 5) * 256);
        STAMP(sb + 24);
        if constexpr (KWPOS == 0) dma_range<8, 16>(rs, l16, wqb, L + w * 16384);
        wsignal<KWPOS == 0 ? 8 : 0>(rs, l0, (cnt_off(C_X + (tte & 31)) + sd));
        STAMP(sb + 25);
      } else {
        const rsrc_t rso = mkrsrc(out, S * D * 2);
#pragma unroll
        for (int s = 0; s < 2; ++s) st128<OAUX>(res[s], rso, r * 1024 + hf * 16 + s * 32, tte * 32768 + n0 * 2);
        if (b == 255 && tid < 16) st64<SC1>(v2u{tag_lo, tag_hi}, rs, tid * 8, EPOCH_OFF);
        STAMP(sb + 24);
      }
    }
  });
  asm volatile(".Lkcode1:");
}

extern "C" void kernel_launch(void* const* d_in, const int* in_sizes, int n_in, void* d_out, int out_size, void* d_ws,
                              size_t ws_size, hipStream_t stream) {
  (void)hipFuncSetAttribute((const void*)k_main, hipFuncAttributeMaxDynamicSharedMemorySize, LDS_BYTES);
  k_main<<<256, 256, LDS_BYTES, stream>>>((const uint16_t*)d_in[0], (const uint16_t*)d_in[1], (const uint16_t*)d_in[2],
                                          (const uint16_t*)d_in[3], (const uint16_t*)d_in[5], (const uint16_t*)d_in[6],
                                          (unsigned char*)d_ws, (unsigned char*)d_out);
}
```

```cpp
#define WSB 54
#include <hip/hip_runtime.h>
#include <hip/hip_bf16.h>
#include <stdint.h>
#include <utility>

typedef unsigned int u32;
typedef u32 v4u __attribute__((ext_vector_type(4)));
typedef u32 v2u __attribute__((ext_vector_type(2)));
typedef short s16x4 __attribute__((ext_vector_type(4)));
typedef __bf16 bf16x8 __attribute__((ext_vector_type(8)));
typedef __bf16 bf16x2 __attribute__((ext_vector_type(2)));
typedef float f32x2 __attribute__((ext_vector_type(2)));
typedef float f32x4 __attribute__((ext_vector_type(4)));
typedef float f32x16 __attribute__((ext_vector_type(16)));
#define LAS __attribute__((address_space(3)))
#define GAS __attribute__((address_space(1)))
#define DI __device__ __forceinline__
typedef __amdgpu_buffer_rsrc_t rsrc_t;

#define DXV 12
#define WINH 56
#define HS0 36
#define HS1 56
#define PK0 59
#define HPRE 0
#define PK2 0
#define T1A 0
#define T1B 7
#define PS5 5
#define PK3 7
#define PK2B 6
#define PK3B 13
#define T2CHK 14
#define T3CHK 21
#define KVCHK 0
#define KVLD 2
#define ODR 8
#define OAUX 0
#define PS1 3
#define PS2 4
#define XMODE 3
#define KMODE 2
#define NQ1 11
#define NQ2 10
#define OPL0 4
#define OPL1 4
#define XDR 4
#define OMODE 2
#define PS6 3
#define PS4 2
#define KWPOS 1
#define PS3 2
#define WOK 2
#define CPF 1
#define CPW 1
#define TLBD 0
#define LK0 4
#define VPK 18
#define PRB 1
#define NFLIP 0
#define ROT2 0
#define QS 11
#define ROT 1
#define XBAR 1
#define JPK 0
#define XA 8
#define XB 20
#define UB 10
#define FPK 4
#define FCK 6
#define KVPK2 14
#define KVPK1 4
#define TLBW 262144
#define W2D 12
#define W2B 4
constexpr int S = 2048, D = 512;
constexpr u32 CTRL = 0;
constexpr u32 WQKV_T = 262144;
constexpr u32 WPHI_T = WQKV_T + 16 * 98304;
constexpr u32 WO_T = WPHI_T + 32768;
constexpr u32 W1_T = WO_T + 524288;
constexpr u32 W2_T = W1_T + 14 * 131072;
constexpr u32 CS_QKV = W2_T + 8 * 4 * 28 * 2048;
constexpr u32 CS_W1 = CS_QKV + 65536;
constexpr u32 MB2 = 2097152;
constexpr u32 XT = 3 * MB2;
constexpr u32 OB = 6 * MB2;
constexpr u32 HB = 8 * MB2;
constexpr u32 ST = 16 * MB2;
constexpr u32 ST_SZ = 262144;
constexpr u32 ZP = ST + 4 * ST_SZ;
constexpr u32 KVP = 17 * MB2;
constexpr u32 WS_END = 18 * MB2;
static_assert(CS_W1 + 65536 <= XT, "workspace map");
constexpr u32 blk(u32 off) { return (off >> 13) * 16384 + (off & 8191); }
constexpr u32 CSTR = 128;
enum { C_H = 0, C_O = 1024, C_X1 = 1280, C_X = 1408, C_J = 1440, C_V = 1504, C_TMO = 1520, C_KV = 1536, NCNT = 1792 };
constexpr u32 INIT_OFF = CTRL + NCNT * CSTR, EPOCH_OFF = INIT_OFF + 16 * CSTR, FLAG_OFF = EPOCH_OFF + CSTR;
static_assert(FLAG_OFF + 192 * CSTR <= WQKV_T, "control area");

#define STAMP(i)
constexpr float LOG2E = 1.4426950408889634f;
constexpr float LN2 = 0.6931471805599453f;

DI u32 pk(float a, float b) {
  f32x2 v = {a, b};
  bf16x2 r = __builtin_convertvector(v, bf16x2);
  return __builtin_bit_cast(u32, r);
}
DI float bflo(u32 u) { return __builtin_bit_cast(float, u << 16); }
DI float bfhi(u32 u) { return __builtin_bit_cast(float, u & 0xffff0000u); }
DI f32x16 mfma32(v4u a, v4u b, f32x16 c) {
  f32x16 d = __builtin_amdgcn_mfma_f32_32x32x16_bf16(__builtin_bit_cast(bf16x8, a), __builtin_bit_cast(bf16x8, b), c, 0, 0, 0);
  asm volatile("" : "+a"(d));
  return d;
}
DI f32x4 mfma16(v4u a, v4u b, f32x4 c) {
  return __builtin_amdgcn_mfma_f32_16x16x32_bf16(__builtin_bit_cast(bf16x8, a), __builtin_bit_cast(bf16x8, b), c, 0, 0, 0);
}
DI float dot2(u32 a, u32 b, float c) {
  return __builtin_amdgcn_fdot2_f32_bf16(__builtin_bit_cast(bf16x2, a), __builtin_bit_cast(bf16x2, b), c, false);
}
template <int AUX> DI v4u ld128(rsrc_t rs, u32 voff, u32 soff) { return __builtin_amdgcn_raw_buffer_load_b128(rs, voff, soff, AUX); }
template <int AUX> DI v2u ld64(rsrc_t rs, u32 voff, u32 soff) { return __builtin_amdgcn_raw_buffer_load_b64(rs, voff, soff, AUX); }
template <int AUX> DI u32 ld32(rsrc_t rs, u32 voff, u32 soff) { return __builtin_amdgcn_raw_buffer_load_b32(rs, voff, soff, AUX); }
template <int AUX> DI void st128(v4u v, rsrc_t rs, u32 voff, u32 soff) {
  __builtin_amdgcn_raw_buffer_store_b128(v, rs, voff, soff, AUX);
  asm volatile("s_nop 1" ::"v"(v) : "memory");
}
template <int AUX> DI void st64(v2u v, rsrc_t rs, u32 voff, u32 soff) { __builtin_amdgcn_raw_buffer_store_b64(v, rs, voff, soff, AUX); }
DI rsrc_t mkrsrc(const void* p, u32 bytes) { return __builtin_amdgcn_make_buffer_rsrc((void*)p, 0, (int)bytes, 0x00020000); }
DI float sum32(float v) {
  u32 u = __builtin_bit_cast(u32, v);
  auto r = __builtin_amdgcn_permlane32_swap(u, u, false, false);
  const u32 r0 = r[0], r1 = r[1];
  return __builtin_bit_cast(float, r0) + __builtin_bit_cast(float, r1);
}
DI float sum16(float v) {
  u32 u = __builtin_bit_cast(u32, v);
  auto r = __builtin_amdgcn_permlane16_swap(u, u, false, false);
  const u32 r0 = r[0], r1 = r[1];
  return __builtin_bit_cast(float, r0) + __builtin_bit_cast(float, r1);
}
DI float mix16(float a, float b) {
  auto r = __builtin_amdgcn_permlane16_swap(__builtin_bit_cast(u32, a), __builtin_bit_cast(u32, b), false, false);
  const u32 r0 = r[0], r1 = r[1];
  return __builtin_bit_cast(float, r0) + __builtin_bit_cast(float, r1);
}
DI v2u trread(LAS unsigned char* __restrict__ p) {
  return __builtin_bit_cast(v2u, __builtin_amdgcn_ds_read_tr16_b64_v4i16((LAS s16x4*)p));
}
DI int rho32(int reg, int hf) { return (reg & 3) + 8 * (reg >> 2) + 4 * hf; }
constexpr u32 ONES2 = 0x3f803f80u;

DI v4u pack8(const f32x16& a, int s) {
  v4u r;
#pragma unroll
  for (int m = 0; m < 4; ++m) r[m] = pk(a[8 * s + 2 * m], a[8 * s + 2 * m + 1]);
  return r;
}

#define SB() __builtin_amdgcn_sched_barrier(0)
#define PIN(x) asm volatile("" : "+v"(x))
#define BAR() asm volatile("s_waitcnt lgkmcnt(0)\n\ts_barrier" ::: "memory")
#define DRAIN() asm volatile("s_waitcnt vmcnt(0)" ::: "memory")
#define RLX __ATOMIC_RELAXED, __HIP_MEMORY_SCOPE_AGENT
constexpr int SC1 = 16;

DI u32 opq(u32 x) { asm volatile("" : "+s"(x)); return x; }
DI u32 opqv(u32 x) { asm volatile("" : "+v"(x)); return x; }
DI v4u ldr(rsrc_t rs, u32 vo, u32 sbase, int ks) { return ld128<16>(rs, vo + (ks & 3) * 1024, sbase + blk((ks >> 2) * 4096)); }
template <int AUX> DI v4u ldf(rsrc_t rs, u32 vo, u32 sbase, u32 off) { return ld128<AUX>(rs, vo + (off & 4095u), sbase + (off & ~4095u)); }
template <int I> DI void dma1(rsrc_t rs, u32 vo, u32 sbase, LAS unsigned char* d) {
  __builtin_amdgcn_raw_ptr_buffer_load_lds(rs, (LAS u32*)(d + (I >> 2) * 4096), 16, vo, sbase + (I >> 2) * 4096, (I & 3) * 1024, SC1);
}
template <int I0, int I1> DI void dma_range(rsrc_t rs, u32 vo, u32 sbase, LAS unsigned char* d) {
  if constexpr (I0 < I1) {
    dma1<I0>(rs, vo, sbase, d);
    dma_range<I0 + 1, I1>(rs, vo, sbase, d);
  }
}
template <class F, int... I> DI void static_for_impl(F&& f, std::integer_sequence<int, I...>) { (f(std::integral_constant<int, I>{}), ...); }
template <int N, class F> DI void static_for(F&& f) { static_for_impl(f, std::make_integer_sequence<int, N>{}); }
DI u32 cnt_off(int idx) { return CTRL + idx * CSTR; }
DI u32 lane0_off() { return (threadIdx.x & 63) == 0 ? 0u : 0x80000000u; }
DI void add1(rsrc_t rs, u32 l0, u32 coff) { (void)__builtin_amdgcn_raw_ptr_buffer_atomic_add_i32(1, rs, l0, coff, 0); }
template <int N> DI void wsignal(rsrc_t rs, u32 l0, u32 coff) {
  SB();
  asm volatile("s_waitcnt vmcnt(%0)" ::"n"(N) : "memory");
  add1(rs, l0, coff);
  SB();
}
DI u32 peek(rsrc_t rs, u32 l0, u32 coff) { return __builtin_amdgcn_raw_ptr_buffer_atomic_add_i32(0, rs, l0, coff, 0); }
DI void wfinish(rsrc_t rs, u32 l0, u32 coff, u32 n, u32 pv) {
  if (__builtin_amdgcn_readfirstlane(pv) < n) {
#pragma unroll 1
    for (u32 spins = 0;; ++spins) {
      if (__builtin_amdgcn_readfirstlane(peek(rs, l0, coff)) >= n) break;
      if (spins > (1u << 16)) { __builtin_amdgcn_raw_buffer_store_b32(1u, rs, 0, cnt_off(C_TMO), SC1); break; }
    }
  }
  asm volatile("" ::: "memory");
}
DI void wwait(rsrc_t rs, u32 l0, u32 coff, u32 n) { wfinish(rs, l0, coff, n, peek(rs, l0, coff)); }
DI void apeek(rsrc_t rs, u32 l0, u32 coff, u32& p) {
  SB();
  asm volatile("v_mov_b32 %0, 0\n\tbuffer_atomic_add %0, %1, %2, %3 offen sc0 ; APEEK" : "=&v"(p) : "v"(l0), "s"(rs), "s"(coff) : "memory");
  SB();
}
#define PCHK(P, W) "s_waitcnt vmcnt(" W ")\n\tv_readfirstlane_b32 %3, " P "\n\ts_cmp_ge_u32 %3, %8\n\ts_cbranch_scc1 .Lpd%=\n\tv_mov_b32 " P ", 0\n\tbuffer_atomic_add " P ", %5, %6, %7 offen sc0\n\t"
template <int N0, int N1, int N2> DI void pfinish(rsrc_t rs, u32 l0, u32 coff, u32 n, u32 (&p)[3]) {
  u32 t, cnt;
  SB();
  asm volatile(PCHK("%0", "%9") PCHK("%1", "%10") PCHK("%2", "%11") "s_movk_i32 %4, 0x7fff\n.Lpl%=:\n\t" PCHK("%0", "2") PCHK("%1", "2") PCHK("%2", "2")
               "s_sub_u32 %4, %4, 1\n\ts_cbranch_scc0 .Lpl%=\n.Lpd%=:"
               : "+v"(p[0]), "+v"(p[1]), "+v"(p[2]), "=&s"(t), "=&s"(cnt)
               : "v"(l0), "s"(rs), "s"(coff), "n"(n), "n"(N0), "n"(N1 + 1), "n"(N2 + 2)
               : "memory", "scc");
  SB();
}
#define PCHK2(P, W) "s_waitcnt vmcnt(" W ")\n\tv_readfirstlane_b32 %2, " P "\n\ts_cmp_ge_u32 %2, %7\n\ts_cbranch_scc1 .Lqd%=\n\tv_mov_b32 " P ", 0\n\tbuffer_atomic_add " P ", %4, %5, %6 offen sc0\n\t"
template <int N0, int N1> DI void pfinish2(rsrc_t rs, u32 l0, u32 coff, u32 n, u32 (&p)[2]) {
  u32 t, cnt;
  SB();
  asm volatile(PCHK2("%0", "%8") PCHK2("%1", "%9") "s_movk_i32 %3, 0x7fff\n.Lql%=:\n\t" PCHK2("%0", "1") PCHK2("%1", "1")
               "s_sub_u32 %3, %3, 1\n\ts_cbranch_scc0 .Lql%=\n.Lqd%=:"
               : "+v"(p[0]), "+v"(p[1]), "=&s"(t), "=&s"(cnt)
               : "v"(l0), "s"(rs), "s"(coff), "n"(n), "n"(N0), "n"(N1 + 1)
               : "memory", "scc");
  SB();
}
DI void pretire(u32 (&p)[2]) { asm volatile("; PRETIRE %0 %1" ::"v"(p[0]), "v"(p[1])); }
DI void pretire(u32 (&p)[3]) { asm volatile("; PRETIRE %0 %1 %2" ::"v"(p[0]), "v"(p[1]), "v"(p[2])); }
#define NAP(n) do { SB(); __builtin_amdgcn_s_sleep(n); SB(); } while (0)
DI void poll3(rsrc_t rs, u32 vo, u32 need) {
#pragma unroll 1
  for (u32 spins = 0;; ++spins) {
    const u32 v = peek(rs, vo, 0);
    if ((__builtin_amdgcn_ballot_w64(v >= need) & 7ull) == 7ull) break;
    if (spins > (1u << 16)) { __builtin_amdgcn_raw_buffer_store_b32(1u, rs, 0, cnt_off(C_TMO), SC1); break; }
  }
  asm volatile("" ::: "memory");
}
template <int CTRL_> DI float dpp_add(float v) {
  return v + __builtin_bit_cast(float, __builtin_amdgcn_update_dpp(0, __builtin_bit_cast(int, v), CTRL_, 0xf, 0xf, true));
}

constexpr int SCR = 131072;
constexpr int PK_OFF = SCR, PK_STR = 144, VV_OFF = PK_OFF + 128 * PK_STR, VV_STR = 80, KF_OFF = VV_OFF + 128 * VV_STR;
constexpr int XS_OFF = 98304;
constexpr int RED_OFF = SCR;
constexpr int LDS_BYTES = 163840;
static_assert(KF_OFF + 4 * 544 <= LDS_BYTES, "lds");

DI void lds_put1(LAS unsigned char* p, const f32x16& a, int q) {
  *(LAS f32x4*)(p + q * 1024) = f32x4{a[4 * q], a[4 * q + 1], a[4 * q + 2], a[4 * q + 3]};
}

extern __shared__ __attribute__((aligned(16))) unsigned char smem[];

struct Sch { int put2[4], put3[4], bar1, rd[8], fu[8], put1[4], bar2, rd1, f1[4]; };
constexpr Sch sch_of(int KL) {
  return KL >= 14 ? Sch{{0, 1, 2, 3}, {4, 5, 6, 7}, 9, {9, 9, 9, 10, 10, 10, 11, 11}, {12, 13, 14, 19, 21, 22, 23, 24}, {15, 16, 17, 18}, 20, 20, {25, 26, 27, 27}}
                  : Sch{{0, 0, 1, 1}, {2, 2, 3, 3}, 5, {5, 5, 5, 5, 6, 6, 6, 6}, {7, 7, 8, 8, 11, 12, 12, 13}, {9, 9, 10, 10}, 11, 11, {14, 14, 15, 15}};
}
struct FrO {
  static constexpr int NS = 16, KL = 8;
  static constexpr bool own(int i) { return i >= 8; }
  static constexpr int ks(int i) { return i & 7; }
  static constexpr u32 off(int i) { return (i & 7) * 1024; }
  static constexpr int lstep(int q, int win) { return q - win; }
  static constexpr int first(int win) { return win; }
};
struct FrH {
  static constexpr int NS = 56, KL = 14;
  static constexpr int part(int i) { return i < 28 ? i / 14 : 2 + ((i / 7) & 1); }
  static constexpr bool own(int i) { return i < 28 ? ((i / 7) & 1) : i >= 42; }
  static constexpr int ks(int i) { return part(i) * 7 + i % 7; }
  static constexpr u32 off(int i) { return part(i) * 65536 + (i % 7) * 1024; }
  static constexpr int lstep(int q, int win) {
    const int g = T3CHK - 21;
    return q < 35 ? q - 14 : q < 42 ? (q - 14 < T3CHK ? T3CHK : q - 14) : q < 42 + g ? q - 21 : q - 14 - g;
  }
  static constexpr int first(int win) { return 14; }
};
template <class Fr> DI v4u ld_fr(rsrc_t rs, u32 l16, u32 a_own, u32 a_oth, int i) { return ldf<SC1>(rs, l16, Fr::own(i) ? a_own : a_oth, Fr::off(i)); }
template <class Fr, int WIN, int PRE, class Win, class Pre, class Mid>
DI void splitk_phase(rsrc_t rs, const v4u (&wf)[Fr::NS / 2][2], v4u (&af)[WIN], u32 a_own, u32 a_oth, LAS unsigned char* L, int w, u32 l16, float scale,
                     v4u (&res)[2], Win&& win, Pre&& pre_loop, Mid&& mid) {
  const f32x16 zero16 = {0.f, 0.f, 0.f, 0.f, 0.f, 0.f, 0.f, 0.f, 0.f, 0.f, 0.f, 0.f, 0.f, 0.f, 0.f, 0.f};
  constexpr int NS = Fr::NS, KP = Fr::KL, E0 = NS - KP, F0 = Fr::first(WIN);
  constexpr Sch sc = sch_of(KP);
  static_assert(WIN >= KP, "window");
  auto ld = [&](int i) { return ld_fr<Fr>(rs, l16, a_own, a_oth, i); };
#pragma unroll
  for (int i = PRE; i < F0; ++i) { win(i); af[i] = ld(i); SB(); }
  LAS unsigned char* r2 = L + opqv(RED_OFF + l16 + (w ^ 2) * 8192);
  LAS unsigned char* r3 = L + opqv(RED_OFF + l16 + (w ^ 3) * 8192 + 4096);
  LAS unsigned char* r0 = L + opqv(RED_OFF + l16 + w * 8192);
  LAS unsigned char* r1 = L + opqv(RED_OFF + l16 + (w ^ 1) * 8192);
  pre_loop();
  SB();
  f32x16 a0 = zero16, a1 = zero16, a2 = zero16, a3 = zero16;
  float u[16];
  static_for<E0>([&](auto I) {
    constexpr int i = I, ks = Fr::ks(i), e = i - (E0 - 4);
    const v4u bb = af[i % WIN];
    if constexpr (!Fr::own(i)) a2 = mfma32(wf[ks][0], bb, a2);
    else a1 = mfma32(wf[ks][1], bb, a1);
    SB();
    mid(I);
    static_for<NS - F0>([&](auto Q) { if constexpr (Fr::lstep(Q + F0, WIN) == i) { af[(Q + F0) % WIN] = ld(Q + F0); SB(); } });
    SB();
    if constexpr (!Fr::own(i)) a3 = mfma32(wf[ks][1], bb, a3);
    else a0 = mfma32(wf[ks][0], bb, a0);
    SB();
    if constexpr (e >= 0) {
      const u32 ra = res[e >> 1][2 * (e & 1)], rb = res[e >> 1][2 * (e & 1) + 1];
      u[4 * e] = bflo(ra); u[4 * e + 1] = bfhi(ra); u[4 * e + 2] = bflo(rb); u[4 * e + 3] = bfhi(rb);
#pragma unroll
      for (int m = 0; m < 4; ++m) PIN(u[4 * e + m]);
    }
    SB();
  });
  f32x4 p[8];
  auto acc4 = [&](int q, const f32x4& x) {
#pragma unroll
    for (int m = 0; m < 4; ++m) { const float xv = x[m]; u[4 * q + m] = __builtin_fmaf(xv, scale, u[4 * q + m]); PIN(u[4 * q + m]); }
  };
  static_for<2 * KP>([&](auto G) {
    constexpr int g = G, k = g % KP, ks = Fr::ks(E0 + k);
    if constexpr (g < KP) a1 = mfma32(wf[ks][1], af[(E0 + k) % WIN], a1);
    else a0 = mfma32(wf[ks][0], af[(E0 + k) % WIN], a0);
    SB();
    if constexpr (g == sc.bar1 || g == sc.bar2) BAR();
    static_for<8>([&](auto Q) { if constexpr (sc.rd[Q] == g) p[Q] = *(LAS f32x4*)(r0 + Q * 1024); });
    if constexpr (g == sc.rd1) static_for<4>([&](auto Q) { p[Q] = *(LAS f32x4*)(r1 + Q * 1024); });
    SB();
    static_for<4>([&](auto Q) { if constexpr (sc.put2[Q] == g) lds_put1(r2, a2, Q); });
    static_for<4>([&](auto Q) { if constexpr (sc.put3[Q] == g) lds_put1(r3, a3, Q); });
    static_for<4>([&](auto Q) { if constexpr (sc.put1[Q] == g) lds_put1(r0, a1, Q); });
    SB();
    static_for<8>([&](auto Q) { if constexpr (sc.fu[Q] == g) acc4(Q & 3, p[Q]); });
    static_for<4>([&](auto Q) { if constexpr (sc.f1[Q] == g) acc4(Q, p[Q]); });
    SB();
    mid(std::integral_constant<int, E0 + g>{});
    SB();
  });
#pragma unroll
  for (int q = 0; q < 4; ++q) {
    acc4(q, f32x4{a0[4 * q], a0[4 * q + 1], a0[4 * q + 2], a0[4 * q + 3]});
    res[q >> 1][2 * (q & 1)] = pk(u[4 * q], u[4 * q + 1]);
    res[q >> 1][2 * (q & 1) + 1] = pk(u[4 * q + 2], u[4 * q + 3]);
  }
}
DI void resid_store(rsrc_t rs, const v4u (&res)[2], u32 l16, u32 sto, u32 xoff, u32 soff) {
#pragma unroll
  for (int s = 0; s < 2; ++s) st128<SC1>(res[s], rs, l16 + s * 1024, xoff);
  SB();
  float s0 = 0.f, q0 = 0.f, s1 = 0.f, q1 = 0.f;
#pragma unroll
  for (int m = 0; m < 4; ++m) {
    s0 = dot2(res[0][m], ONES2, s0); q0 = dot2(res[0][m], res[0][m], q0);
    s1 = dot2(res[1][m], ONES2, s1); q1 = dot2(res[1][m], res[1][m], q1);
  }
  st64<SC1>(__builtin_bit_cast(v2u, f32x2{sum32(s0 + s1), sum32(q0 + q1)}), rs, sto, soff);
}

DI u32 cs_word(float part, u32 hf) {
  const float cs = sum32(part);
  const u32 H = pk(cs, cs);
  const float rem = cs - bflo(H);
  return hf ? pk(rem, rem) : H;
}
DI u32 mean_word(float nmean) { return pk(nmean, nmean - bflo(pk(nmean, nmean))); }
template <int I> DI f32x16 mean_fix(const v4u& cs4, u32 mw, f32x16 c) {
  v4u bm = {0u, 0u, 0u, 0u};
  bm[I] = mw;
  return mfma32(cs4, bm, c);
}
struct Stats { float t[4], s, q, nmean, rstd; u32 mw; };
template <int I> DI void stats_step(Stats& z, const f32x4 (&sp)[4]) {
  if constexpr (I < 3) {
#pragma unroll
    for (int m = 0; m < 4; ++m) { const float a = I ? z.t[m] : sp[0][m], c = sp[I + 1][m]; z.t[m] = a + c; PIN(z.t[m]); }
  } else if constexpr (I == 3) { z.s = mix16(z.t[0], z.t[2]); PIN(z.s); z.q = mix16(z.t[1], z.t[3]); PIN(z.q); }
  else if constexpr (I == 4) { z.nmean = sum32(z.s) * (-1.f / 512.f); PIN(z.nmean); }
  else if constexpr (I == 5) { z.q = sum32(z.q) * (1.f / 512.f); PIN(z.q); }
  else if constexpr (I == 6) { z.mw = mean_word(z.nmean); PIN(z.mw); }
  else if constexpr (I == 7) { z.rstd = __builtin_amdgcn_rsqf(fmaxf(z.q - z.nmean * z.nmean, 0.f) + 1e-5f); PIN(z.rstd); }
}

template <class Hook> DI void dual_softmax(const f32x16& p, float c, u32 (&o)[16], Hook&& hook) {
  float mx = p[0], mn = p[0];
#pragma unroll
  for (int i = 1; i < 16; ++i) { mx = fmaxf(mx, p[i]); mn = fminf(mn, p[i]); }
  float ap = -mx * c, an = mn * c;
  PIN(ap);
  PIN(an);
  SB();
  hook(std::integral_constant<int, 0>{});
  SB();
  f32x2 ep[8], en[8];
#pragma unroll
  for (int i = 0; i < 8; ++i) {
    const f32x2 pv = {p[2 * i], p[2 * i + 1]};
    ep[i] = pv * c + ap;
    en[i] = pv * (-c) + an;
    PIN(ep[i]);
    PIN(en[i]);
  }
  SB();
  hook(std::integral_constant<int, 1>{});
  SB();
  f32x2 sa, sc;
  static_for<8>([&](auto I) {
    constexpr int i = I;
    ep[i] = f32x2{__builtin_amdgcn_exp2f(ep[i].x), __builtin_amdgcn_exp2f(ep[i].y)};
    en[i] = f32x2{__builtin_amdgcn_exp2f(en[i].x), __builtin_amdgcn_exp2f(en[i].y)};
    PIN(ep[i]);
    PIN(en[i]);
    if constexpr (i == 2) { sa = ep[0] + ep[1]; sc = en[0] + en[1]; PIN(sa); PIN(sc); }
    if constexpr (i > 2) { sa += ep[i - 1]; sc += en[i - 1]; PIN(sa); PIN(sc); }
    SB();
    hook(std::integral_constant<int, 2 + i>{});
    SB();
  });
  sa += ep[7];
  sc += en[7];
  const float rp = __builtin_amdgcn_rcpf(sa.x + sa.y), rn = __builtin_amdgcn_rcpf(sc.x + sc.y);
#pragma unroll
  for (int i = 0; i < 8; ++i) { ep[i] *= rp; PIN(ep[i]); }
#pragma unroll
  for (int i = 0; i < 8; ++i) { en[i] *= rn; PIN(en[i]); }
  SB();
  hook(std::integral_constant<int, 10>{});
  SB();
#pragma unroll
  for (int i = 0; i < 8; ++i) { o[i] = pk(ep[i].x, ep[i].y); o[8 + i] = pk(en[i].x, en[i].y); }
}
constexpr int p3_g0(int v) { return (v * 58) / 48; }
constexpr bool p3_w2gap(int gi) { return (gi * W2B) / 8 != ((gi + 1) * W2B) / 8; }
constexpr int p3_pre(int layer, int g) {
  int n = layer == 0 && p3_w2gap(g);
  for (int q = 0; q < 6; ++q) n += p3_g0(8 * q + 7) + 5 == g;
  return n;
}
constexpr int p3_post(int g) { return (g == HS0) + (g == HS1) + (g == PK0); }
constexpr int p3_younger(int layer, int t, int gw) {
  const int gs = p3_g0(16 * t + 15) + 5;
  int n = p3_post(gs);
  for (int g = gs + 1; g < gw; ++g) n += p3_pre(layer, g) + p3_post(g);
  return n + (gw < 64 ? p3_pre(layer, gw) : 0);
}
static_assert(p3_g0(32) >= 35 && p3_g0(47) + 5 < 64 && p3_g0(15) + 5 < HS0 && p3_g0(31) + 5 < HS1 && HS0 != HS1, "P3 schedule");

__global__ void __launch_bounds__(256, 1)
k_main(const uint16_t* __restrict__ x, const uint16_t* __restrict__ wqkv, const uint16_t* __restrict__ wphi,
       const uint16_t* __restrict__ wo_, const uint16_t* __restrict__ w1, const uint16_t* __restrict__ w2,
       unsigned char* ws, unsigned char* out) {
  LAS unsigned char* L = (LAS unsigned char*)smem;
  const int b = blockIdx.x, tid = threadIdx.x, lane = tid & 63, w = __builtin_amdgcn_readfirstlane(tid >> 6);
  const int xcd = b & 7, j = b >> 3;
  const int rt = 4 * (xcd >> 1) + (j >> 3), hp = 8 * (xcd & 1) + (j & 7), tt = 4 * rt + w;
  const bool p3on = (j & 7) < 7;
  const int fb = p3on ? 7 * (xcd & 1) + (j & 7) : 0;
  const int rt2 = 4 * xcd + (j >> 3), cb = j & 7;
  const int tte = 2 * rt2 + (w >> 1), n0 = 64 * cb + 32 * (w & 1);
  const rsrc_t rs = mkrsrc((unsigned char*)(((size_t)ws + (size_t)WSB * MB2 + MB2 - 1) & ~(size_t)(MB2 - 1)), WS_END);
  const f32x16 zero16 = {0.f, 0.f, 0.f, 0.f, 0.f, 0.f, 0.f, 0.f, 0.f, 0.f, 0.f, 0.f, 0.f, 0.f, 0.f, 0.f};
  STAMP(63);
  v4u res[2];
  v4u csq = {0u, 0u, 0u, 0u}, cs1;
  v4u wph[2];
  u32 tag_lo, tag_hi, tws = 0;
  u32 sd;
  const u32 l0 = lane0_off();
  const u32 nlh = !p3on ? 0u : (fb == 3 || fb == 10) ? 2u : 1u;
  v4u xr[32];
  const uint16_t* xo = x;
  asm volatile("" : "+s"(xo));
  const rsrc_t rsx = mkrsrc(xo, S * D * 2);
  auto trfrag = [&](LAS unsigned char* p, int str) { const v2u t0 = trread(p), t1 = trread(p + str); return v4u{t0.x, t0.y, t1.x, t1.y}; };
  const u32 sv = ((lane >> 2) & 1) == ((lane >> 4) & 1) ? ONES2 : 0u;
  v4u selm = {sv, sv, sv, sv};
  if (b >= 248 && w < 2) {
#pragma unroll
    for (int i = 0; i < NCNT * CSTR / 16384; ++i) st128<SC1>(v4u{0u, 0u, 0u, 0u}, rs, lane * 16, CTRL + ((b - 248) * 2 + w) * (NCNT * CSTR / 16) + i * 1024);
  }
  const v2u ep = ld64<SC1>(rs, 0, EPOCH_OFF);
  SB();
  u32 cpf;
  {
    unsigned long long pc;
    u32 len;
    asm volatile("s_getpc_b64 %0\n.Lkcode0:\n\ts_mov_b32 %1, .Lkcode1-.Lkcode0" : "=s"(pc), "=s"(len));
    cpf = ld32<0>(mkrsrc((const void*)pc, len), w == CPW ? lane * 128 + (j & 7) * 8192 : 0x80000000u, 0);
  }
  SB();
  int kind = 4, nb = 0, kq = 0, ld = 512;
  const uint16_t* W = x;
  if (w == 0) {
    if (b < 192) { kind = 0; W = wqkv; ld = 1536; nb = b >> 3; kq = b & 7; }
  } else if (w >= 2) {
    const int hw = w & 1;
    if (b < 112) { kind = 1; W = w1; ld = 1792; nb = b >> 2; kq = (b & 3) * 2 + hw; }
    else if (b < 144) { kind = 2; W = wo_; nb = (b - 112) >> 2; kq = (b & 3) * 2 + hw; }
    else { kind = 3; W = w2; nb = (b - 144) / 14; kq = ((b - 144) - 14 * nb) * 2 + hw; }
  }
  LAS unsigned char* img = L + SCR + w * 8192;
  {
    const rsrc_t rsu = mkrsrc(W, kind < 4 ? (kind == 3 ? 1792 : 512) * ld * 2 : 0);
    const u32 vu = (lane >> 3) * ld * 2 + (((lane & 7) ^ ((lane >> 2) & 4)) * 16);
#pragma unroll
    for (int i = 0; i < 8; ++i) __builtin_amdgcn_raw_ptr_buffer_load_lds(rsu, (LAS u32*)(img + i * 1024), 16, vu, (64 * kq + 8 * i) * ld * 2 + nb * 128, 0, 0);
  }
  SB();
  const u32 vx = (lane >> 3) * 1024 + (lane & 7) * 16;
  const u32 rot = ROT ? j & 7 : 0;
  auto xld = [&](auto I) { constexpr int i = I; xr[i] = ld128<0>(rsx, vx, tt * 32768 + (i & 3) * 8192 + (((i >> 2) ^ rot) * 128)); SB(); };
  static_for<XA>([&](auto I) { xld(I); });
  u32 obase, cstr, kstr, kstr2, csb, ovo, cvo, ua;
  {
    const int t32 = 2 * nb, sec = t32 >> 4, tile = sec == 0 ? 2 : sec - 1;
    obase = opq(kind == 0 ? WQKV_T + (t32 & 15) * 98304 + tile * 32768 + kq * 4096 : kind == 1 ? W1_T + t32 * 32768 + kq * 4096
                : kind == 2 ? WO_T + (nb * 32 + kq * 4) * 2048 : W2_T + (nb * 112 + (kq & 1) * 56 + (kq >> 1) * 2) * 2048);
    cstr = opq(kind == 0 ? 98304 : kind == 1 ? 32768 : 1024);
    kstr = opq(kind < 2 ? 1024 : 2048);
    kstr2 = opq(kind == 3 ? 57344 : 2 * kstr);
    csb = opq(kind == 0 ? CS_QKV + (((t32 & 15) * 3 + tile) * 8 + kq) * 128 : CS_W1 + (t32 * 8 + kq) * 128);
    ovo = opqv(kind < 4 ? lane * 16 : 0x80000000u);
    cvo = opqv(kind < 2 ? (lane >> 5) * (kind == 0 ? 3072 : 1024) + (lane & 31) * 4 : 0x80000000u);
    const u32 c16 = lane & 15, Q = ((lane >> 2) & 4) + (lane & 3);
    const u32 Cc = kind == 0 ? 4 * (Q & 1) + (Q >> 1) : (Q & 4) + 2 * (Q & 1) + ((Q >> 1) & 1);
    ua = opqv((8 * (lane >> 5) + (c16 >> 2)) * 128 + (c16 >> 3) * 64 + Cc * 8);
  }
  const u32 jadd = opq(cnt_off(C_J + (b < 112 ? 16 + (b >> 3) : b < 144 ? (b - 112) >> 2 : (b - 144) / 14)));
  auto unit = [&]() {
    v4u o[8];
#pragma unroll
    for (int i = 0; i < 8; ++i) o[i] = trfrag(img + (ua ^ ((i >> 2) * 64)) + (i & 3) * 2048, 512);
    SB();
    f32x4 cs[2] = {{0.f, 0.f, 0.f, 0.f}, {0.f, 0.f, 0.f, 0.f}};
#pragma unroll
    for (int i = 0; i < 8; ++i) {
      st128<SC1>(o[i], rs, ovo, obase + (i >> 2) * cstr + ((i >> 1) & 1) * kstr2 + (i & 1) * kstr);
      cs[i >> 2] = mfma16(selm, o[i], cs[i >> 2]);
    }
    const float c0 = cs[0][0], c1 = cs[1][0];
    __builtin_amdgcn_raw_buffer_store_b32(__builtin_bit_cast(u32, lane < 32 ? c0 : c1), rs, cvo, csb, SC1);
    SB();
  };
  SB();
  const u32 tvo = opqv((((b >= 248 && w < 2) || kind == 0) && lane < 16) ? lane * 8 : 0x80000000u);
  const u32 tso = opq(b >= 248 ? INIT_OFF + (((b & 7) * 2 + w) & 15) * CSTR : FLAG_OFF + b * CSTR);
  SB();
  v4u w2f[28][2];

  static_for<2>([&](auto LAYER) __attribute__((always_inline)) {
    constexpr int layer = LAYER;
    constexpr int sb = layer * 29;
    v4u wo[8][2];
    const u32 wb = opq(W2_T + cb * 229376 + w * 14336);
    const u32 wsw = (w & 1) * 1024;
    u32 pvx[3], pvy[2];
    const u32 rx = ROT2 ? opq(rot * 4096) : 0u;
#define IC(n) std::integral_constant<int, n>{}
    {
      const u32 ln = opqv(lane), l16 = ln * 16, r = ln & 31, hf = ln >> 5;
      if constexpr (layer) STAMP(sb + 0);
      constexpr int DX = DXV;
      v4u xf[32];
      auto xtile = [&](int i, u32 t) { return XT + i * MB2 + (t & 31) * 65536 + (sd ^ ((i & 1) ? 0 : 8192)); };
      auto stile = [&](int i, u32 t) { return ST + i * ST_SZ + blk((t & 31) * 4096) + sd; };
      const u32 xb = layer ? xtile(1, tt) : 0;
      if constexpr (layer > 0 && XMODE == 1) apeek(rs, l0, (cnt_off(C_X + (tt & 31)) + sd), pvx[0]);
      SB();
      f32x4 sp[4];
      const u32 stb = layer ? stile(1, tt) : 0;
      const u32 wqb = opq(WQKV_T + hp * 98304 + 65536 + w * 8192);
      const u32 g4 = ln >> 4, c16 = ln & 15;
      LAS unsigned char* a_v = L + opqv(VV_OFF + (32 * w + r) * VV_STR + hf * 32);
      LAS unsigned char* a_pk = L + opqv(PK_OFF + (32 * w + r) * PK_STR + hf * 64);
      LAS unsigned char* a_pa = L + opqv(PK_OFF + (8 * g4 + (c16 >> 2)) * PK_STR + w * 32 + (ln & 3) * 8);
      LAS unsigned char* a_pb = L + opqv(VV_OFF + (8 * g4 + (c16 >> 2)) * VV_STR + (w >> 1) * 32 + (ln & 3) * 8);
      const u32 o_kv = opqv((g4 >> 1) * 256 + c16 * 16 + (g4 & 1) * 8), o_z = opqv(hf * 8192 + r * 4);
      u32 jv = 0, lat = 0, vv = 0, vb = 0;
      constexpr int wok = layer ? WOK : 0, NPF = 16 - wok + (layer ? 24 : 32);
      constexpr int s_q1 = 0, s_q2 = s_q1 + NQ1, s_od = s_q2 + NQ2, s_op = s_od + ODR, n_op = layer ? OPL1 : OPL0, s_p2 = s_op + n_op, s_xd = NPF - XDR;
      static_assert(s_p2 <= s_xd && s_xd - s_p2 <= 12 && s_od <= NPF - 8 && s_op >= 16 - wok, "prefetch schedule");
      LAS v4u* WL = (LAS v4u*)(L + l16);
      LAS v4u* WQ = (LAS v4u*)(L + opqv(l16 + 65536));
      const u32 wob = opq(WO_T + (cb * 4 + w) * 16384);
      auto wold = [&](auto I) {
        constexpr int i = I;
        wo[i >> 1][i & 1] = ld128<SC1>(rs, l16 + ((i >> 1) & 1) * 2048, wob + (i >> 2) * 4096 + ((i & 1) ? 1024 - wsw : wsw));
      };
      f32x16 aK = zero16, aV = zero16, aQ;
      float rstd, c;
      Stats zs_;
      const u32 jvo = opqv(ln == 0 ? cnt_off(C_J + cb) : ln == 1 ? cnt_off(C_J + 16 + fb) : 0x80000000u);
      float cp[3][4];
      v4u wk[3], wv[3];
      SB();
      if constexpr (layer == 0) {
        LAS unsigned char* sw[2];
        LAS unsigned char* sr[4];
#pragma unroll
        for (int i = 0; i < 2; ++i) sw[i] = L + opqv(XS_OFF + w * 8192 + (ln >> 3) * 128 + (((ln & 7) ^ (4 * i + (ln >> 4))) * 16));
#pragma unroll
        for (int f = 0; f < 4; ++f) sr[f] = L + opqv(XS_OFF + w * 8192 + r * 128 + (((2 * f + hf) ^ ((r >> 1) & 7)) * 16));
        const u32 fu = ln >> 1, fvo = opqv(ln >= 12 ? 0x80000000u : FLAG_OFF + ((fu < 4 ? (1 + (w >> 1)) * 64 + (w & 1) * 4 + fu : 2 * w + fu - 4) + (hp >> 1) * 8) * CSTR + (ln & 1) * 4);
        PIN(selm);
        SB();
        if (w == 0) {
          asm volatile("s_waitcnt vmcnt(%0)" ::"n"(XA) : "memory");
          STAMP(58);
          unit();
          STAMP(59);
        }
        asm volatile("s_barrier" ::: "memory");
        {
          const unsigned long long e = ((unsigned long long)__builtin_amdgcn_readfirstlane(ep.y) << 32) | __builtin_amdgcn_readfirstlane(ep.x);
          unsigned long long t = e * 0x9E3779B97F4A7C15ull + 0x632BE59BD9B4E019ull;
          if (t == e) t = ~e;
          tag_lo = (u32)t;
          tag_hi = (u32)(t >> 32);
        }
        const u32 fneed = (ln & 1) ? tag_hi : tag_lo;
        f32x4 sa = {0.f, 0.f, 0.f, 0.f};
        f32x16 gr = zero16;
        u32 fv;
        v2u tg;
        SB();
        auto vm = [&](auto N) {
          constexpr int n = N, xi = XA + n, di = n - 8 * FCK;
          if constexpr (xi < 32) xld(IC(xi));
          if constexpr (xi == 32) __builtin_amdgcn_raw_ptr_buffer_load_lds(mkrsrc(wphi, 16384), (LAS u32*)(L + XS_OFF + w * 8192 + 4096), 16, l16, hp * 1024, 0, 0);
          if constexpr (xi == 33) tws = ld32<0>(rs, TLBW ? (ln + 64 * w + (TLBD ? 256 * j : 0)) * TLBW : 0x80000000u, 0);
          if constexpr (xi == XB) {
            if (w == 0) asm volatile("s_waitcnt vmcnt(%0)" ::"n"(XB - XA + 1) : "memory");
            st64<SC1>(v2u{tag_lo, tag_hi}, rs, tvo, tso);
            STAMP(60);
          }
          if constexpr (di >= 0 && di < 16)
            dma1<(di & 3)>(rs, l16, WQKV_T + hp * 98304 + w * 16384 + (di >> 2) * 4096, L + (w >> 1) * 32768 + ((((w & 1) * 4 + (di >> 2)) ^ rot) * 4096));
          if constexpr (di == 16) tg = ld64<SC1>(rs, c16 * CSTR, INIT_OFF);
          SB();
        };
        static_assert(XA + 8 * FCK > 33 && XB < 32 && FCK <= 7 && UB >= FCK + 3 && XA >= 4, "start schedule");
        static_for<11>([&](auto B) {
          constexpr int blk = B;
          if constexpr (blk == UB) { if (w >= 2) unit(); }
          if constexpr (blk == FPK) { fv = peek(rs, fvo, 0); SB(); }
          if constexpr (blk == FCK) {
            STAMP(0);
            if ((__builtin_amdgcn_ballot_w64(fv == fneed) & 0xfffull) != 0xfffull) {
#pragma unroll 1
              for (u32 spins = 0; spins < (1u << 16); ++spins)
                if ((__builtin_amdgcn_ballot_w64(peek(rs, fvo, 0) == fneed) & 0xfffull) == 0xfffull) break;
            }
            asm volatile("" ::: "memory");
            STAMP(61);
          }
          if constexpr (blk < 10) static_for<4>([&](auto I) {
            constexpr int i = I;
            if constexpr (blk > 1) gr = mfma32(xf[4 * blk - 8 + i], xf[4 * blk - 8 + i], gr);
            SB();
            vm(IC(8 * blk + 2 * i));
            if constexpr (blk < 8) *(LAS v4u*)(sw[i & 1] + i * 1024) = xr[4 * blk + i];
            SB();
            if constexpr (blk > 1) { sa = mfma16(selm, xf[4 * blk - 8 + i], sa); asm volatile("" : "+a"(sa)); }
            SB();
            vm(IC(8 * blk + 2 * i + 1));
          });
          if constexpr (blk < 8) static_for<4>([&](auto F) { xf[4 * blk + F] = *(LAS v4u*)(sr[F]); SB(); });
        });
        asm volatile("" ::"v"(tws));
        asm volatile("" ::"v"(cpf));
        {
          const u32 Qc = 4 * (g4 & 1) + (c16 & 3);
          LAS unsigned char* a_ph = L + opqv(XS_OFF + w * 8192 + 4096 + hf * 512 + (c16 >> 2) * 32 + (Qc >> 1) * 8);
#pragma unroll
          for (int s = 0; s < 2; ++s) wph[s] = trfrag(a_ph + s * 256, 128);
        }
        const float sa0 = sa[0];
        const float nmean = sa0 * (-1.f / 512.f);
        zs_.mw = mean_word(nmean);
        auto sel = [](bool cnd, float a0, float a1) { PIN(a0); PIN(a1); return cnd ? a1 : a0; };
        auto g4f = [&](int i) { return sel(r & 2, sel(r & 1, gr[4 * i], gr[4 * i + 1]), sel(r & 1, gr[4 * i + 2], gr[4 * i + 3])); };
        const float gd = sel(r & 16, sel(r & 8, g4f(0), g4f(1)), sel(r & 8, g4f(2), g4f(3)));
        const bool dl = ((r >> 2) & 1) == hf;
        const float dg = dl ? gd : 0.f;
        rstd = __builtin_amdgcn_rsqf(fmaxf(sum32(dg) * (1.f / 512.f) - nmean * nmean, 0.f) + 1e-5f);
        c = rstd * LOG2E;
#pragma unroll
        for (int s = 0; s < 2; ++s)
#pragma unroll
          for (int m = 0; m < 4; ++m) wph[s][m] = dl ? wph[s][m] : 0u;
        PIN(wph[0]);
        PIN(wph[1]);
        PIN(c);
        PIN(rstd);
        PIN(zs_.mw);
        SB();
        if constexpr (UB > FCK) { if (w >= 2) asm volatile("s_waitcnt vmcnt(9)" ::: "memory"); else DRAIN(); }
        else DRAIN();
        if (w == 0 && !__all(tg.x == tag_lo && tg.y == tag_hi)) {
#pragma unroll 1
          for (u32 spins = 0; spins < (1u << 16); ++spins) {
            asm volatile("" ::: "memory");
            const v2u t = ld64<SC1>(rs, c16 * CSTR, INIT_OFF);
            if (__all(t.x == tag_lo && t.y == tag_hi)) break;
          }
        }
        BAR();
#pragma unroll
        for (int i = 0; i < 2; ++i) { wk[i] = WL[i * 64]; wv[i] = WL[(32 + i) * 64]; }
        if constexpr (UB <= FCK) add1(rs, w >= 2 ? l0 : 0x80000000u, jadd);
        SB();
      } else {
        if constexpr (XMODE == 0) { NAP(PS3); pvx[0] = peek(rs, l0, (cnt_off(C_X + (tt & 31)) + sd)); SB(); }
        if constexpr (XMODE == 3) { NAP(PS3); apeek(rs, l0, (cnt_off(C_X + (tt & 31)) + sd), pvy[0]); NAP(PS5); apeek(rs, l0, (cnt_off(C_X + (tt & 31)) + sd), pvy[1]); }
        if constexpr (XMODE == 1) { NAP(PS1); apeek(rs, l0, (cnt_off(C_X + (tt & 31)) + sd), pvx[1]); NAP(PS2); apeek(rs, l0, (cnt_off(C_X + (tt & 31)) + sd), pvx[2]); SB(); }
        asm volatile("s_waitcnt vmcnt(%0)" ::"n"(XMODE == 1 ? 4 : XMODE == 3 ? 3 : XMODE == 0 ? 2 : 1) : "memory");
        BAR();
#pragma unroll
        for (int i = 0; i < 2; ++i) { wk[i] = WL[i * 64]; wv[i] = WL[(32 + i) * 64]; }
        SB();
        if constexpr (XMODE == 1) pfinish<2, 1, 0>(rs, l0, (cnt_off(C_X + (tt & 31)) + sd), 16, pvx);
        else if constexpr (XMODE == 3) pfinish2<1, 0>(rs, l0, (cnt_off(C_X + (tt & 31)) + sd), 16, pvy);
        else if constexpr (XMODE == 0) wfinish(rs, l0, (cnt_off(C_X + (tt & 31)) + sd), 16, pvx[0]);
        else { NAP(PS3); wwait(rs, l0, (cnt_off(C_X + (tt & 31)) + sd), 16); }
        STAMP(sb + 1);
#pragma unroll
        for (int ks = 0; ks < DX; ++ks) { xf[ks] = ldr(rs, l16, xb, ks); SB(); }
      }
      STAMP(sb + 26);
      static_for<32>([&](auto KSI) {
        constexpr int ks = KSI, q0 = layer ? 20 : 8;
        aK = mfma32(wk[ks % 3], xf[ks], aK);
        SB();
        if constexpr (ks + 2 < 32) wk[(ks + 2) % 3] = WL[(ks + 2) * 64];
        SB();
        if constexpr (layer > 0 && ks + DX < 32) xf[ks + DX] = ldr(rs, l16, xb, ks + DX);
        if constexpr (ks >= q0 && ks < q0 + 8) dma1<((ks - q0) & 3)>(rs, l16, wqb + ((ks - q0) >> 2) * 4096, L + 65536 + (((2 * w + ((ks - q0) >> 2)) ^ (layer ? 0 : rot)) * 4096));
        if constexpr (ks >= 28 && ks < 28 + wok) wold(IC(ks - 28));
        if constexpr (layer == 0 && ks < 6) cp[ks >> 1][2 * (ks & 1)] = __builtin_bit_cast(float, ld32<SC1>(rs, ln * 4 + (ks & 1) * 512 + (ks >> 1) * 1024, CS_QKV + hp * 3072));
        SB();
        aV = mfma32(wv[ks % 3], xf[ks], aV);
        SB();
        if constexpr (ks + 2 < 32) wv[(ks + 2) % 3] = WL[(32 + ks + 2) * 64];
        SB();
        if constexpr (layer == 0) {
          if constexpr (ks < 6) cp[ks >> 1][2 * (ks & 1) + 1] = __builtin_bit_cast(float, ld32<SC1>(rs, ln * 4 + (ks & 1) * 512 + 256 + (ks >> 1) * 1024, CS_QKV + hp * 3072));
          if constexpr (ks == 18 || ks == 22) res[(ks - 18) >> 2] = ld128<0>(rsx, r * 1024 + hf * 16 + ((ks - 18) >> 2) * 32, tte * 32768 + n0 * 2);
          if constexpr (ks == 19 || ks == 21 || ks == 23) { constexpr int t = (ks - 19) / 2; cp[t][0] = (cp[t][0] + cp[t][1]) + (cp[t][2] + cp[t][3]); PIN(cp[t][0]); }
          if constexpr (ks == 20 || ks == 22 || ks == 24) { constexpr int t = (ks - 20) / 2; u32 cw = cs_word(cp[t][0], hf); PIN(cw); csq[t] = cw; }
          if constexpr (ks == 19) add1(rs, w >= 2 ? l0 : 0x80000000u, jadd);
        } else {
          if constexpr (ks < 4) sp[ks] = __builtin_bit_cast(f32x4, ld128<SC1>(rs, l16 + ks * 1024, stb));
          if constexpr (ks == 1) pretire(pvy);
          if constexpr (ks >= 16 && ks < 24) stats_step<ks - 16>(zs_, sp);
          if constexpr (ks == 24) { rstd = zs_.rstd; c = rstd * LOG2E; PIN(c); }
        }
        SB();
        if constexpr (ks == 25) aK = mean_fix<0>(csq, zs_.mw, aK);
        if constexpr (ks == 27) aV = mean_fix<1>(csq, zs_.mw, aV);
        SB();
      });
      STAMP(sb + 2);
      const u32 w1b = opq(W1_T + fb * 131072);
      auto w1dma = [&](auto I) {
        constexpr int i = I;
        const u32 o = i < 16 ? w * 16384 + (i >> 2) * 4096 : (i < 24 ? 98304 : 65536) + w * 8192 + ((i >> 2) & 1) * 4096;
        dma1<(i & 3)>(rs, l16, w1b + o, L + (o ^ rx));
      };
      auto pf = [&](auto N) {
        constexpr int n = N + wok;
        if constexpr (n < 16) wold(IC(n));
        else w1dma(IC(n - 16 + (layer && n >= 32 ? 8 : 0)));
        SB();
      };
      {
        const v4u k0 = pack8(aK, 0), k1 = pack8(aK, 1);
        f32x16 p = mfma32(wph[0], k0, zero16);
        p = mfma32(wph[1], k1, p);
        SB();
        if constexpr (layer == 0 && PRB == 0) { vv = peek(rs, l0, cnt_off(C_H + (b >> 5) * 128 + (w & 1) * 64 + (b & 31) * 2 + (w >> 1))); SB(); }
#pragma unroll
        for (int i = 0; i < 16; ++i) { aV[i] *= rstd; PIN(aV[i]); }
        SB();
        *(LAS v4u*)(a_v) = pack8(aV, 0);
        *(LAS v4u*)(a_v + 16) = pack8(aV, 1);
        SB();
        if constexpr (layer == 0 && PRB == 1) { vv = peek(rs, l0, cnt_off(C_H + (b >> 5) * 128 + (w & 1) * 64 + (b & 31) * 2 + (w >> 1))); SB(); }
        u32 o[16];
        auto look = [&](auto I) {
          if constexpr (layer == 0) {
            u32 t;
            if constexpr (I >= LK0) asm volatile("s_getreg_b32 %1, hwreg(HW_REG_IB_STS, 0, 4)\n\ts_cmp_gt_u32 %1, %2\n\ts_addc_u32 %0, %0, 0" : "+s"(lat), "=&s"(t) : "n"(I > JPK ? 1 : 0) : "scc");
            if constexpr (I == JPK) jv = peek(rs, w != 0 ? 0x80000000u : jvo, 0);
          }
        };
        dual_softmax(p, c, o, look);
        static_for<4>([&](auto I) {
          constexpr int i = I;
          *(LAS v4u*)(a_pk + i * 16) = v4u{o[4 * i], o[4 * i + 1], o[4 * i + 2], o[4 * i + 3]};
          if constexpr (layer == 0 && (i & 1)) { SB(); look(IC(11)); SB(); }
        });
        if constexpr (layer == 0) *(LAS u32*)(L + XS_OFF + w * 4) = lat;
      }
      SB();
      aQ = mean_fix<2>(csq, zs_.mw, zero16);
      STAMP(sb + 3);
      asm volatile("s_waitcnt vmcnt(%0)" ::"n"(wok) : "memory");
      if constexpr (layer == 0) {
        asm volatile("" ::"v"(vv));
        const u32 need = ln == 0 ? 36u : ln == 1 ? 16u : 0u;
        if (w == 0 && (__builtin_amdgcn_ballot_w64(jv >= need) & 7ull) != 7ull) poll3(rs, jvo, need);
      }
      BAR();
      v4u lt;
      if constexpr (layer == 0) { lt = *(LAS v4u*)(L + XS_OFF); sd = 0; }
      const u32 pbase = KVP + (layer * 64 + 4 * hp + w) * 16384, zbase = ZP + (layer * 16 + hp) * 16384 + w * 2048;
      v4u wq[4];
      auto qfrag = [&](int ks) { return WQ[ks * 64]; };
      {
        f32x4 kv = {0.f, 0.f, 0.f, 0.f}, zz = {0.f, 0.f, 0.f, 0.f};
        const v4u ones = {ONES2, ONES2, ONES2, ONES2};
        v2u a0[4], a1[4], b0[4], b1[4];
#pragma unroll
        for (int ks = 0; ks < 4; ++ks) {
          a0[ks] = trread(a_pa + (32 * ks) * PK_STR); a1[ks] = trread(a_pa + (32 * ks + 4) * PK_STR);
          b0[ks] = trread(a_pb + (32 * ks) * VV_STR); b1[ks] = trread(a_pb + (32 * ks + 4) * VV_STR);
          SB();
        }
#pragma unroll
        for (int ks = 0; ks < 4; ++ks) {
          const v4u A = {a0[ks].x, a0[ks].y, a1[ks].x, a1[ks].y}, B = {b0[ks].x, b0[ks].y, b1[ks].x, b1[ks].y};
          kv = mfma16(A, B, kv);
          zz = mfma16(ones, A, zz);
          asm volatile("" : "+a"(zz));
          SB();
        }
#pragma unroll
        for (int ks = 0; ks < 4; ++ks) wq[ks] = qfrag(ks);
        SB();
        const v2u kvb = {pk(kv[0], kv[1]), pk(kv[2], kv[3])};
        st64<SC1>(kvb, rs, o_kv, pbase + rt * 512 + sd);
        st64<SC1>(kvb, rs, o_kv, pbase + rt * 512 + (sd ^ 8192));
        const float z0 = zz[0];
        __builtin_amdgcn_raw_buffer_store_b32(__builtin_bit_cast(u32, z0), rs, o_z, zbase + rt * 128, SC1);
      }
      SB();
      STAMP(sb + 4);
      auto qstep = [&](auto I) {
        constexpr int i = I;
        aQ = mfma32(wq[i & 3], xf[i], aQ);
        SB();
        if constexpr (i + 4 < 32) wq[i & 3] = qfrag(i + 4);
      };
      LAS unsigned char *a_kz, *a_kw, *a_kr;
      u32 o_st, o_zl, l2k;
      v4u sel;
      if constexpr (layer == 0) {
        const int df = __builtin_amdgcn_readfirstlane((int)(lt[0] + lt[2]) - (int)(lt[1] + lt[3]));
        sd = df > 0 ? 8192 : 0;
        const u32 vote = df == 0 ? 1 : (((df > 0) ^ (xcd >> 2)) & 1) * 2;
        (void)__builtin_amdgcn_raw_ptr_buffer_atomic_add_i32(1 + (vote << 16), rs, (w == 0 && j < 4 && ln < 16) ? ln * CSTR : 0x80000000u, cnt_off(C_V), 0);
        SB();
      }
      const u32 skv = cnt_off(C_KV + layer * 128 + hp * 4 + w), ckv = skv + sd;
      u32 pvk[2];
      static_for<32>([&](auto I) {
        constexpr int i2 = I - QS, n2 = 32 - QS;
        if constexpr (I == QS) { wsignal<NQ1 + (layer ? 0 : 1)>(rs, l2k, skv); STAMP(sb + 5); }
        qstep(I);
        if constexpr (I < QS) { if constexpr ((I * NQ1 + QS - 1) / QS != ((I + 1) * NQ1 + QS - 1) / QS) pf(IC(s_q1 + (I * NQ1 + QS - 1) / QS)); }
        else {
          if constexpr (layer == 0 && i2 == VPK) { vv = peek(rs, w == 0 ? l0 : 0x80000000u, cnt_off(C_V + (b & 15))); SB(); }
          if constexpr ((i2 == KVPK1 || i2 == KVPK2) && KMODE == 2) apeek(rs, l0, ckv, pvk[i2 == KVPK2]);
          if constexpr ((i2 * NQ2 + n2 - 1) / n2 != ((i2 + 1) * NQ2 + n2 - 1) / n2) pf(IC(s_q2 + (i2 * NQ2 + n2 - 1) / n2));
        }
        const u32 l2 = opqv(l16) >> 4, g2 = l2 >> 4, c2 = l2 & 15;
        if constexpr (I == 2) l2k = opqv(l2 < 2 ? l2 * 8192 : 0x80000000u);
        if constexpr (I == 1) o_zl = opqv(g2 * 128 + c2 * 4);
        if constexpr (I == 3) a_kz = L + opqv(KF_OFF + w * 544 + (c2 >> 3) * 272 + 256 + (c2 & 7) * 2);
        if constexpr (I == 5) a_kw = L + opqv(KF_OFF + w * 544 + (g2 >> 1) * 272 + (g2 & 1) * 8 + rho32(c2 & 7, c2 >> 3) * 16);
        if constexpr (I == 7) a_kr = L + opqv(KF_OFF + (l2 >> 5) * 272 + ((l2 & 31) < 16 ? (l2 & 31) : 16) * 16);
        if constexpr (I == 9) o_st = opqv(l2 >= 32 ? 0x80000000u : c2 * 16 + g2 * 8);
        if constexpr (I == 11 || I == 13 || I == 15 || I == 17) {
          constexpr int m = (I - 11) / 2;
          sel[m] = opqv((c2 >> 1) == 4 * (g2 & 1) + m ? 0x3f80u << (16 * (c2 & 1)) : 0u);
        }
        SB();
      });
      const v4u q0 = pack8(aQ, 0), q1 = pack8(aQ, 1);
      f32x16 pq = mfma32(wph[0], q0, zero16);
      pq = mfma32(wph[1], q1, pq);
      SB();
      v4u phq[4];
      v4u v[8];
      f32x4 z;
      {
        u32 o[16];
        dual_softmax(pq, c, o, [&](auto I) {
          constexpr int i = I;
          if constexpr (i == KVCHK) {
            STAMP(sb + 6);
            if constexpr (KMODE == 0) wfinish(rs, l0, ckv, 16, pvk[0]);
            else pfinish2<1 + NQ2 - (KVPK1 * NQ2 + 31 - QS) / (32 - QS) + (!layer && VPK > KVPK1), NQ2 - (KVPK2 * NQ2 + 31 - QS) / (32 - QS) + (!layer && VPK > KVPK2)>(rs, l0, ckv, 16, pvk);
            STAMP(sb + 7);
#pragma unroll
            for (int k = 0; k < 4; ++k) z[k] = __builtin_bit_cast(float, ld32<SC1>(rs, o_zl + k * 512, zbase + sd));
            SB();
          }
          static_for<8>([&](auto K) {
            constexpr int k = K, at = KVCHK + (k + 1) / KVLD;
            if constexpr ((at < 9 ? at : 9) == i) { {
              v[k] = ldf<SC1>(rs, l16, pbase + (sd ^ 8192), k * 1024);
              SB();
              if constexpr (layer == 0 && k == 7) { vb = peek(rs, w == 0 ? l0 : 0x80000000u, cnt_off(C_V + (b & 15))); SB(); }
            } }
          });
        });
#pragma unroll
        for (int m = 0; m < 4; ++m) {
          auto sw = __builtin_amdgcn_permlane32_swap(o[m], o[4 + m], false, false);
          phq[0][m] = sw[0];
          phq[2][m] = sw[1];
          auto sx = __builtin_amdgcn_permlane32_swap(o[8 + m], o[12 + m], false, false);
          phq[1][m] = sx[0];
          phq[3][m] = sx[1];
        }
#pragma unroll
        for (int m = 0; m < 4; ++m) PIN(phq[m]);
      }
      SB();
      {
        const float zs = sum32(sum16((z[0] + z[1]) + (z[2] + z[3])));
        *(LAS unsigned short*)(a_kz) = (unsigned short)pk(zs, zs);
        if constexpr (KMODE == 2) pretire(pvk);
        SB();
        f32x4 sv = {0.f, 0.f, 0.f, 0.f};
#pragma unroll
        for (int i = 0; i < 8; ++i) { sv = mfma16(sel, v[i], sv); SB(); }
        *(LAS v2u*)(a_kw) = v2u{pk(sv[0], sv[1]), pk(sv[2], sv[3])};
      }
      if constexpr (layer == 0) {
        if (w == 0) {
          if ((__builtin_amdgcn_readfirstlane(vv) & 0xffffu) < 32u) {
            vv = vb;
#pragma unroll 1
            for (u32 spins = 0; spins < (1u << 16) && (__builtin_amdgcn_readfirstlane(vv) & 0xffffu) < 32u; ++spins) vv = peek(rs, l0, cnt_off(C_V + (b & 15)));
          }
          if (ln == 0) *(LAS u32*)(L + XS_OFF + 16) = vv;
        }
      }
      BAR();
      if constexpr (layer == 0) vv = *(LAS u32*)(L + XS_OFF + 16);
      STAMP(sb + 8);
      {
        v4u kvf[4];
#pragma unroll
        for (int i = 0; i < 4; ++i) kvf[i] = *(LAS v4u*)(a_kr + i * 544);
        SB();
        f32x16 nd[2];
        nd[0] = mfma32(kvf[0], phq[0], zero16);
        nd[0] = mfma32(kvf[1], phq[1], nd[0]);
        nd[1] = mfma32(kvf[2], phq[2], zero16);
        nd[1] = mfma32(kvf[3], phq[3], nd[1]);
        SB();
        if constexpr (layer == 0) {
          sd = opq(((((u32)__builtin_amdgcn_readfirstlane(vv) >> 16 > 32u ? 1u : 0u) ^ (xcd >> 2) ^ NFLIP) & 1u) * 8192);
        }
#pragma unroll
        for (int h = 0; h < 2; ++h) {
          const float inv = __builtin_amdgcn_rcpf(nd[h][8] + 1e-6f);
          v4u ob;
#pragma unroll
          for (int m = 0; m < 4; ++m) ob[m] = pk(nd[h][2 * m] * inv, nd[h][2 * m + 1] * inv);
          st128<SC1>(ob, rs, l16 + h * 1024, OB + layer * MB2 + (tt & 31) * 65536 + blk(hp * 2048) + (sd ^ (layer * 8192)));
          SB();
        }
      }
      STAMP(sb + 9);
      static_for<ODR>([&](auto I) { pf(IC(s_od + I)); });
      wsignal<ODR>(rs, l0, cnt_off(C_O + layer * 128 + ((tt >> 1) & 15) * 4 + (hp >> 2)) + sd);
      if constexpr (layer == 0) asm volatile("" ::"v"(vb));
      STAMP(sb + 10);
      const u32 co = cnt_off(C_O + layer * 128 + (rt2 & 15) * 4 + w) + sd;
      u32 pvo[3], pvz[2];
      BAR();
      if constexpr (OMODE == 1) {
        apeek(rs, l0, co, pvo[0]);
        NAP(PS2);
        apeek(rs, l0, co, pvo[1]);
        NAP(PS2);
        apeek(rs, l0, co, pvo[2]);
        static_for<n_op>([&](auto I) { pf(IC(s_op + I)); });
        pfinish<2 + n_op, 1 + n_op, n_op>(rs, l0, co, 8, pvo);
      } else if constexpr (OMODE == 2) {
        NAP(PS4);
        apeek(rs, l0, co, pvz[0]);
        static_for<n_op>([&](auto I) { pf(IC(s_op + I)); });
        NAP(PS6);
        apeek(rs, l0, co, pvz[1]);
        pfinish2<1 + n_op, 0>(rs, l0, co, 8, pvz);
      } else {
        NAP(PS4);
        pvo[0] = peek(rs, l0, co);
        SB();
        static_for<n_op>([&](auto I) { pf(IC(s_op + I)); });
        wfinish(rs, l0, co, 8, pvo[0]);
      }
      STAMP(sb + 11);
      const u32 ab = OB + layer * MB2 + w * 16384 + (sd ^ (layer * 8192));
      v4u af[DXV];
      splitk_phase<FrO, DXV, 0>(rs, wo, af, ab + (tte & 31) * 65536, ab + ((tte & 31) ^ 1) * 65536, L, w, l16, 1.f, res, [&](int) {}, [&]() {}, [&](auto I) {
        if constexpr (I == 1 && OMODE == 1) pretire(pvo);
        if constexpr (I == 1 && OMODE == 2) pretire(pvz);
        if constexpr (I >= 4 && s_p2 + I - 4 < s_xd) pf(IC(s_p2 + I - 4));
      });
      STAMP(sb + 12);
      resid_store(rs, res, l16, o_st, xtile(2 * layer, tte) + blk(n0 * 64), stile(2 * layer, tte) + (n0 >> 5) * 256);
      STAMP(sb + 13);
      static_for<XDR>([&](auto I) { pf(IC(s_xd + I)); });
      wsignal<XDR>(rs, l0, (cnt_off(C_X1 + layer * 32 + (tte & 31)) + sd));
      STAMP(sb + 14);
    }
    const u32 lh = opqv(opqv(lane) < nlh ? lane * CSTR : 0x80000000u);
    v4u af[WINH];
    const u32 ch0 = cnt_off(C_H + layer * 512 + (rt2 & 15) * 4 + w) + sd;
    const u32 sh0 = cnt_off(C_H + layer * 512 + ((tt >> 1) & 15) * 4 + (2 * fb) / 7) + sd;
    auto htile = [&](u32 t) { return HB + layer * 4 * MB2 + (t & 31) * 262144 + (sd ^ ((t & 8) * 1024)); };
    auto xtile = [&](int i, u32 t) { return XT + i * MB2 + (t & 31) * 65536 + (sd ^ ((i & 1) ? 0 : 8192)); };
    auto stile = [&](int i, u32 t) { return ST + i * ST_SZ + blk((t & 31) * 4096) + sd; };
    const u32 hown = htile(tte) + w * 16384, hoth = hown ^ 262144;
    {
      const u32 ln = opqv(lane), l16 = ln * 16, r = ln & 31, hf = ln >> 5;
      auto w2ld = [&](auto I) {
        constexpr int i = I, k = (i >> 1) % 7;
        w2f[i >> 1][i & 1] = ld128<SC1>(rs, l16 + (k & 1) * 2048, wb + (i / 14) * 57344 + (k >> 1) * 4096 + ((i & 1) ? 1024 - wsw : wsw));
      };
      auto hld = [&](int i) { if (i < HPRE) { af[i] = ld_fr<FrH>(rs, l16, hown, hoth, i); SB(); } };
      if (p3on) {
        static_for<3>([&](auto J) {
          if constexpr (XMODE == 1) apeek(rs, l0, (cnt_off(C_X1 + layer * 32 + (tt & 31)) + sd), pvx[J]);
          if constexpr (XMODE == 0 && J == (layer ? 0 : 1)) { if constexpr (layer) NAP(PS3 + 1); pvx[0] = peek(rs, l0, (cnt_off(C_X1 + layer * 32 + (tt & 31)) + sd)); }
          if constexpr (XMODE == 3 && J > 0) { if constexpr (layer) NAP(J == 1 ? PS3 + 1 : PS5); apeek(rs, l0, (cnt_off(C_X1 + layer * 32 + (tt & 31)) + sd), pvy[J - 1]); }
          SB();
          if constexpr (layer == 0) static_for<W2D / 3>([&](auto I) { w2ld(IC(J * (W2D / 3) + I)); SB(); });
          else if constexpr (J < 2 && XMODE == 1) NAP(PS2);
        });
        constexpr int DX = DXV;
        v4u xf[32];
        const u32 xb = xtile(2 * layer, tt);
        f32x4 sp[4];
        const u32 stb = stile(2 * layer, tt);
        LAS v4u* WL = (LAS v4u*)(L + l16);
        LAS v4u* WH = (LAS v4u*)(L + opqv(l16 + 65536));
        u32 hb[2];
#pragma unroll
        for (int s = 0; s < 2; ++s) hb[s] = opq(htile(tt) + ((2 * fb + s) / 7) * 16384 + ((2 * fb + s) % 7) * 1024);
        SB();
        asm volatile("s_waitcnt vmcnt(%0)" ::"n"((layer == 0 ? W2D : 0) + (XMODE == 1 ? 4 : XMODE == 3 ? 3 : XMODE == 0 ? 2 : 1)) : "memory");
        BAR();
        v4u wa[3], wc[3], wq[4];
#pragma unroll
        for (int i = 0; i < 2; ++i) { wa[i] = WL[i * 64]; wc[i] = WL[(32 + i) * 64]; }
        SB();
        STAMP(sb + 15);
        if constexpr (XMODE == 0) wfinish(rs, l0, (cnt_off(C_X1 + layer * 32 + (tt & 31)) + sd), 16, pvx[0]);
        else if constexpr (XMODE == 3) pfinish2<1 + (layer ? 0 : 2 * W2D / 3), (layer ? 0 : W2D / 3)>(rs, l0, (cnt_off(C_X1 + layer * 32 + (tt & 31)) + sd), 16, pvy);
        else if constexpr (XMODE == 2) { if constexpr (layer) NAP(PS3); wwait(rs, l0, (cnt_off(C_X1 + layer * 32 + (tt & 31)) + sd), 16); }
        else pfinish<2 + (layer ? 0 : W2D), 1 + (layer ? 0 : 2 * W2D / 3), (layer ? 0 : W2D / 3)>(rs, l0, (cnt_off(C_X1 + layer * 32 + (tt & 31)) + sd), 16, pvx);
        STAMP(sb + 16);
#pragma unroll
        for (int ks = 0; ks < DX; ++ks) { xf[ks] = ldr(rs, l16, xb, ks); SB(); }
        f32x16 acc[4];
        acc[0] = zero16;
        acc[1] = zero16;
        float nc, cp[4][4];
        Stats zs_;
        SB();
        static_for<32>([&](auto KSI) {
          constexpr int ks = KSI;
          acc[0] = mfma32(wa[ks % 3], xf[ks], acc[0]);
          SB();
          if constexpr (ks + 2 < 32) wa[(ks + 2) % 3] = WL[(ks + 2) * 64];
          else wq[2 * (ks - 30)] = WH[(2 * (ks - 30)) * 64];
          SB();
          if constexpr (ks + DX < 32) xf[ks + DX] = ldr(rs, l16, xb, ks + DX);
          if constexpr (layer == 0 && ks >= 20) w2ld(IC(W2D + ks - 20));
          if constexpr (layer == 0 && ks >= 20 && ks < 28) {
            constexpr int t = (ks - 20) / 2;
            if constexpr (ks & 1) { u32 cw = cs_word(cp[t][0], hf); PIN(cw); cs1[t] = cw; }
            else { cp[t][0] = (cp[t][0] + cp[t][1]) + (cp[t][2] + cp[t][3]); PIN(cp[t][0]); }
          }
          SB();
          acc[1] = mfma32(wc[ks % 3], xf[ks], acc[1]);
          SB();
          if constexpr (ks + 2 < 32) wc[(ks + 2) % 3] = WL[(32 + ks + 2) * 64];
          else wq[2 * (ks - 30) + 1] = WH[(2 * (ks - 30) + 1) * 64];
          SB();
          if constexpr (ks < 4) sp[ks] = __builtin_bit_cast(f32x4, ld128<SC1>(rs, l16 + ks * 1024, stb));
          if constexpr (layer == 0 && ks >= 4 && ks < 8) {
#pragma unroll
            for (int i = 0; i < 4; ++i) cp[ks - 4][i] = __builtin_bit_cast(float, ld32<SC1>(rs, ln * 4 + i * 256 + (ks - 4) * 1024, CS_W1 + fb * 4096));
          }
          if constexpr (ks == 1) pretire(pvy);
          if constexpr (ks >= 16 && ks < 24) stats_step<ks - 16>(zs_, sp);
          if constexpr (ks == 24) { nc = zs_.rstd * -LOG2E; PIN(nc); }
          SB();
          if constexpr (ks == 27) acc[0] = mean_fix<0>(cs1, zs_.mw, acc[0]);
          if constexpr (ks == 28) acc[1] = mean_fix<1>(cs1, zs_.mw, acc[1]);
          if constexpr (ks == 29) acc[2] = mean_fix<2>(cs1, zs_.mw, zero16);
          if constexpr (ks == 30) acc[3] = mean_fix<3>(cs1, zs_.mw, zero16);
          SB();
        });
        STAMP(sb + 17);
        float tv[64], ev[64];
        v4u ho[8];
        u32 pv0;
        auto stage = [&](auto V, auto ST_) {
          constexpr int v = V, st = ST_;
          if constexpr (st == 0) { tv[v] = acc[v >> 4][v & 15] * nc; PIN(tv[v]); }
          else if constexpr (st == 1) { ev[v] = __builtin_amdgcn_exp2f(tv[v]); PIN(ev[v]); }
          else if constexpr (st == 2) { ev[v] = 1.f + ev[v]; PIN(ev[v]); }
          else if constexpr (st == 3) { ev[v] = __builtin_amdgcn_rcpf(ev[v]); PIN(ev[v]); }
          else if constexpr (st == 4) { tv[v] = tv[v] * ev[v]; PIN(tv[v]); }
          else if constexpr (v & 1) {
            u32 hh = pk(tv[v - 1], tv[v]);
            PIN(hh);
            ho[v >> 3][(v >> 1) & 3] = hh;
            if constexpr ((v & 7) == 7) st128<SC1>(ho[v >> 3], rs, l16, hb[(v >> 3) & 1] + (v >> 4) * 65536);
          }
        };
        static_for<64>([&](auto GI) {
          constexpr int gi = GI, t = 2 + (gi >> 5), ks = gi & 31;
          acc[t] = mfma32(wq[gi & 3], xf[ks], acc[t]);
          SB();
          if constexpr (gi + 4 < 64) wq[gi & 3] = WH[(gi + 4) * 64];
          if constexpr (layer == 0 && p3_w2gap(gi)) w2ld(IC(W2D + 12 + (gi * W2B) / 8));
          SB();
          static_for<48>([&](auto V) {
            constexpr int d = gi - p3_g0(V);
            if constexpr (d >= 0 && d < 6) stage(V, IC(d));
          });
          if constexpr (gi == HS0) wsignal<p3_younger(layer, 0, HS0)>(rs, lh, sh0);
          if constexpr (gi == HS1) wsignal<p3_younger(layer, 1, HS1)>(rs, lh, sh0 + 128 * CSTR);
          if constexpr (gi == PK0) pv0 = peek(rs, l0, ch0);
          SB();
        });
        STAMP(sb + 18);
        constexpr int W2E = 56 - W2D - 12 - 8 * W2B;
        static_assert(W2E >= 0, "w2 schedule");
        {
          f32x2 t2[8], e2[8];
#pragma unroll
          for (int m = 0; m < 8; ++m) { t2[m] = f32x2{acc[3][2 * m], acc[3][2 * m + 1]} * nc; PIN(t2[m]); }
          SB();
#pragma unroll
          for (int m = 0; m < 8; ++m) { e2[m] = f32x2{__builtin_amdgcn_exp2f(t2[m].x), __builtin_amdgcn_exp2f(t2[m].y)}; PIN(e2[m]); }
          SB();
          if constexpr (HPRE > 0) wfinish(rs, l0, ch0, 8, pv0);
          STAMP(sb + 21);
          hld(0);
#pragma unroll
          for (int m = 0; m < 8; ++m) { e2[m] = e2[m] + 1.f; PIN(e2[m]); }
          SB();
#pragma unroll
          for (int m = 0; m < 8; ++m) { e2[m] = f32x2{__builtin_amdgcn_rcpf(e2[m].x), __builtin_amdgcn_rcpf(e2[m].y)}; PIN(e2[m]); if (m == 1 || m == 5) { SB(); hld(1 + m / 4); } }
          SB();
#pragma unroll
          for (int m = 0; m < 8; ++m) { t2[m] = t2[m] * e2[m]; PIN(t2[m]); }
          SB();
#pragma unroll
          for (int i = 3; i < HPRE; ++i) hld(i);
#pragma unroll
          for (int s = 0; s < 2; ++s)
            st128<SC1>(v4u{pk(t2[4 * s].x, t2[4 * s].y), pk(t2[4 * s + 1].x, t2[4 * s + 1].y), pk(t2[4 * s + 2].x, t2[4 * s + 2].y), pk(t2[4 * s + 3].x, t2[4 * s + 3].y)},
                       rs, l16, hb[s] + 3 * 65536);
        }
        SB();
        wsignal<p3_younger(layer, 2, 64) + HPRE + 2>(rs, lh, sh0 + 256 * CSTR);
        if constexpr (layer == 0) static_for<W2E>([&](auto E) { w2ld(IC(W2D + 12 + 8 * W2B + E)); });
        SB();
        STAMP(sb + 19);
        if constexpr (HPRE == 0) wfinish(rs, l0, ch0, 8, pv0);
      } else {
        if constexpr (layer == 0) static_for<56>([&](auto E) { w2ld(E); });
        DRAIN();
        BAR();
        wwait(rs, l0, ch0, 8);
#pragma unroll
        for (int i = 0; i < HPRE; ++i) hld(i);
      }
    }
    {
      const u32 ln = opqv(lane), l16 = ln * 16, r = ln & 31, hf = ln >> 5;
      const u32 wqb = opq(WQKV_T + hp * 98304 + w * 16384);
      u32 pv1[2], pv2[2], pv3[2];
      splitk_phase<FrH, WINH, HPRE>(rs, w2f, af, hown, hoth, L, w, l16, -LN2, res,
        [&](int i) { if (i == T1A || i == T1B) apeek(rs, l0, ch0 + 128 * CSTR, pv1[i == T1B]); },
        [&]() {
          wsignal<3 + 14 - HPRE>(rs, lh, sh0 + 384 * CSTR);
          STAMP(sb + 22);
          pfinish2<14 - T1A + 2, 14 - T1B + 1>(rs, l0, ch0 + 128 * CSTR, 8, pv1);
          STAMP(sb + 20);
        },
        [&](auto I) {
          constexpr int i = I, g = i - 42;
          if constexpr (i == 15) pretire(pv1);
          constexpr auto ops = [](int a, int b) {
            int n = 0;
            for (int q = 14; q < 56; ++q) n += FrH::lstep(q, WINH) >= a && FrH::lstep(q, WINH) < b;
            for (int q : {PK2, PK2B, PK3, PK3B}) n += q > a && q < b;
            return n;
          };
          static_assert(PK2 < PK2B && PK2B < T2CHK && PK2B != PK3 && PK2 != PK3 && PK3 < PK3B && PK3B < T3CHK && T2CHK == 14 && T3CHK >= 21 && T3CHK <= 28, "peek order");
          if constexpr (i == T2CHK) pfinish2<ops(PK2, i), ops(PK2B, i)>(rs, l0, ch0 + 256 * CSTR, 8, pv2);
          if constexpr (i == T3CHK) { STAMP(sb + 27); pfinish2<ops(PK3, i), ops(PK3B, i)>(rs, l0, ch0 + 384 * CSTR, 8, pv3); STAMP(sb + 28); }
          if constexpr (i == PK2 || i == PK2B) apeek(rs, l0, ch0 + 256 * CSTR, pv2[i == PK2B]);
          if constexpr (i == PK3 || i == PK3B) apeek(rs, l0, ch0 + 384 * CSTR, pv3[i == PK3B]);
          if constexpr (i == 29) pretire(pv2);
          if constexpr (i == 36) pretire(pv3);
          if constexpr (KWPOS == 0) {
            if constexpr (layer == 0 && g >= 10 && g <= 14) dma1<g - 10>(rs, l16, wqb, L + w * 16384);
            if constexpr (layer == 0 && g >= 19 && g <= 21) dma1<g - 14>(rs, l16, wqb, L + w * 16384);
          } else {
            if constexpr (layer == 0 && g >= 10 && g < 26) dma1<((g - 10) & 3)>(rs, l16, wqb + ((g - 10) >> 2) * 4096, L + ((w * 16384 + ((g - 10) >> 2) * 4096) ^ rx));
          }
        });
      STAMP(sb + 23);
      if constexpr (layer == 0) {
        resid_store(rs, res, l16, hf ? 0x80000000u : (r & 15) * 16 + (r >> 4) * 8, xtile(1, tte) + blk(n0 * 64), stile(1, tte) + (n0 >> 5) * 256);
        STAMP(sb + 24);
        if constexpr (KWPOS == 0) dma_range<8, 16>(rs, l16, wqb, L + w * 16384);
        wsignal<KWPOS == 0 ? 8 : 0>(rs, l0, (cnt_off(C_X + (tte & 31)) + sd));
        STAMP(sb + 25);
      } else {
        const rsrc_t rso = mkrsrc(out, S * D * 2);
#pragma unroll
        for (int s = 0; s < 2; ++s) st128<OAUX>(res[s], rso, r * 1024 + hf * 16 + s * 32, tte * 32768 + n0 * 2);
        if (b == 255 && tid < 16) st64<SC1>(v2u{tag_lo, tag_hi}, rs, tid * 8, EPOCH_OFF);
        STAMP(sb + 24);
      }
    }
  });
  asm volatile(".Lkcode1:");
}

extern "C" void kernel_launch(void* const* d_in, const int* in_sizes, int n_in, void* d_out, int out_size, void* d_ws,
                              size_t ws_size, hipStream_t stream) {
  (void)hipFuncSetAttribute((const void*)k_main, hipFuncAttributeMaxDynamicSharedMemorySize, LDS_BYTES);
  k_main<<<256, 256, LDS_BYTES, stream>>>((const uint16_t*)d_in[0], (const uint16_t*)d_in[1], (const uint16_t*)d_in[2],
                                          (const uint16_t*)d_in[3], (const uint16_t*)d_in[5], (const uint16_t*)d_in[6],
                                          (unsigned char*)d_ws, (unsigned char*)d_out);
}
```

```cpp
#include <hip/hip_runtime.h>
#include <hip/hip_bf16.h>
#include <stdint.h>
#include <utility>

typedef unsigned int u32;
typedef u32 v4u __attribute__((ext_vector_type(4)));
typedef u32 v2u __attribute__((ext_vector_type(2)));
typedef short s16x4 __attribute__((ext_vector_type(4)));
typedef __bf16 bf16x8 __attribute__((ext_vector_type(8)));
typedef __bf16 bf16x2 __attribute__((ext_vector_type(2)));
typedef float f32x2 __attribute__((ext_vector_type(2)));
typedef float f32x4 __attribute__((ext_vector_type(4)));
typedef float f32x16 __attribute__((ext_vector_type(16)));
#define LAS __attribute__((address_space(3)))
#define GAS __attribute__((address_space(1)))
#define DI __device__ __forceinline__
typedef __amdgpu_buffer_rsrc_t rsrc_t;

#define DXV 12
#define WINH 56
#define HS0 36
#define HS1 56
#define PK0 59
#define HPRE 0
#define PK2 0
#define T1A 0
#define T1B 7
#define PS5 5
#define PK3 7
#define PK2B 6
#define PK3B 13
#define T2CHK 14
#define T3CHK 21
#define KVCHK 0
#define KVLD 2
#define ODR 8
#define OAUX 0
#define PS1 3
#define PS2 4
#define XMODE 3
#define KMODE 2
#define NQ1 11
#define NQ2 10
#define OPL0 4
#define OPL1 4
#define XDR 4
#define OMODE 2
#define PS6 3
#define PS4 2
#define KWPOS 1
#define PS3 2
#define WOK 2
#define CPF 1
#define CPW 1
#define TLBD 0
#define LK0 4
#define WSB 0
#define VPK 18
#define PRB 1
#define NFLIP 0
#define ROT2 0
#define QS 11
#define ROT 1
#define XBAR 1
#define JPK 0
#define XA 8
#define XB 20
#define UB 10
#define FPK 4
#define FCK 6
#define KVPK2 14
#define KVPK1 4
#define TLBW 262144
#define W2D 12
#define W2B 4
constexpr int S = 2048, D = 512;
constexpr u32 CTRL = 0;
constexpr u32 WQKV_T = 262144;
constexpr u32 WPHI_T = WQKV_T + 16 * 98304;
constexpr u32 WO_T = WPHI_T + 32768;
constexpr u32 W1_T = WO_T + 524288;
constexpr u32 W2_T = W1_T + 14 * 131072;
constexpr u32 CS_QKV = W2_T + 8 * 4 * 28 * 2048;
constexpr u32 CS_W1 = CS_QKV + 65536;
constexpr u32 MB2 = 2097152;
constexpr u32 XT = 3 * MB2;
constexpr u32 OB = 6 * MB2;
constexpr u32 HB = 8 * MB2;
constexpr u32 ST = 16 * MB2;
constexpr u32 ST_SZ = 262144;
constexpr u32 ZP = ST + 4 * ST_SZ;
constexpr u32 KVP = 17 * MB2;
constexpr u32 WS_END = 18 * MB2;
static_assert(CS_W1 + 65536 <= XT, "workspace map");
constexpr u32 blk(u32 off) { return (off >> 13) * 16384 + (off & 8191); }
constexpr u32 CSTR = 128;
enum { C_H = 0, C_O = 1024, C_X1 = 1280, C_X = 1408, C_J = 1440, C_V = 1504, C_TMO = 1520, C_KV = 1536, NCNT = 1792 };
constexpr u32 INIT_OFF = CTRL + NCNT * CSTR, EPOCH_OFF = INIT_OFF + 16 * CSTR, FLAG_OFF = EPOCH_OFF + CSTR;
static_assert(FLAG_OFF + 192 * CSTR <= WQKV_T, "control area");

#define STAMP(i)
constexpr float LOG2E = 1.4426950408889634f;
constexpr float LN2 = 0.6931471805599453f;

DI u32 pk(float a, float b) {
  f32x2 v = {a, b};
  bf16x2 r = __builtin_convertvector(v, bf16x2);
  return __builtin_bit_cast(u32, r);
}
DI float bflo(u32 u) { return __builtin_bit_cast(float, u << 16); }
DI float bfhi(u32 u) { return __builtin_bit_cast(float, u & 0xffff0000u); }
DI f32x16 mfma32(v4u a, v4u b, f32x16 c) {
  f32x16 d = __builtin_amdgcn_mfma_f32_32x32x16_bf16(__builtin_bit_cast(bf16x8, a), __builtin_bit_cast(bf16x8, b), c, 0, 0, 0);
  asm volatile("" : "+a"(d));
  return d;
}
DI f32x4 mfma16(v4u a, v4u b, f32x4 c) {
  return __builtin_amdgcn_mfma_f32_16x16x32_bf16(__builtin_bit_cast(bf16x8, a), __builtin_bit_cast(bf16x8, b), c, 0, 0, 0);
}
DI float dot2(u32 a, u32 b, float c) {
  return __builtin_amdgcn_fdot2_f32_bf16(__builtin_bit_cast(bf16x2, a), __builtin_bit_cast(bf16x2, b), c, false);
}
template <int AUX> DI v4u ld128(rsrc_t rs, u32 voff, u32 soff) { return __builtin_amdgcn_raw_buffer_load_b128(rs, voff, soff, AUX); }
template <int AUX> DI v2u ld64(rsrc_t rs, u32 voff, u32 soff) { return __builtin_amdgcn_raw_buffer_load_b64(rs, voff, soff, AUX); }
template <int AUX> DI u32 ld32(rsrc_t rs, u32 voff, u32 soff) { return __builtin_amdgcn_raw_buffer_load_b32(rs, voff, soff, AUX); }
template <int AUX> DI void st128(v4u v, rsrc_t rs, u32 voff, u32 soff) {
  __builtin_amdgcn_raw_buffer_store_b128(v, rs, voff, soff, AUX);
  asm volatile("s_nop 1" ::"v"(v) : "memory");
}
template <int AUX> DI void st64(v2u v, rsrc_t rs, u32 voff, u32 soff) { __builtin_amdgcn_raw_buffer_store_b64(v, rs, voff, soff, AUX); }
DI rsrc_t mkrsrc(const void* p, u32 bytes) { return __builtin_amdgcn_make_buffer_rsrc((void*)p, 0, (int)bytes, 0x00020000); }
DI float sum32(float v) {
  u32 u = __builtin_bit_cast(u32, v);
  auto r = __builtin_amdgcn_permlane32_swap(u, u, false, false);
  const u32 r0 = r[0], r1 = r[1];
  return __builtin_bit_cast(float, r0) + __builtin_bit_cast(float, r1);
}
DI float sum16(float v) {
  u32 u = __builtin_bit_cast(u32, v);
  auto r = __builtin_amdgcn_permlane16_swap(u, u, false, false);
  const u32 r0 = r[0], r1 = r[1];
  return __builtin_bit_cast(float, r0) + __builtin_bit_cast(float, r1);
}
DI float mix16(float a, float b) {
  auto r = __builtin_amdgcn_permlane16_swap(__builtin_bit_cast(u32, a), __builtin_bit_cast(u32, b), false, false);
  const u32 r0 = r[0], r1 = r[1];
  return __builtin_bit_cast(float, r0) + __builtin_bit_cast(float, r1);
}
DI v2u trread(LAS unsigned char* __restrict__ p) {
  return __builtin_bit_cast(v2u, __builtin_amdgcn_ds_read_tr16_b64_v4i16((LAS s16x4*)p));
}
DI int rho32(int reg, int hf) { return (reg & 3) + 8 * (reg >> 2) + 4 * hf; }
constexpr u32 ONES2 = 0x3f803f80u;

DI v4u pack8(const f32x16& a, int s) {
  v4u r;
#pragma unroll
  for (int m = 0; m < 4; ++m) r[m] = pk(a[8 * s + 2 * m], a[8 * s + 2 * m + 1]);
  return r;
}

#define SB() __builtin_amdgcn_sched_barrier(0)
#define PIN(x) asm volatile("" : "+v"(x))
#define BAR() asm volatile("s_waitcnt lgkmcnt(0)\n\ts_barrier" ::: "memory")
#define DRAIN() asm volatile("s_waitcnt vmcnt(0)" ::: "memory")
#define RLX __ATOMIC_RELAXED, __HIP_MEMORY_SCOPE_AGENT
constexpr int SC1 = 16;

DI u32 opq(u32 x) { asm volatile("" : "+s"(x)); return x; }
DI u32 opqv(u32 x) { asm volatile("" : "+v"(x)); return x; }
DI v4u ldr(rsrc_t rs, u32 vo, u32 sbase, int ks) { return ld128<16>(rs, vo + (ks & 3) * 1024, sbase + blk((ks >> 2) * 4096)); }
template <int AUX> DI v4u ldf(rsrc_t rs, u32 vo, u32 sbase, u32 off) { return ld128<AUX>(rs, vo + (off & 4095u), sbase + (off & ~4095u)); }
template <int I> DI void dma1(rsrc_t rs, u32 vo, u32 sbase, LAS unsigned char* d) {
  __builtin_amdgcn_raw_ptr_buffer_load_lds(rs, (LAS u32*)(d + (I >> 2) * 4096), 16, vo, sbase + (I >> 2) * 4096, (I & 3) * 1024, SC1);
}
template <int I0, int I1> DI void dma_range(rsrc_t rs, u32 vo, u32 sbase, LAS unsigned char* d) {
  if constexpr (I0 < I1) {
    dma1<I0>(rs, vo, sbase, d);
    dma_range<I0 + 1, I1>(rs, vo, sbase, d);
  }
}
template <class F, int... I> DI void static_for_impl(F&& f, std::integer_sequence<int, I...>) { (f(std::integral_constant<int, I>{}), ...); }
template <int N, class F> DI void static_for(F&& f) { static_for_impl(f, std::make_integer_sequence<int, N>{}); }
DI u32 cnt_off(int idx) { return CTRL + idx * CSTR; }
DI u32 lane0_off() { return (threadIdx.x & 63) == 0 ? 0u : 0x80000000u; }
DI void add1(rsrc_t rs, u32 l0, u32 coff) { (void)__builtin_amdgcn_raw_ptr_buffer_atomic_add_i32(1, rs, l0, coff, 0); }
template <int N> DI void wsignal(rsrc_t rs, u32 l0, u32 coff) {
  SB();
  asm volatile("s_waitcnt vmcnt(%0)" ::"n"(N) : "memory");
  add1(rs, l0, coff);
  SB();
}
DI u32 peek(rsrc_t rs, u32 l0, u32 coff) { return __builtin_amdgcn_raw_ptr_buffer_atomic_add_i32(0, rs, l0, coff, 0); }
DI void wfinish(rsrc_t rs, u32 l0, u32 coff, u32 n, u32 pv) {
  if (__builtin_amdgcn_readfirstlane(pv) < n) {
#pragma unroll 1
    for (u32 spins = 0;; ++spins) {
      if (__builtin_amdgcn_readfirstlane(peek(rs, l0, coff)) >= n) break;
      if (spins > (1u << 16)) { __builtin_amdgcn_raw_buffer_store_b32(1u, rs, 0, cnt_off(C_TMO), SC1); break; }
    }
  }
  asm volatile("" ::: "memory");
}
DI void wwait(rsrc_t rs, u32 l0, u32 coff, u32 n) { wfinish(rs, l0, coff, n, peek(rs, l0, coff)); }
DI void apeek(rsrc_t rs, u32 l0, u32 coff, u32& p) {
  SB();
  asm volatile("v_mov_b32 %0, 0\n\tbuffer_atomic_add %0, %1, %2, %3 offen sc0 ; APEEK" : "=&v"(p) : "v"(l0), "s"(rs), "s"(coff) : "memory");
  SB();
}
#define PCHK(P, W) "s_waitcnt vmcnt(" W ")\n\tv_readfirstlane_b32 %3, " P "\n\ts_cmp_ge_u32 %3, %8\n\ts_cbranch_scc1 .Lpd%=\n\tv_mov_b32 " P ", 0\n\tbuffer_atomic_add " P ", %5, %6, %7 offen sc0\n\t"
template <int N0, int N1, int N2> DI void pfinish(rsrc_t rs, u32 l0, u32 coff, u32 n, u32 (&p)[3]) {
  u32 t, cnt;
  SB();
  asm volatile(PCHK("%0", "%9") PCHK("%1", "%10") PCHK("%2", "%11") "s_movk_i32 %4, 0x7fff\n.Lpl%=:\n\t" PCHK("%0", "2") PCHK("%1", "2") PCHK("%2", "2")
               "s_sub_u32 %4, %4, 1\n\ts_cbranch_scc0 .Lpl%=\n.Lpd%=:"
               : "+v"(p[0]), "+v"(p[1]), "+v"(p[2]), "=&s"(t), "=&s"(cnt)
               : "v"(l0), "s"(rs), "s"(coff), "n"(n), "n"(N0), "n"(N1 + 1), "n"(N2 + 2)
               : "memory", "scc");
  SB();
}
#define PCHK2(P, W) "s_waitcnt vmcnt(" W ")\n\tv_readfirstlane_b32 %2, " P "\n\ts_cmp_ge_u32 %2, %7\n\ts_cbranch_scc1 .Lqd%=\n\tv_mov_b32 " P ", 0\n\tbuffer_atomic_add " P ", %4, %5, %6 offen sc0\n\t"
template <int N0, int N1> DI void pfinish2(rsrc_t rs, u32 l0, u32 coff, u32 n, u32 (&p)[2]) {
  u32 t, cnt;
  SB();
  asm volatile(PCHK2("%0", "%8") PCHK2("%1", "%9") "s_movk_i32 %3, 0x7fff\n.Lql%=:\n\t" PCHK2("%0", "1") PCHK2("%1", "1")
               "s_sub_u32 %3, %3, 1\n\ts_cbranch_scc0 .Lql%=\n.Lqd%=:"
               : "+v"(p[0]), "+v"(p[1]), "=&s"(t), "=&s"(cnt)
               : "v"(l0), "s"(rs), "s"(coff), "n"(n), "n"(N0), "n"(N1 + 1)
               : "memory", "scc");
  SB();
}
DI void pretire(u32 (&p)[2]) { asm volatile("; PRETIRE %0 %1" ::"v"(p[0]), "v"(p[1])); }
DI void pretire(u32 (&p)[3]) { asm volatile("; PRETIRE %0 %1 %2" ::"v"(p[0]), "v"(p[1]), "v"(p[2])); }
#define NAP(n) do { SB(); __builtin_amdgcn_s_sleep(n); SB(); } while (0)
DI void poll3(rsrc_t rs, u32 vo, u32 need) {
#pragma unroll 1
  for (u32 spins = 0;; ++spins) {
    const u32 v = peek(rs, vo, 0);
    if ((__builtin_amdgcn_ballot_w64(v >= need) & 7ull) == 7ull) break;
    if (spins > (1u << 16)) { __builtin_amdgcn_raw_buffer_store_b32(1u, rs, 0, cnt_off(C_TMO), SC1); break; }
  }
  asm volatile("" ::: "memory");
}
template <int CTRL_> DI float dpp_add(float v) {
  return v + __builtin_bit_cast(float, __builtin_amdgcn_update_dpp(0, __builtin_bit_cast(int, v), CTRL_, 0xf, 0xf, true));
}

constexpr int SCR = 131072;
constexpr int PK_OFF = SCR, PK_STR = 144, VV_OFF = PK_OFF + 128 * PK_STR, VV_STR = 80, KF_OFF = VV_OFF + 128 * VV_STR;
constexpr int XS_OFF = 98304;
constexpr int RED_OFF = SCR;
constexpr int LDS_BYTES = 163840;
static_assert(KF_OFF + 4 * 544 <= LDS_BYTES, "lds");

DI void lds_put1(LAS unsigned char* p, const f32x16& a, int q) {
  *(LAS f32x4*)(p + q * 1024) = f32x4{a[4 * q], a[4 * q + 1], a[4 * q + 2], a[4 * q + 3]};
}

extern __shared__ __attribute__((aligned(16))) unsigned char smem[];

struct Sch { int put2[4], put3[4], bar1, rd[8], fu[8], put1[4], bar2, rd1, f1[4]; };
constexpr Sch sch_of(int KL) {
  return KL >= 14 ? Sch{{0, 1, 2, 3}, {4, 5, 6, 7}, 9, {9, 9, 9, 10, 10, 10, 11, 11}, {12, 13, 14, 19, 21, 22, 23, 24}, {15, 16, 17, 18}, 20, 20, {25, 26, 27, 27}}
                  : Sch{{0, 0, 1, 1}, {2, 2, 3, 3}, 5, {5, 5, 5, 5, 6, 6, 6, 6}, {7, 7, 8, 8, 11, 12, 12, 13}, {9, 9, 10, 10}, 11, 11, {14, 14, 15, 15}};
}
struct FrO {
  static constexpr int NS = 16, KL = 8;
  static constexpr bool own(int i) { return i >= 8; }
  static constexpr int ks(int i) { return i & 7; }
  static constexpr u32 off(int i) { return (i & 7) * 1024; }
  static constexpr int lstep(int q, int win) { return q - win; }
  static constexpr int first(int win) { return win; }
};
struct FrH {
  static constexpr int NS = 56, KL = 14;
  static constexpr int part(int i) { return i < 28 ? i / 14 : 2 + ((i / 7) & 1); }
  static constexpr bool own(int i) { return i < 28 ? ((i / 7) & 1) : i >= 42; }
  static constexpr int ks(int i) { return part(i) * 7 + i % 7; }
  static constexpr u32 off(int i) { return part(i) * 65536 + (i % 7) * 1024; }
  static constexpr int lstep(int q, int win) {
    const int g = T3CHK - 21;
    return q < 35 ? q - 14 : q < 42 ? (q - 14 < T3CHK ? T3CHK : q - 14) : q < 42 + g ? q - 21 : q - 14 - g;
  }
  static constexpr int first(int win) { return 14; }
};
template <class Fr> DI v4u ld_fr(rsrc_t rs, u32 l16, u32 a_own, u32 a_oth, int i) { return ldf<SC1>(rs, l16, Fr::own(i) ? a_own : a_oth, Fr::off(i)); }
template <class Fr, int WIN, int PRE, class Win, class Pre, class Mid>
DI void splitk_phase(rsrc_t rs, const v4u (&wf)[Fr::NS / 2][2], v4u (&af)[WIN], u32 a_own, u32 a_oth, LAS unsigned char* L, int w, u32 l16, float scale,
                     v4u (&res)[2], Win&& win, Pre&& pre_loop, Mid&& mid) {
  const f32x16 zero16 = {0.f, 0.f, 0.f, 0.f, 0.f, 0.f, 0.f, 0.f, 0.f, 0.f, 0.f, 0.f, 0.f, 0.f, 0.f, 0.f};
  constexpr int NS = Fr::NS, KP = Fr::KL, E0 = NS - KP, F0 = Fr::first(WIN);
  constexpr Sch sc = sch_of(KP);
  static_assert(WIN >= KP, "window");
  auto ld = [&](int i) { return ld_fr<Fr>(rs, l16, a_own, a_oth, i); };
#pragma unroll
  for (int i = PRE; i < F0; ++i) { win(i); af[i] = ld(i); SB(); }
  LAS unsigned char* r2 = L + opqv(RED_OFF + l16 + (w ^ 2) * 8192);
  LAS unsigned char* r3 = L + opqv(RED_OFF + l16 + (w ^ 3) * 8192 + 4096);
  LAS unsigned char* r0 = L + opqv(RED_OFF + l16 + w * 8192);
  LAS unsigned char* r1 = L + opqv(RED_OFF + l16 + (w ^ 1) * 8192);
  pre_loop();
  SB();
  f32x16 a0 = zero16, a1 = zero16, a2 = zero16, a3 = zero16;
  float u[16];
  static_for<E0>([&](auto I) {
    constexpr int i = I, ks = Fr::ks(i), e = i - (E0 - 4);
    const v4u bb = af[i % WIN];
    if constexpr (!Fr::own(i)) a2 = mfma32(wf[ks][0], bb, a2);
    else a1 = mfma32(wf[ks][1], bb, a1);
    SB();
    mid(I);
    static_for<NS - F0>([&](auto Q) { if constexpr (Fr::lstep(Q + F0, WIN) == i) { af[(Q + F0) % WIN] = ld(Q + F0); SB(); } });
    SB();
    if constexpr (!Fr::own(i)) a3 = mfma32(wf[ks][1], bb, a3);
    else a0 = mfma32(wf[ks][0], bb, a0);
    SB();
    if constexpr (e >= 0) {
      const u32 ra = res[e >> 1][2 * (e & 1)], rb = res[e >> 1][2 * (e & 1) + 1];
      u[4 * e] = bflo(ra); u[4 * e + 1] = bfhi(ra); u[4 * e + 2] = bflo(rb); u[4 * e + 3] = bfhi(rb);
#pragma unroll
      for (int m = 0; m < 4; ++m) PIN(u[4 * e + m]);
    }
    SB();
  });
  f32x4 p[8];
  auto acc4 = [&](int q, const f32x4& x) {
#pragma unroll
    for (int m = 0; m < 4; ++m) { const float xv = x[m]; u[4 * q + m] = __builtin_fmaf(xv, scale, u[4 * q + m]); PIN(u[4 * q + m]); }
  };
  static_for<2 * KP>([&](auto G) {
    constexpr int g = G, k = g % KP, ks = Fr::ks(E0 + k);
    if constexpr (g < KP) a1 = mfma32(wf[ks][1], af[(E0 + k) % WIN], a1);
    else a0 = mfma32(wf[ks][0], af[(E0 + k) % WIN], a0);
    SB();
    if constexpr (g == sc.bar1 || g == sc.bar2) BAR();
    static_for<8>([&](auto Q) { if constexpr (sc.rd[Q] == g) p[Q] = *(LAS f32x4*)(r0 + Q * 1024); });
    if constexpr (g == sc.rd1) static_for<4>([&](auto Q) { p[Q] = *(LAS f32x4*)(r1 + Q * 1024); });
    SB();
    static_for<4>([&](auto Q) { if constexpr (sc.put2[Q] == g) lds_put1(r2, a2, Q); });
    static_for<4>([&](auto Q) { if constexpr (sc.put3[Q] == g) lds_put1(r3, a3, Q); });
    static_for<4>([&](auto Q) { if constexpr (sc.put1[Q] == g) lds_put1(r0, a1, Q); });
    SB();
    static_for<8>([&](auto Q) { if constexpr (sc.fu[Q] == g) acc4(Q & 3, p[Q]); });
    static_for<4>([&](auto Q) { if constexpr (sc.f1[Q] == g) acc4(Q, p[Q]); });
    SB();
    mid(std::integral_constant<int, E0 + g>{});
    SB();
  });
#pragma unroll
  for (int q = 0; q < 4; ++q) {
    acc4(q, f32x4{a0[4 * q], a0[4 * q + 1], a0[4 * q + 2], a0[4 * q + 3]});
    res[q >> 1][2 * (q & 1)] = pk(u[4 * q], u[4 * q + 1]);
    res[q >> 1][2 * (q & 1) + 1] = pk(u[4 * q + 2], u[4 * q + 3]);
  }
}
DI void resid_store(rsrc_t rs, const v4u (&res)[2], u32 l16, u32 sto, u32 xoff, u32 soff) {
#pragma unroll
  for (int s = 0; s < 2; ++s) st128<SC1>(res[s], rs, l16 + s * 1024, xoff);
  SB();
  float s0 = 0.f, q0 = 0.f, s1 = 0.f, q1 = 0.f;
#pragma unroll
  for (int m = 0; m < 4; ++m) {
    s0 = dot2(res[0][m], ONES2, s0); q0 = dot2(res[0][m], res[0][m], q0);
    s1 = dot2(res[1][m], ONES2, s1); q1 = dot2(res[1][m], res[1][m], q1);
  }
  st64<SC1>(__builtin_bit_cast(v2u, f32x2{sum32(s0 + s1), sum32(q0 + q1)}), rs, sto, soff);
}

DI u32 cs_word(float part, u32 hf) {
  const float cs = sum32(part);
  const u32 H = pk(cs, cs);
  const float rem = cs - bflo(H);
  return hf ? pk(rem, rem) : H;
}
DI u32 mean_word(float nmean) { return pk(nmean, nmean - bflo(pk(nmean, nmean))); }
template <int I> DI f32x16 mean_fix(const v4u& cs4, u32 mw, f32x16 c) {
  v4u bm = {0u, 0u, 0u, 0u};
  bm[I] = mw;
  return mfma32(cs4, bm, c);
}
struct Stats { float t[4], s, q, nmean, rstd; u32 mw; };
template <int I> DI void stats_step(Stats& z, const f32x4 (&sp)[4]) {
  if constexpr (I < 3) {
#pragma unroll
    for (int m = 0; m < 4; ++m) { const float a = I ? z.t[m] : sp[0][m], c = sp[I + 1][m]; z.t[m] = a + c; PIN(z.t[m]); }
  } else if constexpr (I == 3) { z.s = mix16(z.t[0], z.t[2]); PIN(z.s); z.q = mix16(z.t[1], z.t[3]); PIN(z.q); }
  else if constexpr (I == 4) { z.nmean = sum32(z.s) * (-1.f / 512.f); PIN(z.nmean); }
  else if constexpr (I == 5) { z.q = sum32(z.q) * (1.f / 512.f); PIN(z.q); }
  else if constexpr (I == 6) { z.mw = mean_word(z.nmean); PIN(z.mw); }
  else if constexpr (I == 7) { z.rstd = __builtin_amdgcn_rsqf(fmaxf(z.q - z.nmean * z.nmean, 0.f) + 1e-5f); PIN(z.rstd); }
}

template <class Hook> DI void dual_softmax(const f32x16& p, float c, u32 (&o)[16], Hook&& hook) {
  float mx = p[0], mn = p[0];
#pragma unroll
  for (int i = 1; i < 16; ++i) { mx = fmaxf(mx, p[i]); mn = fminf(mn, p[i]); }
  float ap = -mx * c, an = mn * c;
  PIN(ap);
  PIN(an);
  SB();
  hook(std::integral_constant<int, 0>{});
  SB();
  f32x2 ep[8], en[8];
#pragma unroll
  for (int i = 0; i < 8; ++i) {
    const f32x2 pv = {p[2 * i], p[2 * i + 1]};
    ep[i] = pv * c + ap;
    en[i] = pv * (-c) + an;
    PIN(ep[i]);
    PIN(en[i]);
  }
  SB();
  hook(std::integral_constant<int, 1>{});
  SB();
  f32x2 sa, sc;
  static_for<8>([&](auto I) {
    constexpr int i = I;
    ep[i] = f32x2{__builtin_amdgcn_exp2f(ep[i].x), __builtin_amdgcn_exp2f(ep[i].y)};
    en[i] = f32x2{__builtin_amdgcn_exp2f(en[i].x), __builtin_amdgcn_exp2f(en[i].y)};
    PIN(ep[i]);
    PIN(en[i]);
    if constexpr (i == 2) { sa = ep[0] + ep[1]; sc = en[0] + en[1]; PIN(sa); PIN(sc); }
    if constexpr (i > 2) { sa += ep[i - 1]; sc += en[i - 1]; PIN(sa); PIN(sc); }
    SB();
    hook(std::integral_constant<int, 2 + i>{});
    SB();
  });
  sa += ep[7];
  sc += en[7];
  const float rp = __builtin_amdgcn_rcpf(sa.x + sa.y), rn = __builtin_amdgcn_rcpf(sc.x + sc.y);
#pragma unroll
  for (int i = 0; i < 8; ++i) { ep[i] *= rp; PIN(ep[i]); }
#pragma unroll
  for (int i = 0; i < 8; ++i) { en[i] *= rn; PIN(en[i]); }
  SB();
  hook(std::integral_constant<int, 10>{});
  SB();
#pragma unroll
  for (int i = 0; i < 8; ++i) { o[i] = pk(ep[i].x, ep[i].y); o[8 + i] = pk(en[i].x, en[i].y); }
}
constexpr int p3_g0(int v) { return (v * 58) / 48; }
constexpr bool p3_w2gap(int gi) { return (gi * W2B) / 8 != ((gi + 1) * W2B) / 8; }
constexpr int p3_pre(int layer, int g) {
  int n = layer == 0 && p3_w2gap(g);
  for (int q = 0; q < 6; ++q) n += p3_g0(8 * q + 7) + 5 == g;
  return n;
}
constexpr int p3_post(int g) { return (g == HS0) + (g == HS1) + (g == PK0); }
constexpr int p3_younger(int layer, int t, int gw) {
  const int gs = p3_g0(16 * t + 15) + 5;
  int n = p3_post(gs);
  for (int g = gs + 1; g < gw; ++g) n += p3_pre(layer, g) + p3_post(g);
  return n + (gw < 64 ? p3_pre(layer, gw) : 0);
}
static_assert(p3_g0(32) >= 35 && p3_g0(47) + 5 < 64 && p3_g0(15) + 5 < HS0 && p3_g0(31) + 5 < HS1 && HS0 != HS1, "P3 schedule");

__global__ void __launch_bounds__(256, 1)
k_main(const uint16_t* __restrict__ x, const uint16_t* __restrict__ wqkv, const uint16_t* __restrict__ wphi,
       const uint16_t* __restrict__ wo_, const uint16_t* __restrict__ w1, const uint16_t* __restrict__ w2,
       unsigned char* ws, unsigned char* out) {
  u32 cpf;
  {
    unsigned long long pc;
    u32 o = threadIdx.x * 128 + (blockIdx.x & 8) * 4096;
    asm volatile("s_getpc_b64 %1\n.Lkcode0:\n\tv_min_u32 %2, .Lkcode1-.Lkcode0, %2\n\tglobal_load_dword %0, %2, %1 ; APEEK" : "=v"(cpf), "=&s"(pc), "+v"(o)::"memory");
  }
  SB();
  unsigned long long px = (unsigned long long)x, pq = (unsigned long long)wqkv, po = (unsigned long long)wo_, p1 = (unsigned long long)w1, p2 = (unsigned long long)w2;
  asm volatile("" : "+s"(px));
  asm volatile("" : "+s"(ws));
  asm volatile("" : "+s"(out));
  asm volatile("" : "+s"(wphi));
  LAS unsigned char* L = (LAS unsigned char*)smem;
  const int b = blockIdx.x, tid = threadIdx.x, lane = tid & 63, w = __builtin_amdgcn_readfirstlane(tid >> 6);
  const int xcd = b & 7, j = b >> 3;
  const int rt = 4 * (xcd >> 1) + (j >> 3), hp = 8 * (xcd & 1) + (j & 7), tt = 4 * rt + w;
  const bool p3on = (j & 7) < 7;
  const int fb = p3on ? 7 * (xcd & 1) + (j & 7) : 0;
  const int rt2 = 4 * xcd + (j >> 3), cb = j & 7;
  const int tte = 2 * rt2 + (w >> 1), n0 = 64 * cb + 32 * (w & 1);
  const rsrc_t rs = mkrsrc((unsigned char*)(((size_t)ws + (size_t)WSB * MB2 + MB2 - 1) & ~(size_t)(MB2 - 1)), WS_END);
  const f32x16 zero16 = {0.f, 0.f, 0.f, 0.f, 0.f, 0.f, 0.f, 0.f, 0.f, 0.f, 0.f, 0.f, 0.f, 0.f, 0.f, 0.f};
  STAMP(63);
  v4u res[2];
  v4u csq = {0u, 0u, 0u, 0u}, cs1;
  v4u wph[2];
  u32 tag_lo, tag_hi, tws = 0;
  u32 sd;
  const u32 l0 = lane0_off();
  const u32 nlh = !p3on ? 0u : (fb == 3 || fb == 10) ? 2u : 1u;
  v4u xr[32];
  const rsrc_t rsx = mkrsrc((const void*)px, S * D * 2);
  auto trfrag = [&](LAS unsigned char* p, int str) { const v2u t0 = trread(p), t1 = trread(p + str); return v4u{t0.x, t0.y, t1.x, t1.y}; };
  const u32 sv = ((lane >> 2) & 1) == ((lane >> 4) & 1) ? ONES2 : 0u;
  v4u selm = {sv, sv, sv, sv};
  const u32 u2 = opq(2 * b + (w & 1));
  const bool k0 = w == 0, k1 = b < 112, k3 = b >= 144, kon = k0 ? b < 192 : w >= 2;
  auto pick = [&](auto a0, auto a1, auto a2, auto a3) {
    asm volatile("" : "+s"(a0));
    asm volatile("" : "+s"(a1));
    asm volatile("" : "+s"(a2));
    asm volatile("" : "+s"(a3));
    auto r = k3 ? a3 : a2;
    r = k1 ? a1 : r;
    r = k0 ? a0 : r;
    asm volatile("" : "+s"(r));
    return r;
  };
  const u32 n3 = opq(opq(u2 - 288) * 2341) >> 16;
  const u32 ub = opq(k0 ? b : k1 ? u2 : u2 - 224), q3 = opq(u2 - 288 - 28 * n3);
  const u32 nb = opq(!k0 && k3 ? n3 : ub >> 3), kq = opq(!k0 && k3 ? q3 : ub & 7), ld2 = opq(k0 ? 3072u : k1 ? 3584u : 1024u);
  LAS unsigned char* img = L + SCR + w * 8192;
  {
    const rsrc_t rsu = mkrsrc((const void*)pick(pq, p1, po, p2), opq(kon ? (k0 || k1 || !k3 ? 512u : 1792u) : 0u) * ld2);
    const u32 vu = (lane >> 3) * ld2 + (((lane & 7) ^ ((lane >> 2) & 4)) * 16);
#pragma unroll
    for (int i = 0; i < 8; ++i) __builtin_amdgcn_raw_ptr_buffer_load_lds(rsu, (LAS u32*)(img + i * 1024), 16, vu, (64 * kq + 8 * i) * ld2 + nb * 128, 0, 0);
  }
  SB();
  if (b >= 248 && w < 2) {
#pragma unroll 1
    for (u32 i = 0; i < NCNT * CSTR / 16; i += 1024) st128<SC1>(v4u{0u, 0u, 0u, 0u}, rs, lane * 16, CTRL + ((b - 248) * 2 + w) * (NCNT * CSTR / 16) + i);
  }
  const v2u ep = ld64<SC1>(rs, 0, EPOCH_OFF);
  SB();
  const u32 vx = (lane >> 3) * 1024 + (lane & 7) * 16;
  const u32 rot = ROT ? j & 7 : 0;
  auto xld = [&](auto I) { constexpr int i = I; xr[i] = ld128<0>(rsx, vx, tt * 32768 + (i & 3) * 8192 + (((i >> 2) ^ rot) * 128)); SB(); };
  static_for<XA>([&](auto I) { xld(I); });
  u32 obase, cstr, kstr, kstr2, csb, ovo, cvo, ua;
  {
    const u32 t32 = 2 * nb, tile = (0x12u >> ((nb >> 3) * 2)) & 3;
    obase = opq(pick(WQKV_T + (t32 & 15) * 98304 + tile * 32768 + kq * 4096, W1_T + t32 * 32768 + kq * 4096, WO_T + (nb * 32 + kq * 4) * 2048,
                     W2_T + (nb * 112 + (kq & 1) * 56 + (kq >> 1) * 2) * 2048));
    cstr = opq(pick(98304u, 32768u, 1024u, 1024u));
    kstr = opq(pick(1024u, 1024u, 2048u, 2048u));
    kstr2 = opq(pick(2048u, 2048u, 4096u, 57344u));
    csb = opq(pick(CS_QKV + (((t32 & 15) * 3 + tile) * 8 + kq) * 128, CS_W1 + (t32 * 8 + kq) * 128, 0u, 0u));
    ovo = opqv(lane * 16 | (kon ? 0u : 0x80000000u));
    cvo = opqv(((lane >> 5) * (k0 ? 3072 : 1024) + (lane & 31) * 4) | (kon && (k0 || k1) ? 0u : 0x80000000u));
    const u32 c16 = lane & 15, Q = ((lane >> 2) & 4) + (lane & 3);
    const u32 Cc = ((k0 ? 0x73625140u : 0x75643120u) >> (4 * Q)) & 7;
    ua = opqv((8 * (lane >> 5) + (c16 >> 2)) * 128 + (c16 >> 3) * 64 + Cc * 8);
  }
  const u32 jadd = opq(cnt_off(C_J + pick(0u, 16 + (nb >> 1), nb, nb)));
  auto unit = [&]() {
    v4u o[8];
#pragma unroll
    for (int i = 0; i < 8; ++i) o[i] = trfrag(img + (ua ^ ((i >> 2) * 64)) + (i & 3) * 2048, 512);
    SB();
    f32x4 cs[2] = {{0.f, 0.f, 0.f, 0.f}, {0.f, 0.f, 0.f, 0.f}};
#pragma unroll
    for (int i = 0; i < 8; ++i) {
      st128<SC1>(o[i], rs, ovo, obase + (i >> 2) * cstr + ((i >> 1) & 1) * kstr2 + (i & 1) * kstr);
      cs[i >> 2] = mfma16(selm, o[i], cs[i >> 2]);
    }
    const float c0 = cs[0][0], c1 = cs[1][0];
    __builtin_amdgcn_raw_buffer_store_b32(__builtin_bit_cast(u32, lane < 32 ? c0 : c1), rs, cvo, csb, SC1);
    SB();
  };
  SB();
  const u32 tvo = opqv((((b >= 248 && w < 2) || (k0 && kon)) && lane < 16) ? lane * 8 : 0x80000000u);
  const u32 tso = opq(b >= 248 ? INIT_OFF + (((b & 7) * 2 + w) & 15) * CSTR : FLAG_OFF + b * CSTR);
  SB();
  v4u w2f[28][2];

  static_for<2>([&](auto LAYER) __attribute__((always_inline)) {
    constexpr int layer = LAYER;
    constexpr int sb = layer * 29;
    v4u wo[8][2];
    const u32 wb = opq(W2_T + cb * 229376 + w * 14336);
    const u32 wsw = (w & 1) * 1024;
    u32 pvx[3], pvy[2];
    const u32 rx = ROT2 ? opq(rot * 4096) : 0u;
#define IC(n) std::integral_constant<int, n>{}
    {
      const u32 ln = opqv(lane), l16 = ln * 16, r = ln & 31, hf = ln >> 5;
      if constexpr (layer) STAMP(sb + 0);
      constexpr int DX = DXV;
      v4u xf[32];
      auto xtile = [&](int i, u32 t) { return XT + i * MB2 + (t & 31) * 65536 + (sd ^ ((i & 1) ? 0 : 8192)); };
      auto stile = [&](int i, u32 t) { return ST + i * ST_SZ + blk((t & 31) * 4096) + sd; };
      const u32 xb = layer ? xtile(1, tt) : 0;
      if constexpr (layer > 0 && XMODE == 1) apeek(rs, l0, (cnt_off(C_X + (tt & 31)) + sd), pvx[0]);
      SB();
      f32x4 sp[4];
      const u32 stb = layer ? stile(1, tt) : 0;
      const u32 wqb = opq(WQKV_T + hp * 98304 + 65536 + w * 8192);
      const u32 g4 = ln >> 4, c16 = ln & 15;
      LAS unsigned char* a_v = L + opqv(VV_OFF + (32 * w + r) * VV_STR + hf * 32);
      LAS unsigned char* a_pk = L + opqv(PK_OFF + (32 * w + r) * PK_STR + hf * 64);
      LAS unsigned char* a_pa = L + opqv(PK_OFF + (8 * g4 + (c16 >> 2)) * PK_STR + w * 32 + (ln & 3) * 8);
      LAS unsigned char* a_pb = L + opqv(VV_OFF + (8 * g4 + (c16 >> 2)) * VV_STR + (w >> 1) * 32 + (ln & 3) * 8);
      const u32 o_kv = opqv((g4 >> 1) * 256 + c16 * 16 + (g4 & 1) * 8), o_z = opqv(hf * 8192 + r * 4);
      u32 jv = 0, lat = 0, vv = 0, vb = 0;
      constexpr int wok = layer ? WOK : 0, NPF = 16 - wok + (layer ? 24 : 32);
      constexpr int s_q1 = 0, s_q2 = s_q1 + NQ1, s_od = s_q2 + NQ2, s_op = s_od + ODR, n_op = layer ? OPL1 : OPL0, s_p2 = s_op + n_op, s_xd = NPF - XDR;
      static_assert(s_p2 <= s_xd && s_xd - s_p2 <= 12 && s_od <= NPF - 8 && s_op >= 16 - wok, "prefetch schedule");
      LAS v4u* WL = (LAS v4u*)(L + l16);
      LAS v4u* WQ = (LAS v4u*)(L + opqv(l16 + 65536));
      const u32 wob = opq(WO_T + (cb * 4 + w) * 16384);
      auto wold = [&](auto I) {
        constexpr int i = I;
        wo[i >> 1][i & 1] = ld128<SC1>(rs, l16 + ((i >> 1) & 1) * 2048, wob + (i >> 2) * 4096 + ((i & 1) ? 1024 - wsw : wsw));
      };
      f32x16 aK = zero16, aV = zero16, aQ;
      float rstd, c;
      Stats zs_;
      const u32 jvo = opqv(ln == 0 ? cnt_off(C_J + cb) : ln == 1 ? cnt_off(C_J + 16 + fb) : 0x80000000u);
      float cp[3][4];
      v4u wk[3], wv[3];
      SB();
      if constexpr (layer == 0) {
        LAS unsigned char* sw[2];
        LAS unsigned char* sr[4];
#pragma unroll
        for (int i = 0; i < 2; ++i) sw[i] = L + opqv(XS_OFF + w * 8192 + (ln >> 3) * 128 + (((ln & 7) ^ (4 * i + (ln >> 4))) * 16));
#pragma unroll
        for (int f = 0; f < 4; ++f) sr[f] = L + opqv(XS_OFF + w * 8192 + r * 128 + (((2 * f + hf) ^ ((r >> 1) & 7)) * 16));
        const u32 fu = ln >> 1, fvo = opqv(ln >= 12 ? 0x80000000u : FLAG_OFF + ((fu < 4 ? (1 + (w >> 1)) * 64 + (w & 1) * 4 + fu : 2 * w + fu - 4) + (hp >> 1) * 8) * CSTR + (ln & 1) * 4);
        PIN(selm);
        SB();
        if (w == 0) {
          asm volatile("s_waitcnt vmcnt(%0)" ::"n"(XA + 1) : "memory");
          STAMP(58);
          unit();
          STAMP(59);
        }
        asm volatile("s_barrier" ::: "memory");
        {
          const unsigned long long e = ((unsigned long long)__builtin_amdgcn_readfirstlane(ep.y) << 32) | __builtin_amdgcn_readfirstlane(ep.x);
          unsigned long long t = e * 0x9E3779B97F4A7C15ull + 0x632BE59BD9B4E019ull;
          if (t == e) t = ~e;
          tag_lo = (u32)t;
          tag_hi = (u32)(t >> 32);
        }
        const u32 fneed = (ln & 1) ? tag_hi : tag_lo;
        f32x4 sa = {0.f, 0.f, 0.f, 0.f};
        f32x16 gr = zero16;
        u32 fv;
        v2u tg;
        SB();
        auto vm = [&](auto N) {
          constexpr int n = N, xi = XA + n, di = n - 8 * FCK;
          if constexpr (xi < 32) xld(IC(xi));
          if constexpr (xi == 32) __builtin_amdgcn_raw_ptr_buffer_load_lds(mkrsrc(wphi, 16384), (LAS u32*)(L + XS_OFF + w * 8192 + 4096), 16, l16, hp * 1024, 0, 0);
          if constexpr (xi == 33) tws = ld32<0>(rs, TLBW ? (ln + 64 * w + (TLBD ? 256 * j : 0)) * TLBW : 0x80000000u, 0);
          if constexpr (xi == XB) {
            if (w == 0) asm volatile("s_waitcnt vmcnt(%0)" ::"n"(XB - XA + 1) : "memory");
            st64<SC1>(v2u{tag_lo, tag_hi}, rs, tvo, tso);
            STAMP(60);
          }
          if constexpr (di >= 0 && di < 16)
            dma1<(di & 3)>(rs, l16, WQKV_T + hp * 98304 + w * 16384 + (di >> 2) * 4096, L + (w >> 1) * 32768 + ((((w & 1) * 4 + (di >> 2)) ^ rot) * 4096));
          if constexpr (di == 16) tg = ld64<SC1>(rs, c16 * CSTR, INIT_OFF);
          SB();
        };
        static_assert(XA + 8 * FCK > 33 && XB < 32 && FCK <= 7 && UB >= FCK + 3 && XA >= 4, "start schedule");
        static_for<11>([&](auto B) {
          constexpr int blk = B;
          if constexpr (blk == UB) { if (w >= 2) unit(); }
          if constexpr (blk == FPK) { fv = peek(rs, fvo, 0); SB(); }
          if constexpr (blk == FCK) {
            STAMP(0);
            if ((__builtin_amdgcn_ballot_w64(fv == fneed) & 0xfffull) != 0xfffull) {
#pragma unroll 1
              for (u32 spins = 0; spins < (1u << 16); ++spins)
                if ((__builtin_amdgcn_ballot_w64(peek(rs, fvo, 0) == fneed) & 0xfffull) == 0xfffull) break;
            }
            asm volatile("" ::: "memory");
            STAMP(61);
          }
          if constexpr (blk < 10) static_for<4>([&](auto I) {
            constexpr int i = I;
            if constexpr (blk > 1) gr = mfma32(xf[4 * blk - 8 + i], xf[4 * blk - 8 + i], gr);
            SB();
            vm(IC(8 * blk + 2 * i));
            if constexpr (blk < 8) *(LAS v4u*)(sw[i & 1] + i * 1024) = xr[4 * blk + i];
            SB();
            if constexpr (blk > 1) { sa = mfma16(selm, xf[4 * blk - 8 + i], sa); asm volatile("" : "+a"(sa)); }
            SB();
            vm(IC(8 * blk + 2 * i + 1));
          });
          if constexpr (blk < 8) static_for<4>([&](auto F) { xf[4 * blk + F] = *(LAS v4u*)(sr[F]); SB(); });
        });
        asm volatile("" ::"v"(tws));
        asm volatile("; PRETIRE %0" ::"v"(cpf));
        {
          const u32 Qc = 4 * (g4 & 1) + (c16 & 3);
          LAS unsigned char* a_ph = L + opqv(XS_OFF + w * 8192 + 4096 + hf * 512 + (c16 >> 2) * 32 + (Qc >> 1) * 8);
#pragma unroll
          for (int s = 0; s < 2; ++s) wph[s] = trfrag(a_ph + s * 256, 128);
        }
        const float sa0 = sa[0];
        const float nmean = sa0 * (-1.f / 512.f);
        zs_.mw = mean_word(nmean);
        auto sel = [](bool cnd, float a0, float a1) { PIN(a0); PIN(a1); return cnd ? a1 : a0; };
        auto g4f = [&](int i) { return sel(r & 2, sel(r & 1, gr[4 * i], gr[4 * i + 1]), sel(r & 1, gr[4 * i + 2], gr[4 * i + 3])); };
        const float gd = sel(r & 16, sel(r & 8, g4f(0), g4f(1)), sel(r & 8, g4f(2), g4f(3)));
        const bool dl = ((r >> 2) & 1) == hf;
        const float dg = dl ? gd : 0.f;
        rstd = __builtin_amdgcn_rsqf(fmaxf(sum32(dg) * (1.f / 512.f) - nmean * nmean, 0.f) + 1e-5f);
        c = rstd * LOG2E;
#pragma unroll
        for (int s = 0; s < 2; ++s)
#pragma unroll
          for (int m = 0; m < 4; ++m) wph[s][m] = dl ? wph[s][m] : 0u;
        PIN(wph[0]);
        PIN(wph[1]);
        PIN(c);
        PIN(rstd);
        PIN(zs_.mw);
        SB();
        if constexpr (UB > FCK) { if (w >= 2) asm volatile("s_waitcnt vmcnt(9)" ::: "memory"); else DRAIN(); }
        else DRAIN();
        if (w == 0 && !__all(tg.x == tag_lo && tg.y == tag_hi)) {
#pragma unroll 1
          for (u32 spins = 0; spins < (1u << 16); ++spins) {
            asm volatile("" ::: "memory");
            const v2u t = ld64<SC1>(rs, c16 * CSTR, INIT_OFF);
            if (__all(t.x == tag_lo && t.y == tag_hi)) break;
          }
        }
        BAR();
#pragma unroll
        for (int i = 0; i < 2; ++i) { wk[i] = WL[i * 64]; wv[i] = WL[(32 + i) * 64]; }
        if constexpr (UB <= FCK) add1(rs, w >= 2 ? l0 : 0x80000000u, jadd);
        SB();
      } else {
        if constexpr (XMODE == 0) { NAP(PS3); pvx[0] = peek(rs, l0, (cnt_off(C_X + (tt & 31)) + sd)); SB(); }
        if constexpr (XMODE == 3) { NAP(PS3); apeek(rs, l0, (cnt_off(C_X + (tt & 31)) + sd), pvy[0]); NAP(PS5); apeek(rs, l0, (cnt_off(C_X + (tt & 31)) + sd), pvy[1]); }
        if constexpr (XMODE == 1) { NAP(PS1); apeek(rs, l0, (cnt_off(C_X + (tt & 31)) + sd), pvx[1]); NAP(PS2); apeek(rs, l0, (cnt_off(C_X + (tt & 31)) + sd), pvx[2]); SB(); }
        asm volatile("s_waitcnt vmcnt(%0)" ::"n"(XMODE == 1 ? 4 : XMODE == 3 ? 3 : XMODE == 0 ? 2 : 1) : "memory");
        BAR();
#pragma unroll
        for (int i = 0; i < 2; ++i) { wk[i] = WL[i * 64]; wv[i] = WL[(32 + i) * 64]; }
        SB();
        if constexpr (XMODE == 1) pfinish<2, 1, 0>(rs, l0, (cnt_off(C_X + (tt & 31)) + sd), 16, pvx);
        else if constexpr (XMODE == 3) pfinish2<1, 0>(rs, l0, (cnt_off(C_X + (tt & 31)) + sd), 16, pvy);
        else if constexpr (XMODE == 0) wfinish(rs, l0, (cnt_off(C_X + (tt & 31)) + sd), 16, pvx[0]);
        else { NAP(PS3); wwait(rs, l0, (cnt_off(C_X + (tt & 31)) + sd), 16); }
        STAMP(sb + 1);
#pragma unroll
        for (int ks = 0; ks < DX; ++ks) { xf[ks] = ldr(rs, l16, xb, ks); SB(); }
      }
      STAMP(sb + 26);
      static_for<32>([&](auto KSI) {
        constexpr int ks = KSI, q0 = layer ? 20 : 8;
        aK = mfma32(wk[ks % 3], xf[ks], aK);
        SB();
        if constexpr (ks + 2 < 32) wk[(ks + 2) % 3] = WL[(ks + 2) * 64];
        SB();
        if constexpr (layer > 0 && ks + DX < 32) xf[ks + DX] = ldr(rs, l16, xb, ks + DX);
        if constexpr (ks >= q0 && ks < q0 + 8) dma1<((ks - q0) & 3)>(rs, l16, wqb + ((ks - q0) >> 2) * 4096, L + 65536 + (((2 * w + ((ks - q0) >> 2)) ^ (layer ? 0 : rot)) * 4096));
        if constexpr (ks >= 28 && ks < 28 + wok) wold(IC(ks - 28));
        if constexpr (layer == 0 && ks < 6) cp[ks >> 1][2 * (ks & 1)] = __builtin_bit_cast(float, ld32<SC1>(rs, ln * 4 + (ks & 1) * 512 + (ks >> 1) * 1024, CS_QKV + hp * 3072));
        SB();
        aV = mfma32(wv[ks % 3], xf[ks], aV);
        SB();
        if constexpr (ks + 2 < 32) wv[(ks + 2) % 3] = WL[(32 + ks + 2) * 64];
        SB();
        if constexpr (layer == 0) {
          if constexpr (ks < 6) cp[ks >> 1][2 * (ks & 1) + 1] = __builtin_bit_cast(float, ld32<SC1>(rs, ln * 4 + (ks & 1) * 512 + 256 + (ks >> 1) * 1024, CS_QKV + hp * 3072));
          if constexpr (ks == 18 || ks == 22) res[(ks - 18) >> 2] = ld128<0>(rsx, r * 1024 + hf * 16 + ((ks - 18) >> 2) * 32, tte * 32768 + n0 * 2);
          if constexpr (ks == 19 || ks == 21 || ks == 23) { constexpr int t = (ks - 19) / 2; cp[t][0] = (cp[t][0] + cp[t][1]) + (cp[t][2] + cp[t][3]); PIN(cp[t][0]); }
          if constexpr (ks == 20 || ks == 22 || ks == 24) { constexpr int t = (ks - 20) / 2; u32 cw = cs_word(cp[t][0], hf); PIN(cw); csq[t] = cw; }
          if constexpr (ks == 19) add1(rs, w >= 2 ? l0 : 0x80000000u, jadd);
        } else {
          if constexpr (ks < 4) sp[ks] = __builtin_bit_cast(f32x4, ld128<SC1>(rs, l16 + ks * 1024, stb));
          if constexpr (ks == 1) pretire(pvy);
          if constexpr (ks >= 16 && ks < 24) stats_step<ks - 16>(zs_, sp);
          if constexpr (ks == 24) { rstd = zs_.rstd; c = rstd * LOG2E; PIN(c); }
        }
        SB();
        if constexpr (ks == 25) aK = mean_fix<0>(csq, zs_.mw, aK);
        if constexpr (ks == 27) aV = mean_fix<1>(csq, zs_.mw, aV);
        SB();
      });
      STAMP(sb + 2);
      const u32 w1b = opq(W1_T + fb * 131072);
      auto w1dma = [&](auto I) {
        constexpr int i = I;
        const u32 o = i < 16 ? w * 16384 + (i >> 2) * 4096 : (i < 24 ? 98304 : 65536) + w * 8192 + ((i >> 2) & 1) * 4096;
        dma1<(i & 3)>(rs, l16, w1b + o, L + (o ^ rx));
      };
      auto pf = [&](auto N) {
        constexpr int n = N + wok;
        if constexpr (n < 16) wold(IC(n));
        else w1dma(IC(n - 16 + (layer && n >= 32 ? 8 : 0)));
        SB();
      };
      {
        const v4u k0 = pack8(aK, 0), k1 = pack8(aK, 1);
        f32x16 p = mfma32(wph[0], k0, zero16);
        p = mfma32(wph[1], k1, p);
        SB();
        if constexpr (layer == 0 && PRB == 0) { vv = peek(rs, l0, cnt_off(C_H + (b >> 5) * 128 + (w & 1) * 64 + (b & 31) * 2 + (w >> 1))); SB(); }
#pragma unroll
        for (int i = 0; i < 16; ++i) { aV[i] *= rstd; PIN(aV[i]); }
        SB();
        *(LAS v4u*)(a_v) = pack8(aV, 0);
        *(LAS v4u*)(a_v + 16) = pack8(aV, 1);
        SB();
        if constexpr (layer == 0 && PRB == 1) { vv = peek(rs, l0, cnt_off(C_H + (b >> 5) * 128 + (w & 1) * 64 + (b & 31) * 2 + (w >> 1))); SB(); }
        u32 o[16];
        auto look = [&](auto I) {
          if constexpr (layer == 0) {
            u32 t;
            if constexpr (I >= LK0) asm volatile("s_getreg_b32 %1, hwreg(HW_REG_IB_STS, 0, 4)\n\ts_cmp_gt_u32 %1, %2\n\ts_addc_u32 %0, %0, 0" : "+s"(lat), "=&s"(t) : "n"(I > JPK ? 1 : 0) : "scc");
            if constexpr (I == JPK) jv = peek(rs, w != 0 ? 0x80000000u : jvo, 0);
          }
        };
        dual_softmax(p, c, o, look);
        static_for<4>([&](auto I) {
          constexpr int i = I;
          *(LAS v4u*)(a_pk + i * 16) = v4u{o[4 * i], o[4 * i + 1], o[4 * i + 2], o[4 * i + 3]};
          if constexpr (layer == 0 && (i & 1)) { SB(); look(IC(11)); SB(); }
        });
        if constexpr (layer == 0) *(LAS u32*)(L + XS_OFF + w * 4) = lat;
      }
      SB();
      aQ = mean_fix<2>(csq, zs_.mw, zero16);
      STAMP(sb + 3);
      asm volatile("s_waitcnt vmcnt(%0)" ::"n"(wok) : "memory");
      if constexpr (layer == 0) {
        asm volatile("" ::"v"(vv));
        const u32 need = ln == 0 ? 36u : ln == 1 ? 16u : 0u;
        if (w == 0 && (__builtin_amdgcn_ballot_w64(jv >= need) & 7ull) != 7ull) poll3(rs, jvo, need);
      }
      BAR();
      v4u lt;
      if constexpr (layer == 0) { lt = *(LAS v4u*)(L + XS_OFF); sd = 0; }
      const u32 pbase = KVP + (layer * 64 + 4 * hp + w) * 16384, zbase = ZP + (layer * 16 + hp) * 16384 + w * 2048;
      v4u wq[4];
      auto qfrag = [&](int ks) { return WQ[ks * 64]; };
      {
        f32x4 kv = {0.f, 0.f, 0.f, 0.f}, zz = {0.f, 0.f, 0.f, 0.f};
        const v4u ones = {ONES2, ONES2, ONES2, ONES2};
        v2u a0[4], a1[4], b0[4], b1[4];
#pragma unroll
        for (int ks = 0; ks < 4; ++ks) {
          a0[ks] = trread(a_pa + (32 * ks) * PK_STR); a1[ks] = trread(a_pa + (32 * ks + 4) * PK_STR);
          b0[ks] = trread(a_pb + (32 * ks) * VV_STR); b1[ks] = trread(a_pb + (32 * ks + 4) * VV_STR);
          SB();
        }
#pragma unroll
        for (int ks = 0; ks < 4; ++ks) {
          const v4u A = {a0[ks].x, a0[ks].y, a1[ks].x, a1[ks].y}, B = {b0[ks].x, b0[ks].y, b1[ks].x, b1[ks].y};
          kv = mfma16(A, B, kv);
          zz = mfma16(ones, A, zz);
          asm volatile("" : "+a"(zz));
          SB();
        }
#pragma unroll
        for (int ks = 0; ks < 4; ++ks) wq[ks] = qfrag(ks);
        SB();
        const v2u kvb = {pk(kv[0], kv[1]), pk(kv[2], kv[3])};
        st64<SC1>(kvb, rs, o_kv, pbase + rt * 512 + sd);
        st64<SC1>(kvb, rs, o_kv, pbase + rt * 512 + (sd ^ 8192));
        const float z0 = zz[0];
        __builtin_amdgcn_raw_buffer_store_b32(__builtin_bit_cast(u32, z0), rs, o_z, zbase + rt * 128, SC1);
      }
      SB();
      STAMP(sb + 4);
      auto qstep = [&](auto I) {
        constexpr int i = I;
        aQ = mfma32(wq[i & 3], xf[i], aQ);
        SB();
        if constexpr (i + 4 < 32) wq[i & 3] = qfrag(i + 4);
      };
      LAS unsigned char *a_kz, *a_kw, *a_kr;
      u32 o_st, o_zl, l2k;
      v4u sel;
      if constexpr (layer == 0) {
        const int df = __builtin_amdgcn_readfirstlane((int)(lt[0] + lt[2]) - (int)(lt[1] + lt[3]));
        sd = df > 0 ? 8192 : 0;
        const u32 vote = df == 0 ? 1 : (((df > 0) ^ (xcd >> 2)) & 1) * 2;
        (void)__builtin_amdgcn_raw_ptr_buffer_atomic_add_i32(1 + (vote << 16), rs, (w == 0 && j < 4 && ln < 16) ? ln * CSTR : 0x80000000u, cnt_off(C_V), 0);
        SB();
      }
      const u32 skv = cnt_off(C_KV + layer * 128 + hp * 4 + w), ckv = skv + sd;
      u32 pvk[2];
      static_for<32>([&](auto I) {
        constexpr int i2 = I - QS, n2 = 32 - QS;
        if constexpr (I == QS) { wsignal<NQ1 + (layer ? 0 : 1)>(rs, l2k, skv); STAMP(sb + 5); }
        qstep(I);
        if constexpr (I < QS) { if constexpr ((I * NQ1 + QS - 1) / QS != ((I + 1) * NQ1 + QS - 1) / QS) pf(IC(s_q1 + (I * NQ1 + QS - 1) / QS)); }
        else {
          if constexpr (layer == 0 && i2 == VPK) { vv = peek(rs, w == 0 ? l0 : 0x80000000u, cnt_off(C_V + (b & 15))); SB(); }
          if constexpr ((i2 == KVPK1 || i2 == KVPK2) && KMODE == 2) apeek(rs, l0, ckv, pvk[i2 == KVPK2]);
          if constexpr ((i2 * NQ2 + n2 - 1) / n2 != ((i2 + 1) * NQ2 + n2 - 1) / n2) pf(IC(s_q2 + (i2 * NQ2 + n2 - 1) / n2));
        }
        const u32 l2 = opqv(l16) >> 4, g2 = l2 >> 4, c2 = l2 & 15;
        if constexpr (I == 2) l2k = opqv(l2 < 2 ? l2 * 8192 : 0x80000000u);
        if constexpr (I == 1) o_zl = opqv(g2 * 128 + c2 * 4);
        if constexpr (I == 3) a_kz = L + opqv(KF_OFF + w * 544 + (c2 >> 3) * 272 + 256 + (c2 & 7) * 2);
        if constexpr (I == 5) a_kw = L + opqv(KF_OFF + w * 544 + (g2 >> 1) * 272 + (g2 & 1) * 8 + rho32(c2 & 7, c2 >> 3) * 16);
        if constexpr (I == 7) a_kr = L + opqv(KF_OFF + (l2 >> 5) * 272 + ((l2 & 31) < 16 ? (l2 & 31) : 16) * 16);
        if constexpr (I == 9) o_st = opqv(l2 >= 32 ? 0x80000000u : c2 * 16 + g2 * 8);
        if constexpr (I == 11 || I == 13 || I == 15 || I == 17) {
          constexpr int m = (I - 11) / 2;
          sel[m] = opqv((c2 >> 1) == 4 * (g2 & 1) + m ? 0x3f80u << (16 * (c2 & 1)) : 0u);
        }
        SB();
      });
      const v4u q0 = pack8(aQ, 0), q1 = pack8(aQ, 1);
      f32x16 pq = mfma32(wph[0], q0, zero16);
      pq = mfma32(wph[1], q1, pq);
      SB();
      v4u phq[4];
      v4u v[8];
      f32x4 z;
      {
        u32 o[16];
        dual_softmax(pq, c, o, [&](auto I) {
          constexpr int i = I;
          if constexpr (i == KVCHK) {
            STAMP(sb + 6);
            if constexpr (KMODE == 0) wfinish(rs, l0, ckv, 16, pvk[0]);
            else pfinish2<1 + NQ2 - (KVPK1 * NQ2 + 31 - QS) / (32 - QS) + (!layer && VPK > KVPK1), NQ2 - (KVPK2 * NQ2 + 31 - QS) / (32 - QS) + (!layer && VPK > KVPK2)>(rs, l0, ckv, 16, pvk);
            STAMP(sb + 7);
#pragma unroll
            for (int k = 0; k < 4; ++k) z[k] = __builtin_bit_cast(float, ld32<SC1>(rs, o_zl + k * 512, zbase + sd));
            SB();
          }
          static_for<8>([&](auto K) {
            constexpr int k = K, at = KVCHK + (k + 1) / KVLD;
            if constexpr ((at < 9 ? at : 9) == i) { {
              v[k] = ldf<SC1>(rs, l16, pbase + (sd ^ 8192), k * 1024);
              SB();
              if constexpr (layer == 0 && k == 7) { vb = peek(rs, w == 0 ? l0 : 0x80000000u, cnt_off(C_V + (b & 15))); SB(); }
            } }
          });
        });
#pragma unroll
        for (int m = 0; m < 4; ++m) {
          auto sw = __builtin_amdgcn_permlane32_swap(o[m], o[4 + m], false, false);
          phq[0][m] = sw[0];
          phq[2][m] = sw[1];
          auto sx = __builtin_amdgcn_permlane32_swap(o[8 + m], o[12 + m], false, false);
          phq[1][m] = sx[0];
          phq[3][m] = sx[1];
        }
#pragma unroll
        for (int m = 0; m < 4; ++m) PIN(phq[m]);
      }
      SB();
      {
        const float zs = sum32(sum16((z[0] + z[1]) + (z[2] + z[3])));
        *(LAS unsigned short*)(a_kz) = (unsigned short)pk(zs, zs);
        if constexpr (KMODE == 2) pretire(pvk);
        SB();
        f32x4 sv = {0.f, 0.f, 0.f, 0.f};
#pragma unroll
        for (int i = 0; i < 8; ++i) { sv = mfma16(sel, v[i], sv); SB(); }
        *(LAS v2u*)(a_kw) = v2u{pk(sv[0], sv[1]), pk(sv[2], sv[3])};
      }
      if constexpr (layer == 0) {
        if (w == 0) {
          if ((__builtin_amdgcn_readfirstlane(vv) & 0xffffu) < 32u) {
            vv = vb;
#pragma unroll 1
            for (u32 spins = 0; spins < (1u << 16) && (__builtin_amdgcn_readfirstlane(vv) & 0xffffu) < 32u; ++spins) vv = peek(rs, l0, cnt_off(C_V + (b & 15)));
          }
          if (ln == 0) *(LAS u32*)(L + XS_OFF + 16) = vv;
        }
      }
      BAR();
      if constexpr (layer == 0) vv = *(LAS u32*)(L + XS_OFF + 16);
      STAMP(sb + 8);
      {
        v4u kvf[4];
#pragma unroll
        for (int i = 0; i < 4; ++i) kvf[i] = *(LAS v4u*)(a_kr + i * 544);
        SB();
        f32x16 nd[2];
        nd[0] = mfma32(kvf[0], phq[0], zero16);
        nd[0] = mfma32(kvf[1], phq[1], nd[0]);
        nd[1] = mfma32(kvf[2], phq[2], zero16);
        nd[1] = mfma32(kvf[3], phq[3], nd[1]);
        SB();
        if constexpr (layer == 0) {
          sd = opq(((((u32)__builtin_amdgcn_readfirstlane(vv) >> 16 > 32u ? 1u : 0u) ^ (xcd >> 2) ^ NFLIP) & 1u) * 8192);
        }
#pragma unroll
        for (int h = 0; h < 2; ++h) {
          const float inv = __builtin_amdgcn_rcpf(nd[h][8] + 1e-6f);
          v4u ob;
#pragma unroll
          for (int m = 0; m < 4; ++m) ob[m] = pk(nd[h][2 * m] * inv, nd[h][2 * m + 1] * inv);
          st128<SC1>(ob, rs, l16 + h * 1024, OB + layer * MB2 + (tt & 31) * 65536 + blk(hp * 2048) + (sd ^ (layer * 8192)));
          SB();
        }
      }
      STAMP(sb + 9);
      static_for<ODR>([&](auto I) { pf(IC(s_od + I)); });
      wsignal<ODR>(rs, l0, cnt_off(C_O + layer * 128 + ((tt >> 1) & 15) * 4 + (hp >> 2)) + sd);
      if constexpr (layer == 0) asm volatile("" ::"v"(vb));
      STAMP(sb + 10);
      const u32 co = cnt_off(C_O + layer * 128 + (rt2 & 15) * 4 + w) + sd;
      u32 pvo[3], pvz[2];
      BAR();
      if constexpr (OMODE == 1) {
        apeek(rs, l0, co, pvo[0]);
        NAP(PS2);
        apeek(rs, l0, co, pvo[1]);
        NAP(PS2);
        apeek(rs, l0, co, pvo[2]);
        static_for<n_op>([&](auto I) { pf(IC(s_op + I)); });
        pfinish<2 + n_op, 1 + n_op, n_op>(rs, l0, co, 8, pvo);
      } else if constexpr (OMODE == 2) {
        NAP(PS4);
        apeek(rs, l0, co, pvz[0]);
        static_for<n_op>([&](auto I) { pf(IC(s_op + I)); });
        NAP(PS6);
        apeek(rs, l0, co, pvz[1]);
        pfinish2<1 + n_op, 0>(rs, l0, co, 8, pvz);
      } else {
        NAP(PS4);
        pvo[0] = peek(rs, l0, co);
        SB();
        static_for<n_op>([&](auto I) { pf(IC(s_op + I)); });
        wfinish(rs, l0, co, 8, pvo[0]);
      }
      STAMP(sb + 11);
      const u32 ab = OB + layer * MB2 + w * 16384 + (sd ^ (layer * 8192));
      v4u af[DXV];
      splitk_phase<FrO, DXV, 0>(rs, wo, af, ab + (tte & 31) * 65536, ab + ((tte & 31) ^ 1) * 65536, L, w, l16, 1.f, res, [&](int) {}, [&]() {}, [&](auto I) {
        if constexpr (I == 1 && OMODE == 1) pretire(pvo);
        if constexpr (I == 1 && OMODE == 2) pretire(pvz);
        if constexpr (I >= 4 && s_p2 + I - 4 < s_xd) pf(IC(s_p2 + I - 4));
      });
      STAMP(sb + 12);
      resid_store(rs, res, l16, o_st, xtile(2 * layer, tte) + blk(n0 * 64), stile(2 * layer, tte) + (n0 >> 5) * 256);
      STAMP(sb + 13);
      static_for<XDR>([&](auto I) { pf(IC(s_xd + I)); });
      wsignal<XDR>(rs, l0, (cnt_off(C_X1 + layer * 32 + (tte & 31)) + sd));
      STAMP(sb + 14);
    }
    const u32 lh = opqv(opqv(lane) < nlh ? lane * CSTR : 0x80000000u);
    v4u af[WINH];
    const u32 ch0 = cnt_off(C_H + layer * 512 + (rt2 & 15) * 4 + w) + sd;
    const u32 sh0 = cnt_off(C_H + layer * 512 + ((tt >> 1) & 15) * 4 + (2 * fb) / 7) + sd;
    auto htile = [&](u32 t) { return HB + layer * 4 * MB2 + (t & 31) * 262144 + (sd ^ ((t & 8) * 1024)); };
    auto xtile = [&](int i, u32 t) { return XT + i * MB2 + (t & 31) * 65536 + (sd ^ ((i & 1) ? 0 : 8192)); };
    auto stile = [&](int i, u32 t) { return ST + i * ST_SZ + blk((t & 31) * 4096) + sd; };
    const u32 hown = htile(tte) + w * 16384, hoth = hown ^ 262144;
    {
      const u32 ln = opqv(lane), l16 = ln * 16, r = ln & 31, hf = ln >> 5;
      auto w2ld = [&](auto I) {
        constexpr int i = I, k = (i >> 1) % 7;
        w2f[i >> 1][i & 1] = ld128<SC1>(rs, l16 + (k & 1) * 2048, wb + (i / 14) * 57344 + (k >> 1) * 4096 + ((i & 1) ? 1024 - wsw : wsw));
      };
      auto hld = [&](int i) { if (i < HPRE) { af[i] = ld_fr<FrH>(rs, l16, hown, hoth, i); SB(); } };
      if (p3on) {
        static_for<3>([&](auto J) {
          if constexpr (XMODE == 1) apeek(rs, l0, (cnt_off(C_X1 + layer * 32 + (tt & 31)) + sd), pvx[J]);
          if constexpr (XMODE == 0 && J == (layer ? 0 : 1)) { if constexpr (layer) NAP(PS3 + 1); pvx[0] = peek(rs, l0, (cnt_off(C_X1 + layer * 32 + (tt & 31)) + sd)); }
          if constexpr (XMODE == 3 && J > 0) { if constexpr (layer) NAP(J == 1 ? PS3 + 1 : PS5); apeek(rs, l0, (cnt_off(C_X1 + layer * 32 + (tt & 31)) + sd), pvy[J - 1]); }
          SB();
          if constexpr (layer == 0) static_for<W2D / 3>([&](auto I) { w2ld(IC(J * (W2D / 3) + I)); SB(); });
          else if constexpr (J < 2 && XMODE == 1) NAP(PS2);
        });
        constexpr int DX = DXV;
        v4u xf[32];
        const u32 xb = xtile(2 * layer, tt);
        f32x4 sp[4];
        const u32 stb = stile(2 * layer, tt);
        LAS v4u* WL = (LAS v4u*)(L + l16);
        LAS v4u* WH = (LAS v4u*)(L + opqv(l16 + 65536));
        u32 hb[2];
#pragma unroll
        for (int s = 0; s < 2; ++s) hb[s] = opq(htile(tt) + ((2 * fb + s) / 7) * 16384 + ((2 * fb + s) % 7) * 1024);
        SB();
        asm volatile("s_waitcnt vmcnt(%0)" ::"n"((layer == 0 ? W2D : 0) + (XMODE == 1 ? 4 : XMODE == 3 ? 3 : XMODE == 0 ? 2 : 1)) : "memory");
        BAR();
        v4u wa[3], wc[3], wq[4];
#pragma unroll
        for (int i = 0; i < 2; ++i) { wa[i] = WL[i * 64]; wc[i] = WL[(32 + i) * 64]; }
        SB();
        STAMP(sb + 15);
        if constexpr (XMODE == 0) wfinish(rs, l0, (cnt_off(C_X1 + layer * 32 + (tt & 31)) + sd), 16, pvx[0]);
        else if constexpr (XMODE == 3) pfinish2<1 + (layer ? 0 : 2 * W2D / 3), (layer ? 0 : W2D / 3)>(rs, l0, (cnt_off(C_X1 + layer * 32 + (tt & 31)) + sd), 16, pvy);
        else if constexpr (XMODE == 2) { if constexpr (layer) NAP(PS3); wwait(rs, l0, (cnt_off(C_X1 + layer * 32 + (tt & 31)) + sd), 16); }
        else pfinish<2 + (layer ? 0 : W2D), 1 + (layer ? 0 : 2 * W2D / 3), (layer ? 0 : W2D / 3)>(rs, l0, (cnt_off(C_X1 + layer * 32 + (tt & 31)) + sd), 16, pvx);
        STAMP(sb + 16);
#pragma unroll
        for (int ks = 0; ks < DX; ++ks) { xf[ks] = ldr(rs, l16, xb, ks); SB(); }
        f32x16 acc[4];
        acc[0] = zero16;
        acc[1] = zero16;
        float nc, cp[4][4];
        Stats zs_;
        SB();
        static_for<32>([&](auto KSI) {
          constexpr int ks = KSI;
          acc[0] = mfma32(wa[ks % 3], xf[ks], acc[0]);
          SB();
          if constexpr (ks + 2 < 32) wa[(ks + 2) % 3] = WL[(ks + 2) * 64];
          else wq[2 * (ks - 30)] = WH[(2 * (ks - 30)) * 64];
          SB();
          if constexpr (ks + DX < 32) xf[ks + DX] = ldr(rs, l16, xb, ks + DX);
          if constexpr (layer == 0 && ks >= 20) w2ld(IC(W2D + ks - 20));
          if constexpr (layer == 0 && ks >= 20 && ks < 28) {
            constexpr int t = (ks - 20) / 2;
            if constexpr (ks & 1) { u32 cw = cs_word(cp[t][0], hf); PIN(cw); cs1[t] = cw; }
            else { cp[t][0] = (cp[t][0] + cp[t][1]) + (cp[t][2] + cp[t][3]); PIN(cp[t][0]); }
          }
          SB();
          acc[1] = mfma32(wc[ks % 3], xf[ks], acc[1]);
          SB();
          if constexpr (ks + 2 < 32) wc[(ks + 2) % 3] = WL[(32 + ks + 2) * 64];
          else wq[2 * (ks - 30) + 1] = WH[(2 * (ks - 30) + 1) * 64];
          SB();
          if constexpr (ks < 4) sp[ks] = __builtin_bit_cast(f32x4, ld128<SC1>(rs, l16 + ks * 1024, stb));
          if constexpr (layer == 0 && ks >= 4 && ks < 8) {
#pragma unroll
            for (int i = 0; i < 4; ++i) cp[ks - 4][i] = __builtin_bit_cast(float, ld32<SC1>(rs, ln * 4 + i * 256 + (ks - 4) * 1024, CS_W1 + fb * 4096));
          }
          if constexpr (ks == 1) pretire(pvy);
          if constexpr (ks >= 16 && ks < 24) stats_step<ks - 16>(zs_, sp);
          if constexpr (ks == 24) { nc = zs_.rstd * -LOG2E; PIN(nc); }
          SB();
          if constexpr (ks == 27) acc[0] = mean_fix<0>(cs1, zs_.mw, acc[0]);
          if constexpr (ks == 28) acc[1] = mean_fix<1>(cs1, zs_.mw, acc[1]);
          if constexpr (ks == 29) acc[2] = mean_fix<2>(cs1, zs_.mw, zero16);
          if constexpr (ks == 30) acc[3] = mean_fix<3>(cs1, zs_.mw, zero16);
          SB();
        });
        STAMP(sb + 17);
        float tv[64], ev[64];
        v4u ho[8];
        u32 pv0;
        auto stage = [&](auto V, auto ST_) {
          constexpr int v = V, st = ST_;
          if constexpr (st == 0) { tv[v] = acc[v >> 4][v & 15] * nc; PIN(tv[v]); }
          else if constexpr (st == 1) { ev[v] = __builtin_amdgcn_exp2f(tv[v]); PIN(ev[v]); }
          else if constexpr (st == 2) { ev[v] = 1.f + ev[v]; PIN(ev[v]); }
          else if constexpr (st == 3) { ev[v] = __builtin_amdgcn_rcpf(ev[v]); PIN(ev[v]); }
          else if constexpr (st == 4) { tv[v] = tv[v] * ev[v]; PIN(tv[v]); }
          else if constexpr (v & 1) {
            u32 hh = pk(tv[v - 1], tv[v]);
            PIN(hh);
            ho[v >> 3][(v >> 1) & 3] = hh;
            if constexpr ((v & 7) == 7) st128<SC1>(ho[v >> 3], rs, l16, hb[(v >> 3) & 1] + (v >> 4) * 65536);
          }
        };
        static_for<64>([&](auto GI) {
          constexpr int gi = GI, t = 2 + (gi >> 5), ks = gi & 31;
          acc[t] = mfma32(wq[gi & 3], xf[ks], acc[t]);
          SB();
          if constexpr (gi + 4 < 64) wq[gi & 3] = WH[(gi + 4) * 64];
          if constexpr (layer == 0 && p3_w2gap(gi)) w2ld(IC(W2D + 12 + (gi * W2B) / 8));
          SB();
          static_for<48>([&](auto V) {
            constexpr int d = gi - p3_g0(V);
            if constexpr (d >= 0 && d < 6) stage(V, IC(d));
          });
          if constexpr (gi == HS0) wsignal<p3_younger(layer, 0, HS0)>(rs, lh, sh0);
          if constexpr (gi == HS1) wsignal<p3_younger(layer, 1, HS1)>(rs, lh, sh0 + 128 * CSTR);
          if constexpr (gi == PK0) pv0 = peek(rs, l0, ch0);
          SB();
        });
        STAMP(sb + 18);
        constexpr int W2E = 56 - W2D - 12 - 8 * W2B;
        static_assert(W2E >= 0, "w2 schedule");
        {
          f32x2 t2[8], e2[8];
#pragma unroll
          for (int m = 0; m < 8; ++m) { t2[m] = f32x2{acc[3][2 * m], acc[3][2 * m + 1]} * nc; PIN(t2[m]); }
          SB();
#pragma unroll
          for (int m = 0; m < 8; ++m) { e2[m] = f32x2{__builtin_amdgcn_exp2f(t2[m].x), __builtin_amdgcn_exp2f(t2[m].y)}; PIN(e2[m]); }
          SB();
          if constexpr (HPRE > 0) wfinish(rs, l0, ch0, 8, pv0);
          STAMP(sb + 21);
          hld(0);
#pragma unroll
          for (int m = 0; m < 8; ++m) { e2[m] = e2[m] + 1.f; PIN(e2[m]); }
          SB();
#pragma unroll
          for (int m = 0; m < 8; ++m) { e2[m] = f32x2{__builtin_amdgcn_rcpf(e2[m].x), __builtin_amdgcn_rcpf(e2[m].y)}; PIN(e2[m]); if (m == 1 || m == 5) { SB(); hld(1 + m / 4); } }
          SB();
#pragma unroll
          for (int m = 0; m < 8; ++m) { t2[m] = t2[m] * e2[m]; PIN(t2[m]); }
          SB();
#pragma unroll
          for (int i = 3; i < HPRE; ++i) hld(i);
#pragma unroll
          for (int s = 0; s < 2; ++s)
            st128<SC1>(v4u{pk(t2[4 * s].x, t2[4 * s].y), pk(t2[4 * s + 1].x, t2[4 * s + 1].y), pk(t2[4 * s + 2].x, t2[4 * s + 2].y), pk(t2[4 * s + 3].x, t2[4 * s + 3].y)},
                       rs, l16, hb[s] + 3 * 65536);
        }
        SB();
        wsignal<p3_younger(layer, 2, 64) + HPRE + 2>(rs, lh, sh0 + 256 * CSTR);
        if constexpr (layer == 0) static_for<W2E>([&](auto E) { w2ld(IC(W2D + 12 + 8 * W2B + E)); });
        SB();
        STAMP(sb + 19);
        if constexpr (HPRE == 0) wfinish(rs, l0, ch0, 8, pv0);
      } else {
        if constexpr (layer == 0) static_for<56>([&](auto E) { w2ld(E); });
        DRAIN();
        BAR();
        wwait(rs, l0, ch0, 8);
#pragma unroll
        for (int i = 0; i < HPRE; ++i) hld(i);
      }
    }
    {
      const u32 ln = opqv(lane), l16 = ln * 16, r = ln & 31, hf = ln >> 5;
      const u32 wqb = opq(WQKV_T + hp * 98304 + w * 16384);
      u32 pv1[2], pv2[2], pv3[2];
      splitk_phase<FrH, WINH, HPRE>(rs, w2f, af, hown, hoth, L, w, l16, -LN2, res,
        [&](int i) { if (i == T1A || i == T1B) apeek(rs, l0, ch0 + 128 * CSTR, pv1[i == T1B]); },
        [&]() {
          wsignal<3 + 14 - HPRE>(rs, lh, sh0 + 384 * CSTR);
          STAMP(sb + 22);
          pfinish2<14 - T1A + 2, 14 - T1B + 1>(rs, l0, ch0 + 128 * CSTR, 8, pv1);
          STAMP(sb + 20);
        },
        [&](auto I) {
          constexpr int i = I, g = i - 42;
          if constexpr (i == 15) pretire(pv1);
          constexpr auto ops = [](int a, int b) {
            int n = 0;
            for (int q = 14; q < 56; ++q) n += FrH::lstep(q, WINH) >= a && FrH::lstep(q, WINH) < b;
            for (int q : {PK2, PK2B, PK3, PK3B}) n += q > a && q < b;
            return n;
          };
          static_assert(PK2 < PK2B && PK2B < T2CHK && PK2B != PK3 && PK2 != PK3 && PK3 < PK3B && PK3B < T3CHK && T2CHK == 14 && T3CHK >= 21 && T3CHK <= 28, "peek order");
          if constexpr (i == T2CHK) pfinish2<ops(PK2, i), ops(PK2B, i)>(rs, l0, ch0 + 256 * CSTR, 8, pv2);
          if constexpr (i == T3CHK) { STAMP(sb + 27); pfinish2<ops(PK3, i), ops(PK3B, i)>(rs, l0, ch0 + 384 * CSTR, 8, pv3); STAMP(sb + 28); }
          if constexpr (i == PK2 || i == PK2B) apeek(rs, l0, ch0 + 256 * CSTR, pv2[i == PK2B]);
          if constexpr (i == PK3 || i == PK3B) apeek(rs, l0, ch0 + 384 * CSTR, pv3[i == PK3B]);
          if constexpr (i == 29) pretire(pv2);
          if constexpr (i == 36) pretire(pv3);
          if constexpr (KWPOS == 0) {
            if constexpr (layer == 0 && g >= 10 && g <= 14) dma1<g - 10>(rs, l16, wqb, L + w * 16384);
            if constexpr (layer == 0 && g >= 19 && g <= 21) dma1<g - 14>(rs, l16, wqb, L + w * 16384);
          } else {
            if constexpr (layer == 0 && g >= 10 && g < 26) dma1<((g - 10) & 3)>(rs, l16, wqb + ((g - 10) >> 2) * 4096, L + ((w * 16384 + ((g - 10) >> 2) * 4096) ^ rx));
          }
        });
      STAMP(sb + 23);
      if constexpr (layer == 0) {
        resid_store(rs, res, l16, hf ? 0x80000000u : (r & 15) * 16 + (r >> 4) * 8, xtile(1, tte) + blk(n0 * 64), stile(1, tte) + (n0 >> 5) * 256);
        STAMP(sb + 24);
        if constexpr (KWPOS == 0) dma_range<8, 16>(rs, l16, wqb, L + w * 16384);
        wsignal<KWPOS == 0 ? 8 : 0>(rs, l0, (cnt_off(C_X + (tte & 31)) + sd));
        STAMP(sb + 25);
      } else {
        const rsrc_t rso = mkrsrc(out, S * D * 2);
#pragma unroll
        for (int s = 0; s < 2; ++s) st128<OAUX>(res[s], rso, r * 1024 + hf * 16 + s * 32, tte * 32768 + n0 * 2);
        if (b == 255 && tid < 16) st64<SC1>(v2u{tag_lo, tag_hi}, rs, tid * 8, EPOCH_OFF);
        STAMP(sb + 24);
      }
    }
  });
  asm volatile(".Lkcode1:");
}

extern "C" void kernel_launch(void* const* d_in, const int* in_sizes, int n_in, void* d_out, int out_size, void* d_ws,
                              size_t ws_size, hipStream_t stream) {
  (void)hipFuncSetAttribute((const void*)k_main, hipFuncAttributeMaxDynamicSharedMemorySize, LDS_BYTES);
  k_main<<<256, 256, LDS_BYTES, stream>>>((const uint16_t*)d_in[0], (const uint16_t*)d_in[1], (const uint16_t*)d_in[2],
                                          (const uint16_t*)d_in[3], (const uint16_t*)d_in[5], (const uint16_t*)d_in[6],
                                          (unsigned char*)d_ws, (unsigned char*)d_out);
}
```

```cpp
#define DL_AT 53
#define DL_T 5500
#define DL_C (b < 8)
#include <hip/hip_runtime.h>
#include <hip/hip_bf16.h>
#include <stdint.h>
#include <utility>

typedef unsigned int u32;
typedef u32 v4u __attribute__((ext_vector_type(4)));
typedef u32 v2u __attribute__((ext_vector_type(2)));
typedef short s16x4 __attribute__((ext_vector_type(4)));
typedef __bf16 bf16x8 __attribute__((ext_vector_type(8)));
typedef __bf16 bf16x2 __attribute__((ext_vector_type(2)));
typedef float f32x2 __attribute__((ext_vector_type(2)));
typedef float f32x4 __attribute__((ext_vector_type(4)));
typedef float f32x16 __attribute__((ext_vector_type(16)));
#define LAS __attribute__((address_space(3)))
#define GAS __attribute__((address_space(1)))
#define DI __device__ __forceinline__
typedef __amdgpu_buffer_rsrc_t rsrc_t;

#define DXV 12
#define WINH 56
#define HS0 36
#define HS1 56
#define PK0 59
#define HPRE 0
#define PK2 0
#define T1A 0
#define T1B 7
#define PS5 5
#define PK3 7
#define PK2B 6
#define PK3B 13
#define T2CHK 14
#define T3CHK 21
#define KVCHK 0
#define KVLD 2
#define ODR 8
#define OAUX 0
#define PS1 3
#define PS2 4
#define XMODE 3
#define KMODE 2
#define NQ1 11
#define NQ2 10
#define OPL0 4
#define OPL1 4
#define XDR 4
#define OMODE 2
#define PS6 3
#define PS4 2
#define KWPOS 1
#define PS3 2
#define WOK 2
#define CPF 1
#define CPW 1
#define TLBD 0
#define LK0 4
#define WSB 0
#define VPK 18
#define PRB 1
#define NFLIP 0
#define ROT2 0
#define QS 11
#define ROT 1
#define XBAR 1
#define JPK 0
#define XA 8
#define XB 20
#define UB 10
#define FPK 4
#define FCK 6
#define KVPK2 14
#define KVPK1 4
#define TLBW 262144
#define W2D 12
#define W2B 4
constexpr int S = 2048, D = 512;
constexpr u32 CTRL = 0;
constexpr u32 WQKV_T = 262144;
constexpr u32 WPHI_T = WQKV_T + 16 * 98304;
constexpr u32 WO_T = WPHI_T + 32768;
constexpr u32 W1_T = WO_T + 524288;
constexpr u32 W2_T = W1_T + 14 * 131072;
constexpr u32 CS_QKV = W2_T + 8 * 4 * 28 * 2048;
constexpr u32 CS_W1 = CS_QKV + 65536;
constexpr u32 MB2 = 2097152;
constexpr u32 XT = 3 * MB2;
constexpr u32 OB = 6 * MB2;
constexpr u32 HB = 8 * MB2;
constexpr u32 ST = 16 * MB2;
constexpr u32 ST_SZ = 262144;
constexpr u32 ZP = ST + 4 * ST_SZ;
constexpr u32 KVP = 17 * MB2;
constexpr u32 WS_END = 18 * MB2;
static_assert(CS_W1 + 65536 <= XT, "workspace map");
constexpr u32 blk(u32 off) { return (off >> 13) * 16384 + (off & 8191); }
constexpr u32 CSTR = 128;
enum { C_H = 0, C_O = 1024, C_X1 = 1280, C_X = 1408, C_J = 1440, C_V = 1504, C_TMO = 1520, C_KV = 1536, NCNT = 1792 };
constexpr u32 INIT_OFF = CTRL + NCNT * CSTR, EPOCH_OFF = INIT_OFF + 16 * CSTR, FLAG_OFF = EPOCH_OFF + CSTR;
static_assert(FLAG_OFF + 192 * CSTR <= WQKV_T, "control area");

#define STAMP(i) do { if constexpr ((i) == 63) dl0 = __builtin_amdgcn_s_memrealtime(); if constexpr ((i) == DL_AT) { if (DL_C) while (__builtin_amdgcn_s_memrealtime() - dl0 < DL_T) __builtin_amdgcn_s_sleep(1); } } while (0)
constexpr float LOG2E = 1.4426950408889634f;
constexpr float LN2 = 0.6931471805599453f;

DI u32 pk(float a, float b) {
  f32x2 v = {a, b};
  bf16x2 r = __builtin_convertvector(v, bf16x2);
  return __builtin_bit_cast(u32, r);
}
DI float bflo(u32 u) { return __builtin_bit_cast(float, u << 16); }
DI float bfhi(u32 u) { return __builtin_bit_cast(float, u & 0xffff0000u); }
DI f32x16 mfma32(v4u a, v4u b, f32x16 c) {
  f32x16 d = __builtin_amdgcn_mfma_f32_32x32x16_bf16(__builtin_bit_cast(bf16x8, a), __builtin_bit_cast(bf16x8, b), c, 0, 0, 0);
  asm volatile("" : "+a"(d));
  return d;
}
DI f32x4 mfma16(v4u a, v4u b, f32x4 c) {
  return __builtin_amdgcn_mfma_f32_16x16x32_bf16(__builtin_bit_cast(bf16x8, a), __builtin_bit_cast(bf16x8, b), c, 0, 0, 0);
}
DI float dot2(u32 a, u32 b, float c) {
  return __builtin_amdgcn_fdot2_f32_bf16(__builtin_bit_cast(bf16x2, a), __builtin_bit_cast(bf16x2, b), c, false);
}
template <int AUX> DI v4u ld128(rsrc_t rs, u32 voff, u32 soff) { return __builtin_amdgcn_raw_buffer_load_b128(rs, voff, soff, AUX); }
template <int AUX> DI v2u ld64(rsrc_t rs, u32 voff, u32 soff) { return __builtin_amdgcn_raw_buffer_load_b64(rs, voff, soff, AUX); }
template <int AUX> DI u32 ld32(rsrc_t rs, u32 voff, u32 soff) { return __builtin_amdgcn_raw_buffer_load_b32(rs, voff, soff, AUX); }
template <int AUX> DI void st128(v4u v, rsrc_t rs, u32 voff, u32 soff) {
  __builtin_amdgcn_raw_buffer_store_b128(v, rs, voff, soff, AUX);
  asm volatile("s_nop 1" ::"v"(v) : "memory");
}
template <int AUX> DI void st64(v2u v, rsrc_t rs, u32 voff, u32 soff) { __builtin_amdgcn_raw_buffer_store_b64(v, rs, voff, soff, AUX); }
DI rsrc_t mkrsrc(const void* p, u32 bytes) { return __builtin_amdgcn_make_buffer_rsrc((void*)p, 0, (int)bytes, 0x00020000); }
DI float sum32(float v) {
  u32 u = __builtin_bit_cast(u32, v);
  auto r = __builtin_amdgcn_permlane32_swap(u, u, false, false);
  const u32 r0 = r[0], r1 = r[1];
  return __builtin_bit_cast(float, r0) + __builtin_bit_cast(float, r1);
}
DI float sum16(float v) {
  u32 u = __builtin_bit_cast(u32, v);
  auto r = __builtin_amdgcn_permlane16_swap(u, u, false, false);
  const u32 r0 = r[0], r1 = r[1];
  return __builtin_bit_cast(float, r0) + __builtin_bit_cast(float, r1);
}
DI float mix16(float a, float b) {
  auto r = __builtin_amdgcn_permlane16_swap(__builtin_bit_cast(u32, a), __builtin_bit_cast(u32, b), false, false);
  const u32 r0 = r[0], r1 = r[1];
  return __builtin_bit_cast(float, r0) + __builtin_bit_cast(float, r1);
}
DI v2u trread(LAS unsigned char* __restrict__ p) {
  return __builtin_bit_cast(v2u, __builtin_amdgcn_ds_read_tr16_b64_v4i16((LAS s16x4*)p));
}
DI int rho32(int reg, int hf) { return (reg & 3) + 8 * (reg >> 2) + 4 * hf; }
constexpr u32 ONES2 = 0x3f803f80u;

DI v4u pack8(const f32x16& a, int s) {
  v4u r;
#pragma unroll
  for (int m = 0; m < 4; ++m) r[m] = pk(a[8 * s + 2 * m], a[8 * s + 2 * m + 1]);
  return r;
}

#define SB() __builtin_amdgcn_sched_barrier(0)
#define PIN(x) asm volatile("" : "+v"(x))
#define BAR() asm volatile("s_waitcnt lgkmcnt(0)\n\ts_barrier" ::: "memory")
#define DRAIN() asm volatile("s_waitcnt vmcnt(0)" ::: "memory")
#define RLX __ATOMIC_RELAXED, __HIP_MEMORY_SCOPE_AGENT
constexpr int SC1 = 16;

DI u32 opq(u32 x) { asm volatile("" : "+s"(x)); return x; }
DI u32 opqv(u32 x) { asm volatile("" : "+v"(x)); return x; }
DI v4u ldr(rsrc_t rs, u32 vo, u32 sbase, int ks) { return ld128<16>(rs, vo + (ks & 3) * 1024, sbase + blk((ks >> 2) * 4096)); }
template <int AUX> DI v4u ldf(rsrc_t rs, u32 vo, u32 sbase, u32 off) { return ld128<AUX>(rs, vo + (off & 4095u), sbase + (off & ~4095u)); }
template <int I> DI void dma1(rsrc_t rs, u32 vo, u32 sbase, LAS unsigned char* d) {
  __builtin_amdgcn_raw_ptr_buffer_load_lds(rs, (LAS u32*)(d + (I >> 2) * 4096), 16, vo, sbase + (I >> 2) * 4096, (I & 3) * 1024, SC1);
}
template <int I0, int I1> DI void dma_range(rsrc_t rs, u32 vo, u32 sbase, LAS unsigned char* d) {
  if constexpr (I0 < I1) {
    dma1<I0>(rs, vo, sbase, d);
    dma_range<I0 + 1, I1>(rs, vo, sbase, d);
  }
}
template <class F, int... I> DI void static_for_impl(F&& f, std::integer_sequence<int, I...>) { (f(std::integral_constant<int, I>{}), ...); }
template <int N, class F> DI void static_for(F&& f) { static_for_impl(f, std::make_integer_sequence<int, N>{}); }
DI u32 cnt_off(int idx) { return CTRL + idx * CSTR; }
DI u32 lane0_off() { return (threadIdx.x & 63) == 0 ? 0u : 0x80000000u; }
DI void add1(rsrc_t rs, u32 l0, u32 coff) { (void)__builtin_amdgcn_raw_ptr_buffer_atomic_add_i32(1, rs, l0, coff, 0); }
template <int N> DI void wsignal(rsrc_t rs, u32 l0, u32 coff) {
  SB();
  asm volatile("s_waitcnt vmcnt(%0)" ::"n"(N) : "memory");
  add1(rs, l0, coff);
  SB();
}
DI u32 peek(rsrc_t rs, u32 l0, u32 coff) { return __builtin_amdgcn_raw_ptr_buffer_atomic_add_i32(0, rs, l0, coff, 0); }
DI void wfinish(rsrc_t rs, u32 l0, u32 coff, u32 n, u32 pv) {
  if (__builtin_amdgcn_readfirstlane(pv) < n) {
#pragma unroll 1
    for (u32 spins = 0;; ++spins) {
      if (__builtin_amdgcn_readfirstlane(peek(rs, l0, coff)) >= n) break;
      if (spins > (1u << 16)) { __builtin_amdgcn_raw_buffer_store_b32(1u, rs, 0, cnt_off(C_TMO), SC1); break; }
    }
  }
  asm volatile("" ::: "memory");
}
DI void wwait(rsrc_t rs, u32 l0, u32 coff, u32 n) { wfinish(rs, l0, coff, n, peek(rs, l0, coff)); }
DI void apeek(rsrc_t rs, u32 l0, u32 coff, u32& p) {
  SB();
  asm volatile("v_mov_b32 %0, 0\n\tbuffer_atomic_add %0, %1, %2, %3 offen sc0 ; APEEK" : "=&v"(p) : "v"(l0), "s"(rs), "s"(coff) : "memory");
  SB();
}
#define PCHK(P, W) "s_waitcnt vmcnt(" W ")\n\tv_readfirstlane_b32 %3, " P "\n\ts_cmp_ge_u32 %3, %8\n\ts_cbranch_scc1 .Lpd%=\n\tv_mov_b32 " P ", 0\n\tbuffer_atomic_add " P ", %5, %6, %7 offen sc0\n\t"
template <int N0, int N1, int N2> DI void pfinish(rsrc_t rs, u32 l0, u32 coff, u32 n, u32 (&p)[3]) {
  u32 t, cnt;
  SB();
  asm volatile(PCHK("%0", "%9") PCHK("%1", "%10") PCHK("%2", "%11") "s_movk_i32 %4, 0x7fff\n.Lpl%=:\n\t" PCHK("%0", "2") PCHK("%1", "2") PCHK("%2", "2")
               "s_sub_u32 %4, %4, 1\n\ts_cbranch_scc0 .Lpl%=\n.Lpd%=:"
               : "+v"(p[0]), "+v"(p[1]), "+v"(p[2]), "=&s"(t), "=&s"(cnt)
               : "v"(l0), "s"(rs), "s"(coff), "n"(n), "n"(N0), "n"(N1 + 1), "n"(N2 + 2)
               : "memory", "scc");
  SB();
}
#define PCHK2(P, W) "s_waitcnt vmcnt(" W ")\n\tv_readfirstlane_b32 %2, " P "\n\ts_cmp_ge_u32 %2, %7\n\ts_cbranch_scc1 .Lqd%=\n\tv_mov_b32 " P ", 0\n\tbuffer_atomic_add " P ", %4, %5, %6 offen sc0\n\t"
template <int N0, int N1> DI void pfinish2(rsrc_t rs, u32 l0, u32 coff, u32 n, u32 (&p)[2]) {
  u32 t, cnt;
  SB();
  asm volatile(PCHK2("%0", "%8") PCHK2("%1", "%9") "s_movk_i32 %3, 0x7fff\n.Lql%=:\n\t" PCHK2("%0", "1") PCHK2("%1", "1")
               "s_sub_u32 %3, %3, 1\n\ts_cbranch_scc0 .Lql%=\n.Lqd%=:"
               : "+v"(p[0]), "+v"(p[1]), "=&s"(t), "=&s"(cnt)
               : "v"(l0), "s"(rs), "s"(coff), "n"(n), "n"(N0), "n"(N1 + 1)
               : "memory", "scc");
  SB();
}
DI void pretire(u32 (&p)[2]) { asm volatile("; PRETIRE %0 %1" ::"v"(p[0]), "v"(p[1])); }
DI void pretire(u32 (&p)[3]) { asm volatile("; PRETIRE %0 %1 %2" ::"v"(p[0]), "v"(p[1]), "v"(p[2])); }
#define NAP(n) do { SB(); __builtin_amdgcn_s_sleep(n); SB(); } while (0)
DI void poll3(rsrc_t rs, u32 vo, u32 need) {
#pragma unroll 1
  for (u32 spins = 0;; ++spins) {
    const u32 v = peek(rs, vo, 0);
    if ((__builtin_amdgcn_ballot_w64(v >= need) & 7ull) == 7ull) break;
    if (spins > (1u << 16)) { __builtin_amdgcn_raw_buffer_store_b32(1u, rs, 0, cnt_off(C_TMO), SC1); break; }
  }
  asm volatile("" ::: "memory");
}
template <int CTRL_> DI float dpp_add(float v) {
  return v + __builtin_bit_cast(float, __builtin_amdgcn_update_dpp(0, __builtin_bit_cast(int, v), CTRL_, 0xf, 0xf, true));
}

constexpr int SCR = 131072;
constexpr int PK_OFF = SCR, PK_STR = 144, VV_OFF = PK_OFF + 128 * PK_STR, VV_STR = 80, KF_OFF = VV_OFF + 128 * VV_STR;
constexpr int XS_OFF = 98304;
constexpr int RED_OFF = SCR;
constexpr int LDS_BYTES = 163840;
static_assert(KF_OFF + 4 * 544 <= LDS_BYTES, "lds");

DI void lds_put1(LAS unsigned char* p, const f32x16& a, int q) {
  *(LAS f32x4*)(p + q * 1024) = f32x4{a[4 * q], a[4 * q + 1], a[4 * q + 2], a[4 * q + 3]};
}

extern __shared__ __attribute__((aligned(16))) unsigned char smem[];

struct Sch { int put2[4], put3[4], bar1, rd[8], fu[8], put1[4], bar2, rd1, f1[4]; };
constexpr Sch sch_of(int KL) {
  return KL >= 14 ? Sch{{0, 1, 2, 3}, {4, 5, 6, 7}, 9, {9, 9, 9, 10, 10, 10, 11, 11}, {12, 13, 14, 19, 21, 22, 23, 24}, {15, 16, 17, 18}, 20, 20, {25, 26, 27, 27}}
                  : Sch{{0, 0, 1, 1}, {2, 2, 3, 3}, 5, {5, 5, 5, 5, 6, 6, 6, 6}, {7, 7, 8, 8, 11, 12, 12, 13}, {9, 9, 10, 10}, 11, 11, {14, 14, 15, 15}};
}
struct FrO {
  static constexpr int NS = 16, KL = 8;
  static constexpr bool own(int i) { return i >= 8; }
  static constexpr int ks(int i) { return i & 7; }
  static constexpr u32 off(int i) { return (i & 7) * 1024; }
  static constexpr int lstep(int q, int win) { return q - win; }
  static constexpr int first(int win) { return win; }
};
struct FrH {
  static constexpr int NS = 56, KL = 14;
  static constexpr int part(int i) { return i < 28 ? i / 14 : 2 + ((i / 7) & 1); }
  static constexpr bool own(int i) { return i < 28 ? ((i / 7) & 1) : i >= 42; }
  static constexpr int ks(int i) { return part(i) * 7 + i % 7; }
  static constexpr u32 off(int i) { return part(i) * 65536 + (i % 7) * 1024; }
  static constexpr int lstep(int q, int win) {
    const int g = T3CHK - 21;
    return q < 35 ? q - 14 : q < 42 ? (q - 14 < T3CHK ? T3CHK : q - 14) : q < 42 + g ? q - 21 : q - 14 - g;
  }
  static constexpr int first(int win) { return 14; }
};
template <class Fr> DI v4u ld_fr(rsrc_t rs, u32 l16, u32 a_own, u32 a_oth, int i) { return ldf<SC1>(rs, l16, Fr::own(i) ? a_own : a_oth, Fr::off(i)); }
template <class Fr, int WIN, int PRE, class Win, class Pre, class Mid>
DI void splitk_phase(rsrc_t rs, const v4u (&wf)[Fr::NS / 2][2], v4u (&af)[WIN], u32 a_own, u32 a_oth, LAS unsigned char* L, int w, u32 l16, float scale,
                     v4u (&res)[2], Win&& win, Pre&& pre_loop, Mid&& mid) {
  const f32x16 zero16 = {0.f, 0.f, 0.f, 0.f, 0.f, 0.f, 0.f, 0.f, 0.f, 0.f, 0.f, 0.f, 0.f, 0.f, 0.f, 0.f};
  constexpr int NS = Fr::NS, KP = Fr::KL, E0 = NS - KP, F0 = Fr::first(WIN);
  constexpr Sch sc = sch_of(KP);
  static_assert(WIN >= KP, "window");
  auto ld = [&](int i) { return ld_fr<Fr>(rs, l16, a_own, a_oth, i); };
#pragma unroll
  for (int i = PRE; i < F0; ++i) { win(i); af[i] = ld(i); SB(); }
  LAS unsigned char* r2 = L + opqv(RED_OFF + l16 + (w ^ 2) * 8192);
  LAS unsigned char* r3 = L + opqv(RED_OFF + l16 + (w ^ 3) * 8192 + 4096);
  LAS unsigned char* r0 = L + opqv(RED_OFF + l16 + w * 8192);
  LAS unsigned char* r1 = L + opqv(RED_OFF + l16 + (w ^ 1) * 8192);
  pre_loop();
  SB();
  f32x16 a0 = zero16, a1 = zero16, a2 = zero16, a3 = zero16;
  float u[16];
  static_for<E0>([&](auto I) {
    constexpr int i = I, ks = Fr::ks(i), e = i - (E0 - 4);
    const v4u bb = af[i % WIN];
    if constexpr (!Fr::own(i)) a2 = mfma32(wf[ks][0], bb, a2);
    else a1 = mfma32(wf[ks][1], bb, a1);
    SB();
    mid(I);
    static_for<NS - F0>([&](auto Q) { if constexpr (Fr::lstep(Q + F0, WIN) == i) { af[(Q + F0) % WIN] = ld(Q + F0); SB(); } });
    SB();
    if constexpr (!Fr::own(i)) a3 = mfma32(wf[ks][1], bb, a3);
    else a0 = mfma32(wf[ks][0], bb, a0);
    SB();
    if constexpr (e >= 0) {
      const u32 ra = res[e >> 1][2 * (e & 1)], rb = res[e >> 1][2 * (e & 1) + 1];
      u[4 * e] = bflo(ra); u[4 * e + 1] = bfhi(ra); u[4 * e + 2] = bflo(rb); u[4 * e + 3] = bfhi(rb);
#pragma unroll
      for (int m = 0; m < 4; ++m) PIN(u[4 * e + m]);
    }
    SB();
  });
  f32x4 p[8];
  auto acc4 = [&](int q, const f32x4& x) {
#pragma unroll
    for (int m = 0; m < 4; ++m) { const float xv = x[m]; u[4 * q + m] = __builtin_fmaf(xv, scale, u[4 * q + m]); PIN(u[4 * q + m]); }
  };
  static_for<2 * KP>([&](auto G) {
    constexpr int g = G, k = g % KP, ks = Fr::ks(E0 + k);
    if constexpr (g < KP) a1 = mfma32(wf[ks][1], af[(E0 + k) % WIN], a1);
    else a0 = mfma32(wf[ks][0], af[(E0 + k) % WIN], a0);
    SB();
    if constexpr (g == sc.bar1 || g == sc.bar2) BAR();
    static_for<8>([&](auto Q) { if constexpr (sc.rd[Q] == g) p[Q] = *(LAS f32x4*)(r0 + Q * 1024); });
    if constexpr (g == sc.rd1) static_for<4>([&](auto Q) { p[Q] = *(LAS f32x4*)(r1 + Q * 1024); });
    SB();
    static_for<4>([&](auto Q) { if constexpr (sc.put2[Q] == g) lds_put1(r2, a2, Q); });
    static_for<4>([&](auto Q) { if constexpr (sc.put3[Q] == g) lds_put1(r3, a3, Q); });
    static_for<4>([&](auto Q) { if constexpr (sc.put1[Q] == g) lds_put1(r0, a1, Q); });
    SB();
    static_for<8>([&](auto Q) { if constexpr (sc.fu[Q] == g) acc4(Q & 3, p[Q]); });
    static_for<4>([&](auto Q) { if constexpr (sc.f1[Q] == g) acc4(Q, p[Q]); });
    SB();
    mid(std::integral_constant<int, E0 + g>{});
    SB();
  });
#pragma unroll
  for (int q = 0; q < 4; ++q) {
    acc4(q, f32x4{a0[4 * q], a0[4 * q + 1], a0[4 * q + 2], a0[4 * q + 3]});
    res[q >> 1][2 * (q & 1)] = pk(u[4 * q], u[4 * q + 1]);
    res[q >> 1][2 * (q & 1) + 1] = pk(u[4 * q + 2], u[4 * q + 3]);
  }
}
DI void resid_store(rsrc_t rs, const v4u (&res)[2], u32 l16, u32 sto, u32 xoff, u32 soff) {
#pragma unroll
  for (int s = 0; s < 2; ++s) st128<SC1>(res[s], rs, l16 + s * 1024, xoff);
  SB();
  float s0 = 0.f, q0 = 0.f, s1 = 0.f, q1 = 0.f;
#pragma unroll
  for (int m = 0; m < 4; ++m) {
    s0 = dot2(res[0][m], ONES2, s0); q0 = dot2(res[0][m], res[0][m], q0);
    s1 = dot2(res[1][m], ONES2, s1); q1 = dot2(res[1][m], res[1][m], q1);
  }
  st64<SC1>(__builtin_bit_cast(v2u, f32x2{sum32(s0 + s1), sum32(q0 + q1)}), rs, sto, soff);
}

DI u32 cs_word(float part, u32 hf) {
  const float cs = sum32(part);
  const u32 H = pk(cs, cs);
  const float rem = cs - bflo(H);
  return hf ? pk(rem, rem) : H;
}
DI u32 mean_word(float nmean) { return pk(nmean, nmean - bflo(pk(nmean, nmean))); }
template <int I> DI f32x16 mean_fix(const v4u& cs4, u32 mw, f32x16 c) {
  v4u bm = {0u, 0u, 0u, 0u};
  bm[I] = mw;
  return mfma32(cs4, bm, c);
}
struct Stats { float t[4], s, q, nmean, rstd; u32 mw; };
template <int I> DI void stats_step(Stats& z, const f32x4 (&sp)[4]) {
  if constexpr (I < 3) {
#pragma unroll
    for (int m = 0; m < 4; ++m) { const float a = I ? z.t[m] : sp[0][m], c = sp[I + 1][m]; z.t[m] = a + c; PIN(z.t[m]); }
  } else if constexpr (I == 3) { z.s = mix16(z.t[0], z.t[2]); PIN(z.s); z.q = mix16(z.t[1], z.t[3]); PIN(z.q); }
  else if constexpr (I == 4) { z.nmean = sum32(z.s) * (-1.f / 512.f); PIN(z.nmean); }
  else if constexpr (I == 5) { z.q = sum32(z.q) * (1.f / 512.f); PIN(z.q); }
  else if constexpr (I == 6) { z.mw = mean_word(z.nmean); PIN(z.mw); }
  else if constexpr (I == 7) { z.rstd = __builtin_amdgcn_rsqf(fmaxf(z.q - z.nmean * z.nmean, 0.f) + 1e-5f); PIN(z.rstd); }
}

template <class Hook> DI void dual_softmax(const f32x16& p, float c, u32 (&o)[16], Hook&& hook) {
  float mx = p[0], mn = p[0];
#pragma unroll
  for (int i = 1; i < 16; ++i) { mx = fmaxf(mx, p[i]); mn = fminf(mn, p[i]); }
  float ap = -mx * c, an = mn * c;
  PIN(ap);
  PIN(an);
  SB();
  hook(std::integral_constant<int, 0>{});
  SB();
  f32x2 ep[8], en[8];
#pragma unroll
  for (int i = 0; i < 8; ++i) {
    const f32x2 pv = {p[2 * i], p[2 * i + 1]};
    ep[i] = pv * c + ap;
    en[i] = pv * (-c) + an;
    PIN(ep[i]);
    PIN(en[i]);
  }
  SB();
  hook(std::integral_constant<int, 1>{});
  SB();
  f32x2 sa, sc;
  static_for<8>([&](auto I) {
    constexpr int i = I;
    ep[i] = f32x2{__builtin_amdgcn_exp2f(ep[i].x), __builtin_amdgcn_exp2f(ep[i].y)};
    en[i] = f32x2{__builtin_amdgcn_exp2f(en[i].x), __builtin_amdgcn_exp2f(en[i].y)};
    PIN(ep[i]);
    PIN(en[i]);
    if constexpr (i == 2) { sa = ep[0] + ep[1]; sc = en[0] + en[1]; PIN(sa); PIN(sc); }
    if constexpr (i > 2) { sa += ep[i - 1]; sc += en[i - 1]; PIN(sa); PIN(sc); }
    SB();
    hook(std::integral_constant<int, 2 + i>{});
    SB();
  });
  sa += ep[7];
  sc += en[7];
  const float rp = __builtin_amdgcn_rcpf(sa.x + sa.y), rn = __builtin_amdgcn_rcpf(sc.x + sc.y);
#pragma unroll
  for (int i = 0; i < 8; ++i) { ep[i] *= rp; PIN(ep[i]); }
#pragma unroll
  for (int i = 0; i < 8; ++i) { en[i] *= rn; PIN(en[i]); }
  SB();
  hook(std::integral_constant<int, 10>{});
  SB();
#pragma unroll
  for (int i = 0; i < 8; ++i) { o[i] = pk(ep[i].x, ep[i].y); o[8 + i] = pk(en[i].x, en[i].y); }
}
constexpr int p3_g0(int v) { return (v * 58) / 48; }
constexpr bool p3_w2gap(int gi) { return (gi * W2B) / 8 != ((gi + 1) * W2B) / 8; }
constexpr int p3_pre(int layer, int g) {
  int n = layer == 0 && p3_w2gap(g);
  for (int q = 0; q < 6; ++q) n += p3_g0(8 * q + 7) + 5 == g;
  return n;
}
constexpr int p3_post(int g) { return (g == HS0) + (g == HS1) + (g == PK0); }
constexpr int p3_younger(int layer, int t, int gw) {
  const int gs = p3_g0(16 * t + 15) + 5;
  int n = p3_post(gs);
  for (int g = gs + 1; g < gw; ++g) n += p3_pre(layer, g) + p3_post(g);
  return n + (gw < 64 ? p3_pre(layer, gw) : 0);
}
static_assert(p3_g0(32) >= 35 && p3_g0(47) + 5 < 64 && p3_g0(15) + 5 < HS0 && p3_g0(31) + 5 < HS1 && HS0 != HS1, "P3 schedule");

__global__ void __launch_bounds__(256, 1)
k_main(const uint16_t* __restrict__ x, const uint16_t* __restrict__ wqkv, const uint16_t* __restrict__ wphi,
       const uint16_t* __restrict__ wo_, const uint16_t* __restrict__ w1, const uint16_t* __restrict__ w2,
       unsigned char* ws, unsigned char* out) {
  u32 cpf;
  {
    unsigned long long pc;
    u32 o = threadIdx.x * 128 + (blockIdx.x & 8) * 4096;
    asm volatile("s_getpc_b64 %1\n.Lkcode0:\n\tv_min_u32 %2, .Lkcode1-.Lkcode0, %2\n\tglobal_load_dword %0, %2, %1 ; APEEK" : "=v"(cpf), "=&s"(pc), "+v"(o)::"memory");
  }
  SB();
  unsigned long long px = (unsigned long long)x, pq = (unsigned long long)wqkv, po = (unsigned long long)wo_, p1 = (unsigned long long)w1, p2 = (unsigned long long)w2;
  asm volatile("" : "+s"(px));
  asm volatile("" : "+s"(ws));
  asm volatile("" : "+s"(out));
  asm volatile("" : "+s"(wphi));
  LAS unsigned char* L = (LAS unsigned char*)smem;
  const int b = blockIdx.x, tid = threadIdx.x, lane = tid & 63, w = __builtin_amdgcn_readfirstlane(tid >> 6);
  const int xcd = b & 7, j = b >> 3;
  const int rt = 4 * (xcd >> 1) + (j >> 3), hp = 8 * (xcd & 1) + (j & 7), tt = 4 * rt + w;
  const bool p3on = (j & 7) < 7;
  const int fb = p3on ? 7 * (xcd & 1) + (j & 7) : 0;
  const int rt2 = 4 * xcd + (j >> 3), cb = j & 7;
  const int tte = 2 * rt2 + (w >> 1), n0 = 64 * cb + 32 * (w & 1);
  const rsrc_t rs = mkrsrc((unsigned char*)(((size_t)ws + (size_t)WSB * MB2 + MB2 - 1) & ~(size_t)(MB2 - 1)), WS_END);
  const f32x16 zero16 = {0.f, 0.f, 0.f, 0.f, 0.f, 0.f, 0.f, 0.f, 0.f, 0.f, 0.f, 0.f, 0.f, 0.f, 0.f, 0.f};
  unsigned long long dl0;
  STAMP(63);
  v4u res[2];
  v4u csq = {0u, 0u, 0u, 0u}, cs1;
  v4u wph[2];
  u32 tag_lo, tag_hi, tws = 0;
  u32 sd;
  const u32 l0 = lane0_off();
  const u32 nlh = !p3on ? 0u : (fb == 3 || fb == 10) ? 2u : 1u;
  v4u xr[32];
  const rsrc_t rsx = mkrsrc((const void*)px, S * D * 2);
  auto trfrag = [&](LAS unsigned char* p, int str) { const v2u t0 = trread(p), t1 = trread(p + str); return v4u{t0.x, t0.y, t1.x, t1.y}; };
  const u32 sv = ((lane >> 2) & 1) == ((lane >> 4) & 1) ? ONES2 : 0u;
  v4u selm = {sv, sv, sv, sv};
  const u32 u2 = opq(2 * b + (w & 1));
  const bool k0 = w == 0, k1 = b < 112, k3 = b >= 144, kon = k0 ? b < 192 : w >= 2;
  auto pick = [&](auto a0, auto a1, auto a2, auto a3) {
    asm volatile("" : "+s"(a0));
    asm volatile("" : "+s"(a1));
    asm volatile("" : "+s"(a2));
    asm volatile("" : "+s"(a3));
    auto r = k3 ? a3 : a2;
    r = k1 ? a1 : r;
    r = k0 ? a0 : r;
    asm volatile("" : "+s"(r));
    return r;
  };
  const u32 n3 = opq(opq(u2 - 288) * 2341) >> 16;
  const u32 ub = opq(k0 ? b : k1 ? u2 : u2 - 224), q3 = opq(u2 - 288 - 28 * n3);
  const u32 nb = opq(!k0 && k3 ? n3 : ub >> 3), kq = opq(!k0 && k3 ? q3 : ub & 7), ld2 = opq(k0 ? 3072u : k1 ? 3584u : 1024u);
  LAS unsigned char* img = L + SCR + w * 8192;
  {
    const rsrc_t rsu = mkrsrc((const void*)pick(pq, p1, po, p2), opq(kon ? (k0 || k1 || !k3 ? 512u : 1792u) : 0u) * ld2);
    const u32 vu = (lane >> 3) * ld2 + (((lane & 7) ^ ((lane >> 2) & 4)) * 16);
#pragma unroll
    for (int i = 0; i < 8; ++i) __builtin_amdgcn_raw_ptr_buffer_load_lds(rsu, (LAS u32*)(img + i * 1024), 16, vu, (64 * kq + 8 * i) * ld2 + nb * 128, 0, 0);
  }
  SB();
  if (b >= 248 && w < 2) {
#pragma unroll 1
    for (u32 i = 0; i < NCNT * CSTR / 16; i += 1024) st128<SC1>(v4u{0u, 0u, 0u, 0u}, rs, lane * 16, CTRL + ((b - 248) * 2 + w) * (NCNT * CSTR / 16) + i);
  }
  const v2u ep = ld64<SC1>(rs, 0, EPOCH_OFF);
  SB();
  const u32 vx = (lane >> 3) * 1024 + (lane & 7) * 16;
  const u32 rot = ROT ? j & 7 : 0;
  auto xld = [&](auto I) { constexpr int i = I; xr[i] = ld128<0>(rsx, vx, tt * 32768 + (i & 3) * 8192 + (((i >> 2) ^ rot) * 128)); SB(); };
  static_for<XA>([&](auto I) { xld(I); });
  u32 obase, cstr, kstr, kstr2, csb, ovo, cvo, ua;
  {
    const u32 t32 = 2 * nb, tile = (0x12u >> ((nb >> 3) * 2)) & 3;
    obase = opq(pick(WQKV_T + (t32 & 15) * 98304 + tile * 32768 + kq * 4096, W1_T + t32 * 32768 + kq * 4096, WO_T + (nb * 32 + kq * 4) * 2048,
                     W2_T + (nb * 112 + (kq & 1) * 56 + (kq >> 1) * 2) * 2048));
    cstr = opq(pick(98304u, 32768u, 1024u, 1024u));
    kstr = opq(pick(1024u, 1024u, 2048u, 2048u));
    kstr2 = opq(pick(2048u, 2048u, 4096u, 57344u));
    csb = opq(pick(CS_QKV + (((t32 & 15) * 3 + tile) * 8 + kq) * 128, CS_W1 + (t32 * 8 + kq) * 128, 0u, 0u));
    ovo = opqv(lane * 16 | (kon ? 0u : 0x80000000u));
    cvo = opqv(((lane >> 5) * (k0 ? 3072 : 1024) + (lane & 31) * 4) | (kon && (k0 || k1) ? 0u : 0x80000000u));
    const u32 c16 = lane & 15, Q = ((lane >> 2) & 4) + (lane & 3);
    const u32 Cc = ((k0 ? 0x73625140u : 0x75643120u) >> (4 * Q)) & 7;
    ua = opqv((8 * (lane >> 5) + (c16 >> 2)) * 128 + (c16 >> 3) * 64 + Cc * 8);
  }
  const u32 jadd = opq(cnt_off(C_J + pick(0u, 16 + (nb >> 1), nb, nb)));
  auto unit = [&]() {
    v4u o[8];
#pragma unroll
    for (int i = 0; i < 8; ++i) o[i] = trfrag(img + (ua ^ ((i >> 2) * 64)) + (i & 3) * 2048, 512);
    SB();
    f32x4 cs[2] = {{0.f, 0.f, 0.f, 0.f}, {0.f, 0.f, 0.f, 0.f}};
#pragma unroll
    for (int i = 0; i < 8; ++i) {
      st128<SC1>(o[i], rs, ovo, obase + (i >> 2) * cstr + ((i >> 1) & 1) * kstr2 + (i & 1) * kstr);
      cs[i >> 2] = mfma16(selm, o[i], cs[i >> 2]);
    }
    const float c0 = cs[0][0], c1 = cs[1][0];
    __builtin_amdgcn_raw_buffer_store_b32(__builtin_bit_cast(u32, lane < 32 ? c0 : c1), rs, cvo, csb, SC1);
    SB();
  };
  SB();
  const u32 tvo = opqv((((b >= 248 && w < 2) || (k0 && kon)) && lane < 16) ? lane * 8 : 0x80000000u);
  const u32 tso = opq(b >= 248 ? INIT_OFF + (((b & 7) * 2 + w) & 15) * CSTR : FLAG_OFF + b * CSTR);
  SB();
  v4u w2f[28][2];

  static_for<2>([&](auto LAYER) __attribute__((always_inline)) {
    constexpr int layer = LAYER;
    constexpr int sb = layer * 29;
    v4u wo[8][2];
    const u32 wb = opq(W2_T + cb * 229376 + w * 14336);
    const u32 wsw = (w & 1) * 1024;
    u32 pvx[3], pvy[2];
    const u32 rx = ROT2 ? opq(rot * 4096) : 0u;
#define IC(n) std::integral_constant<int, n>{}
    {
      const u32 ln = opqv(lane), l16 = ln * 16, r = ln & 31, hf = ln >> 5;
      if constexpr (layer) STAMP(sb + 0);
      constexpr int DX = DXV;
      v4u xf[32];
      auto xtile = [&](int i, u32 t) { return XT + i * MB2 + (t & 31) * 65536 + (sd ^ ((i & 1) ? 0 : 8192)); };
      auto stile = [&](int i, u32 t) { return ST + i * ST_SZ + blk((t & 31) * 4096) + sd; };
      const u32 xb = layer ? xtile(1, tt) : 0;
      if constexpr (layer > 0 && XMODE == 1) apeek(rs, l0, (cnt_off(C_X + (tt & 31)) + sd), pvx[0]);
      SB();
      f32x4 sp[4];
      const u32 stb = layer ? stile(1, tt) : 0;
      const u32 wqb = opq(WQKV_T + hp * 98304 + 65536 + w * 8192);
      const u32 g4 = ln >> 4, c16 = ln & 15;
      LAS unsigned char* a_v = L + opqv(VV_OFF + (32 * w + r) * VV_STR + hf * 32);
      LAS unsigned char* a_pk = L + opqv(PK_OFF + (32 * w + r) * PK_STR + hf * 64);
      LAS unsigned char* a_pa = L + opqv(PK_OFF + (8 * g4 + (c16 >> 2)) * PK_STR + w * 32 + (ln & 3) * 8);
      LAS unsigned char* a_pb = L + opqv(VV_OFF + (8 * g4 + (c16 >> 2)) * VV_STR + (w >> 1) * 32 + (ln & 3) * 8);
      const u32 o_kv = opqv((g4 >> 1) * 256 + c16 * 16 + (g4 & 1) * 8), o_z = opqv(hf * 8192 + r * 4);
      u32 jv = 0, lat = 0, vv = 0, vb = 0;
      constexpr int wok = layer ? WOK : 0, NPF = 16 - wok + (layer ? 24 : 32);
      constexpr int s_q1 = 0, s_q2 = s_q1 + NQ1, s_od = s_q2 + NQ2, s_op = s_od + ODR, n_op = layer ? OPL1 : OPL0, s_p2 = s_op + n_op, s_xd = NPF - XDR;
      static_assert(s_p2 <= s_xd && s_xd - s_p2 <= 12 && s_od <= NPF - 8 && s_op >= 16 - wok, "prefetch schedule");
      LAS v4u* WL = (LAS v4u*)(L + l16);
      LAS v4u* WQ = (LAS v4u*)(L + opqv(l16 + 65536));
      const u32 wob = opq(WO_T + (cb * 4 + w) * 16384);
      auto wold = [&](auto I) {
        constexpr int i = I;
        wo[i >> 1][i & 1] = ld128<SC1>(rs, l16 + ((i >> 1) & 1) * 2048, wob + (i >> 2) * 4096 + ((i & 1) ? 1024 - wsw : wsw));
      };
      f32x16 aK = zero16, aV = zero16, aQ;
      float rstd, c;
      Stats zs_;
      const u32 jvo = opqv(ln == 0 ? cnt_off(C_J + cb) : ln == 1 ? cnt_off(C_J + 16 + fb) : 0x80000000u);
      float cp[3][4];
      v4u wk[3], wv[3];
      SB();
      if constexpr (layer == 0) {
        LAS unsigned char* sw[2];
        LAS unsigned char* sr[4];
#pragma unroll
        for (int i = 0; i < 2; ++i) sw[i] = L + opqv(XS_OFF + w * 8192 + (ln >> 3) * 128 + (((ln & 7) ^ (4 * i + (ln >> 4))) * 16));
#pragma unroll
        for (int f = 0; f < 4; ++f) sr[f] = L + opqv(XS_OFF + w * 8192 + r * 128 + (((2 * f + hf) ^ ((r >> 1) & 7)) * 16));
        const u32 fu = ln >> 1, fvo = opqv(ln >= 12 ? 0x80000000u : FLAG_OFF + ((fu < 4 ? (1 + (w >> 1)) * 64 + (w & 1) * 4 + fu : 2 * w + fu - 4) + (hp >> 1) * 8) * CSTR + (ln & 1) * 4);
        PIN(selm);
        SB();
        if (w == 0) {
          asm volatile("s_waitcnt vmcnt(%0)" ::"n"(XA + 1) : "memory");
          STAMP(58);
          unit();
          STAMP(59);
        }
        asm volatile("s_barrier" ::: "memory");
        {
          const unsigned long long e = ((unsigned long long)__builtin_amdgcn_readfirstlane(ep.y) << 32) | __builtin_amdgcn_readfirstlane(ep.x);
          unsigned long long t = e * 0x9E3779B97F4A7C15ull + 0x632BE59BD9B4E019ull;
          if (t == e) t = ~e;
          tag_lo = (u32)t;
          tag_hi = (u32)(t >> 32);
        }
        const u32 fneed = (ln & 1) ? tag_hi : tag_lo;
        f32x4 sa = {0.f, 0.f, 0.f, 0.f};
        f32x16 gr = zero16;
        u32 fv;
        v2u tg;
        SB();
        auto vm = [&](auto N) {
          constexpr int n = N, xi = XA + n, di = n - 8 * FCK;
          if constexpr (xi < 32) xld(IC(xi));
          if constexpr (xi == 32) __builtin_amdgcn_raw_ptr_buffer_load_lds(mkrsrc(wphi, 16384), (LAS u32*)(L + XS_OFF + w * 8192 + 4096), 16, l16, hp * 1024, 0, 0);
          if constexpr (xi == 33) tws = ld32<0>(rs, TLBW ? (ln + 64 * w + (TLBD ? 256 * j : 0)) * TLBW : 0x80000000u, 0);
          if constexpr (xi == XB) {
            if (w == 0) asm volatile("s_waitcnt vmcnt(%0)" ::"n"(XB - XA + 1) : "memory");
            st64<SC1>(v2u{tag_lo, tag_hi}, rs, tvo, tso);
            STAMP(60);
          }
          if constexpr (di >= 0 && di < 16)
            dma1<(di & 3)>(rs, l16, WQKV_T + hp * 98304 + w * 16384 + (di >> 2) * 4096, L + (w >> 1) * 32768 + ((((w & 1) * 4 + (di >> 2)) ^ rot) * 4096));
          if constexpr (di == 16) tg = ld64<SC1>(rs, c16 * CSTR, INIT_OFF);
          SB();
        };
        static_assert(XA + 8 * FCK > 33 && XB < 32 && FCK <= 7 && UB >= FCK + 3 && XA >= 4, "start schedule");
        static_for<11>([&](auto B) {
          constexpr int blk = B;
          if constexpr (blk == UB) { if (w >= 2) unit(); }
          if constexpr (blk == FPK) { fv = peek(rs, fvo, 0); SB(); }
          if constexpr (blk == FCK) {
            STAMP(0);
            if ((__builtin_amdgcn_ballot_w64(fv == fneed) & 0xfffull) != 0xfffull) {
#pragma unroll 1
              for (u32 spins = 0; spins < (1u << 16); ++spins)
                if ((__builtin_amdgcn_ballot_w64(peek(rs, fvo, 0) == fneed) & 0xfffull) == 0xfffull) break;
            }
            asm volatile("" ::: "memory");
            STAMP(61);
          }
          if constexpr (blk < 10) static_for<4>([&](auto I) {
            constexpr int i = I;
            if constexpr (blk > 1) gr = mfma32(xf[4 * blk - 8 + i], xf[4 * blk - 8 + i], gr);
            SB();
            vm(IC(8 * blk + 2 * i));
            if constexpr (blk < 8) *(LAS v4u*)(sw[i & 1] + i * 1024) = xr[4 * blk + i];
            SB();
            if constexpr (blk > 1) { sa = mfma16(selm, xf[4 * blk - 8 + i], sa); asm volatile("" : "+a"(sa)); }
            SB();
            vm(IC(8 * blk + 2 * i + 1));
          });
          if constexpr (blk < 8) static_for<4>([&](auto F) { xf[4 * blk + F] = *(LAS v4u*)(sr[F]); SB(); });
        });
        asm volatile("" ::"v"(tws));
        asm volatile("; PRETIRE %0" ::"v"(cpf));
        {
          const u32 Qc = 4 * (g4 & 1) + (c16 & 3);
          LAS unsigned char* a_ph = L + opqv(XS_OFF + w * 8192 + 4096 + hf * 512 + (c16 >> 2) * 32 + (Qc >> 1) * 8);
#pragma unroll
          for (int s = 0; s < 2; ++s) wph[s] = trfrag(a_ph + s * 256, 128);
        }
        const float sa0 = sa[0];
        const float nmean = sa0 * (-1.f / 512.f);
        zs_.mw = mean_word(nmean);
        auto sel = [](bool cnd, float a0, float a1) { PIN(a0); PIN(a1); return cnd ? a1 : a0; };
        auto g4f = [&](int i) { return sel(r & 2, sel(r & 1, gr[4 * i], gr[4 * i + 1]), sel(r & 1, gr[4 * i + 2], gr[4 * i + 3])); };
        const float gd = sel(r & 16, sel(r & 8, g4f(0), g4f(1)), sel(r & 8, g4f(2), g4f(3)));
        const bool dl = ((r >> 2) & 1) == hf;
        const float dg = dl ? gd : 0.f;
        rstd = __builtin_amdgcn_rsqf(fmaxf(sum32(dg) * (1.f / 512.f) - nmean * nmean, 0.f) + 1e-5f);
        c = rstd * LOG2E;
#pragma unroll
        for (int s = 0; s < 2; ++s)
#pragma unroll
          for (int m = 0; m < 4; ++m) wph[s][m] = dl ? wph[s][m] : 0u;
        PIN(wph[0]);
        PIN(wph[1]);
        PIN(c);
        PIN(rstd);
        PIN(zs_.mw);
        SB();
        if constexpr (UB > FCK) { if (w >= 2) asm volatile("s_waitcnt vmcnt(9)" ::: "memory"); else DRAIN(); }
        else DRAIN();
        if (w == 0 && !__all(tg.x == tag_lo && tg.y == tag_hi)) {
#pragma unroll 1
          for (u32 spins = 0; spins < (1u << 16); ++spins) {
            asm volatile("" ::: "memory");
            const v2u t = ld64<SC1>(rs, c16 * CSTR, INIT_OFF);
            if (__all(t.x == tag_lo && t.y == tag_hi)) break;
          }
        }
        BAR();
#pragma unroll
        for (int i = 0; i < 2; ++i) { wk[i] = WL[i * 64]; wv[i] = WL[(32 + i) * 64]; }
        if constexpr (UB <= FCK) add1(rs, w >= 2 ? l0 : 0x80000000u, jadd);
        SB();
      } else {
        if constexpr (XMODE == 0) { NAP(PS3); pvx[0] = peek(rs, l0, (cnt_off(C_X + (tt & 31)) + sd)); SB(); }
        if constexpr (XMODE == 3) { NAP(PS3); apeek(rs, l0, (cnt_off(C_X + (tt & 31)) + sd), pvy[0]); NAP(PS5); apeek(rs, l0, (cnt_off(C_X + (tt & 31)) + sd), pvy[1]); }
        if constexpr (XMODE == 1) { NAP(PS1); apeek(rs, l0, (cnt_off(C_X + (tt & 31)) + sd), pvx[1]); NAP(PS2); apeek(rs, l0, (cnt_off(C_X + (tt & 31)) + sd), pvx[2]); SB(); }
        asm volatile("s_waitcnt vmcnt(%0)" ::"n"(XMODE == 1 ? 4 : XMODE == 3 ? 3 : XMODE == 0 ? 2 : 1) : "memory");
        BAR();
#pragma unroll
        for (int i = 0; i < 2; ++i) { wk[i] = WL[i * 64]; wv[i] = WL[(32 + i) * 64]; }
        SB();
        if constexpr (XMODE == 1) pfinish<2, 1, 0>(rs, l0, (cnt_off(C_X + (tt & 31)) + sd), 16, pvx);
        else if constexpr (XMODE == 3) pfinish2<1, 0>(rs, l0, (cnt_off(C_X + (tt & 31)) + sd), 16, pvy);
        else if constexpr (XMODE == 0) wfinish(rs, l0, (cnt_off(C_X + (tt & 31)) + sd), 16, pvx[0]);
        else { NAP(PS3); wwait(rs, l0, (cnt_off(C_X + (tt & 31)) + sd), 16); }
        STAMP(sb + 1);
#pragma unroll
        for (int ks = 0; ks < DX; ++ks) { xf[ks] = ldr(rs, l16, xb, ks); SB(); }
      }
      STAMP(sb + 26);
      static_for<32>([&](auto KSI) {
        constexpr int ks = KSI, q0 = layer ? 20 : 8;
        aK = mfma32(wk[ks % 3], xf[ks], aK);
        SB();
        if constexpr (ks + 2 < 32) wk[(ks + 2) % 3] = WL[(ks + 2) * 64];
        SB();
        if constexpr (layer > 0 && ks + DX < 32) xf[ks + DX] = ldr(rs, l16, xb, ks + DX);
        if constexpr (ks >= q0 && ks < q0 + 8) dma1<((ks - q0) & 3)>(rs, l16, wqb + ((ks - q0) >> 2) * 4096, L + 65536 + (((2 * w + ((ks - q0) >> 2)) ^ (layer ? 0 : rot)) * 4096));
        if constexpr (ks >= 28 && ks < 28 + wok) wold(IC(ks - 28));
        if constexpr (layer == 0 && ks < 6) cp[ks >> 1][2 * (ks & 1)] = __builtin_bit_cast(float, ld32<SC1>(rs, ln * 4 + (ks & 1) * 512 + (ks >> 1) * 1024, CS_QKV + hp * 3072));
        SB();
        aV = mfma32(wv[ks % 3], xf[ks], aV);
        SB();
        if constexpr (ks + 2 < 32) wv[(ks + 2) % 3] = WL[(32 + ks + 2) * 64];
        SB();
        if constexpr (layer == 0) {
          if constexpr (ks < 6) cp[ks >> 1][2 * (ks & 1) + 1] = __builtin_bit_cast(float, ld32<SC1>(rs, ln * 4 + (ks & 1) * 512 + 256 + (ks >> 1) * 1024, CS_QKV + hp * 3072));
          if constexpr (ks == 18 || ks == 22) res[(ks - 18) >> 2] = ld128<0>(rsx, r * 1024 + hf * 16 + ((ks - 18) >> 2) * 32, tte * 32768 + n0 * 2);
          if constexpr (ks == 19 || ks == 21 || ks == 23) { constexpr int t = (ks - 19) / 2; cp[t][0] = (cp[t][0] + cp[t][1]) + (cp[t][2] + cp[t][3]); PIN(cp[t][0]); }
          if constexpr (ks == 20 || ks == 22 || ks == 24) { constexpr int t = (ks - 20) / 2; u32 cw = cs_word(cp[t][0], hf); PIN(cw); csq[t] = cw; }
          if constexpr (ks == 19) add1(rs, w >= 2 ? l0 : 0x80000000u, jadd);
        } else {
          if constexpr (ks < 4) sp[ks] = __builtin_bit_cast(f32x4, ld128<SC1>(rs, l16 + ks * 1024, stb));
          if constexpr (ks == 1) pretire(pvy);
          if constexpr (ks >= 16 && ks < 24) stats_step<ks - 16>(zs_, sp);
          if constexpr (ks == 24) { rstd = zs_.rstd; c = rstd * LOG2E; PIN(c); }
        }
        SB();
        if constexpr (ks == 25) aK = mean_fix<0>(csq, zs_.mw, aK);
        if constexpr (ks == 27) aV = mean_fix<1>(csq, zs_.mw, aV);
        SB();
      });
      STAMP(sb + 2);
      const u32 w1b = opq(W1_T + fb * 131072);
      auto w1dma = [&](auto I) {
        constexpr int i = I;
        const u32 o = i < 16 ? w * 16384 + (i >> 2) * 4096 : (i < 24 ? 98304 : 65536) + w * 8192 + ((i >> 2) & 1) * 4096;
        dma1<(i & 3)>(rs, l16, w1b + o, L + (o ^ rx));
      };
      auto pf = [&](auto N) {
        constexpr int n = N + wok;
        if constexpr (n < 16) wold(IC(n));
        else w1dma(IC(n - 16 + (layer && n >= 32 ? 8 : 0)));
        SB();
      };
      {
        const v4u k0 = pack8(aK, 0), k1 = pack8(aK, 1);
        f32x16 p = mfma32(wph[0], k0, zero16);
        p = mfma32(wph[1], k1, p);
        SB();
        if constexpr (layer == 0 && PRB == 0) { vv = peek(rs, l0, cnt_off(C_H + (b >> 5) * 128 + (w & 1) * 64 + (b & 31) * 2 + (w >> 1))); SB(); }
#pragma unroll
        for (int i = 0; i < 16; ++i) { aV[i] *= rstd; PIN(aV[i]); }
        SB();
        *(LAS v4u*)(a_v) = pack8(aV, 0);
        *(LAS v4u*)(a_v + 16) = pack8(aV, 1);
        SB();
        if constexpr (layer == 0 && PRB == 1) { vv = peek(rs, l0, cnt_off(C_H + (b >> 5) * 128 + (w & 1) * 64 + (b & 31) * 2 + (w >> 1))); SB(); }
        u32 o[16];
        auto look = [&](auto I) {
          if constexpr (layer == 0) {
            u32 t;
            if constexpr (I >= LK0) asm volatile("s_getreg_b32 %1, hwreg(HW_REG_IB_STS, 0, 4)\n\ts_cmp_gt_u32 %1, %2\n\ts_addc_u32 %0, %0, 0" : "+s"(lat), "=&s"(t) : "n"(I > JPK ? 1 : 0) : "scc");
            if constexpr (I == JPK) jv = peek(rs, w != 0 ? 0x80000000u : jvo, 0);
          }
        };
        dual_softmax(p, c, o, look);
        static_for<4>([&](auto I) {
          constexpr int i = I;
          *(LAS v4u*)(a_pk + i * 16) = v4u{o[4 * i], o[4 * i + 1], o[4 * i + 2], o[4 * i + 3]};
          if constexpr (layer == 0 && (i & 1)) { SB(); look(IC(11)); SB(); }
        });
        if constexpr (layer == 0) *(LAS u32*)(L + XS_OFF + w * 4) = lat;
      }
      SB();
      aQ = mean_fix<2>(csq, zs_.mw, zero16);
      STAMP(sb + 3);
      asm volatile("s_waitcnt vmcnt(%0)" ::"n"(wok) : "memory");
      if constexpr (layer == 0) {
        asm volatile("" ::"v"(vv));
        const u32 need = ln == 0 ? 36u : ln == 1 ? 16u : 0u;
        if (w == 0 && (__builtin_amdgcn_ballot_w64(jv >= need) & 7ull) != 7ull) poll3(rs, jvo, need);
      }
      BAR();
      v4u lt;
      if constexpr (layer == 0) { lt = *(LAS v4u*)(L + XS_OFF); sd = 0; }
      const u32 pbase = KVP + (layer * 64 + 4 * hp + w) * 16384, zbase = ZP + (layer * 16 + hp) * 16384 + w * 2048;
      v4u wq[4];
      auto qfrag = [&](int ks) { return WQ[ks * 64]; };
      {
        f32x4 kv = {0.f, 0.f, 0.f, 0.f}, zz = {0.f, 0.f, 0.f, 0.f};
        const v4u ones = {ONES2, ONES2, ONES2, ONES2};
        v2u a0[4], a1[4], b0[4], b1[4];
#pragma unroll
        for (int ks = 0; ks < 4; ++ks) {
          a0[ks] = trread(a_pa + (32 * ks) * PK_STR); a1[ks] = trread(a_pa + (32 * ks + 4) * PK_STR);
          b0[ks] = trread(a_pb + (32 * ks) * VV_STR); b1[ks] = trread(a_pb + (32 * ks + 4) * VV_STR);
          SB();
        }
#pragma unroll
        for (int ks = 0; ks < 4; ++ks) {
          const v4u A = {a0[ks].x, a0[ks].y, a1[ks].x, a1[ks].y}, B = {b0[ks].x, b0[ks].y, b1[ks].x, b1[ks].y};
          kv = mfma16(A, B, kv);
          zz = mfma16(ones, A, zz);
          asm volatile("" : "+a"(zz));
          SB();
        }
#pragma unroll
        for (int ks = 0; ks < 4; ++ks) wq[ks] = qfrag(ks);
        SB();
        const v2u kvb = {pk(kv[0], kv[1]), pk(kv[2], kv[3])};
        st64<SC1>(kvb, rs, o_kv, pbase + rt * 512 + sd);
        st64<SC1>(kvb, rs, o_kv, pbase + rt * 512 + (sd ^ 8192));
        const float z0 = zz[0];
        __builtin_amdgcn_raw_buffer_store_b32(__builtin_bit_cast(u32, z0), rs, o_z, zbase + rt * 128, SC1);
      }
      SB();
      STAMP(sb + 4);
      auto qstep = [&](auto I) {
        constexpr int i = I;
        aQ = mfma32(wq[i & 3], xf[i], aQ);
        SB();
        if constexpr (i + 4 < 32) wq[i & 3] = qfrag(i + 4);
      };
      LAS unsigned char *a_kz, *a_kw, *a_kr;
      u32 o_st, o_zl, l2k;
      v4u sel;
      if constexpr (layer == 0) {
        const int df = __builtin_amdgcn_readfirstlane((int)(lt[0] + lt[2]) - (int)(lt[1] + lt[3]));
        sd = df > 0 ? 8192 : 0;
        const u32 vote = df == 0 ? 1 : (((df > 0) ^ (xcd >> 2)) & 1) * 2;
        (void)__builtin_amdgcn_raw_ptr_buffer_atomic_add_i32(1 + (vote << 16), rs, (w == 0 && j < 4 && ln < 16) ? ln * CSTR : 0x80000000u, cnt_off(C_V), 0);
        SB();
      }
      const u32 skv = cnt_off(C_KV + layer * 128 + hp * 4 + w), ckv = skv + sd;
      u32 pvk[2];
      static_for<32>([&](auto I) {
        constexpr int i2 = I - QS, n2 = 32 - QS;
        if constexpr (I == QS) { wsignal<NQ1 + (layer ? 0 : 1)>(rs, l2k, skv); STAMP(sb + 5); }
        qstep(I);
        if constexpr (I < QS) { if constexpr ((I * NQ1 + QS - 1) / QS != ((I + 1) * NQ1 + QS - 1) / QS) pf(IC(s_q1 + (I * NQ1 + QS - 1) / QS)); }
        else {
          if constexpr (layer == 0 && i2 == VPK) { vv = peek(rs, w == 0 ? l0 : 0x80000000u, cnt_off(C_V + (b & 15))); SB(); }
          if constexpr ((i2 == KVPK1 || i2 == KVPK2) && KMODE == 2) apeek(rs, l0, ckv, pvk[i2 == KVPK2]);
          if constexpr ((i2 * NQ2 + n2 - 1) / n2 != ((i2 + 1) * NQ2 + n2 - 1) / n2) pf(IC(s_q2 + (i2 * NQ2 + n2 - 1) / n2));
        }
        const u32 l2 = opqv(l16) >> 4, g2 = l2 >> 4, c2 = l2 & 15;
        if constexpr (I == 2) l2k = opqv(l2 < 2 ? l2 * 8192 : 0x80000000u);
        if constexpr (I == 1) o_zl = opqv(g2 * 128 + c2 * 4);
        if constexpr (I == 3) a_kz = L + opqv(KF_OFF + w * 544 + (c2 >> 3) * 272 + 256 + (c2 & 7) * 2);
        if constexpr (I == 5) a_kw = L + opqv(KF_OFF + w * 544 + (g2 >> 1) * 272 + (g2 & 1) * 8 + rho32(c2 & 7, c2 >> 3) * 16);
        if constexpr (I == 7) a_kr = L + opqv(KF_OFF + (l2 >> 5) * 272 + ((l2 & 31) < 16 ? (l2 & 31) : 16) * 16);
        if constexpr (I == 9) o_st = opqv(l2 >= 32 ? 0x80000000u : c2 * 16 + g2 * 8);
        if constexpr (I == 11 || I == 13 || I == 15 || I == 17) {
          constexpr int m = (I - 11) / 2;
          sel[m] = opqv((c2 >> 1) == 4 * (g2 & 1) + m ? 0x3f80u << (16 * (c2 & 1)) : 0u);
        }
        SB();
      });
      const v4u q0 = pack8(aQ, 0), q1 = pack8(aQ, 1);
      f32x16 pq = mfma32(wph[0], q0, zero16);
      pq = mfma32(wph[1], q1, pq);
      SB();
      v4u phq[4];
      v4u v[8];
      f32x4 z;
      {
        u32 o[16];
        dual_softmax(pq, c, o, [&](auto I) {
          constexpr int i = I;
          if constexpr (i == KVCHK) {
            STAMP(sb + 6);
            if constexpr (KMODE == 0) wfinish(rs, l0, ckv, 16, pvk[0]);
            else pfinish2<1 + NQ2 - (KVPK1 * NQ2 + 31 - QS) / (32 - QS) + (!layer && VPK > KVPK1), NQ2 - (KVPK2 * NQ2 + 31 - QS) / (32 - QS) + (!layer && VPK > KVPK2)>(rs, l0, ckv, 16, pvk);
            STAMP(sb + 7);
#pragma unroll
            for (int k = 0; k < 4; ++k) z[k] = __builtin_bit_cast(float, ld32<SC1>(rs, o_zl + k * 512, zbase + sd));
            SB();
          }
          static_for<8>([&](auto K) {
            constexpr int k = K, at = KVCHK + (k + 1) / KVLD;
            if constexpr ((at < 9 ? at : 9) == i) { {
              v[k] = ldf<SC1>(rs, l16, pbase + (sd ^ 8192), k * 1024);
              SB();
              if constexpr (layer == 0 && k == 7) { vb = peek(rs, w == 0 ? l0 : 0x80000000u, cnt_off(C_V + (b & 15))); SB(); }
            } }
          });
        });
#pragma unroll
        for (int m = 0; m < 4; ++m) {
          auto sw = __builtin_amdgcn_permlane32_swap(o[m], o[4 + m], false, false);
          phq[0][m] = sw[0];
          phq[2][m] = sw[1];
          auto sx = __builtin_amdgcn_permlane32_swap(o[8 + m], o[12 + m], false, false);
          phq[1][m] = sx[0];
          phq[3][m] = sx[1];
        }
#pragma unroll
        for (int m = 0; m < 4; ++m) PIN(phq[m]);
      }
      SB();
      {
        const float zs = sum32(sum16((z[0] + z[1]) + (z[2] + z[3])));
        *(LAS unsigned short*)(a_kz) = (unsigned short)pk(zs, zs);
        if constexpr (KMODE == 2) pretire(pvk);
        SB();
        f32x4 sv = {0.f, 0.f, 0.f, 0.f};
#pragma unroll
        for (int i = 0; i < 8; ++i) { sv = mfma16(sel, v[i], sv); SB(); }
        *(LAS v2u*)(a_kw) = v2u{pk(sv[0], sv[1]), pk(sv[2], sv[3])};
      }
      if constexpr (layer == 0) {
        if (w == 0) {
          if ((__builtin_amdgcn_readfirstlane(vv) & 0xffffu) < 32u) {
            vv = vb;
#pragma unroll 1
            for (u32 spins = 0; spins < (1u << 16) && (__builtin_amdgcn_readfirstlane(vv) & 0xffffu) < 32u; ++spins) vv = peek(rs, l0, cnt_off(C_V + (b & 15)));
          }
          if (ln == 0) *(LAS u32*)(L + XS_OFF + 16) = vv;
        }
      }
      BAR();
      if constexpr (layer == 0) vv = *(LAS u32*)(L + XS_OFF + 16);
      STAMP(sb + 8);
      {
        v4u kvf[4];
#pragma unroll
        for (int i = 0; i < 4; ++i) kvf[i] = *(LAS v4u*)(a_kr + i * 544);
        SB();
        f32x16 nd[2];
        nd[0] = mfma32(kvf[0], phq[0], zero16);
        nd[0] = mfma32(kvf[1], phq[1], nd[0]);
        nd[1] = mfma32(kvf[2], phq[2], zero16);
        nd[1] = mfma32(kvf[3], phq[3], nd[1]);
        SB();
        if constexpr (layer == 0) {
          sd = opq(((((u32)__builtin_amdgcn_readfirstlane(vv) >> 16 > 32u ? 1u : 0u) ^ (xcd >> 2) ^ NFLIP) & 1u) * 8192);
        }
#pragma unroll
        for (int h = 0; h < 2; ++h) {
          const float inv = __builtin_amdgcn_rcpf(nd[h][8] + 1e-6f);
          v4u ob;
#pragma unroll
          for (int m = 0; m < 4; ++m) ob[m] = pk(nd[h][2 * m] * inv, nd[h][2 * m + 1] * inv);
          st128<SC1>(ob, rs, l16 + h * 1024, OB + layer * MB2 + (tt & 31) * 65536 + blk(hp * 2048) + (sd ^ (layer * 8192)));
          SB();
        }
      }
      STAMP(sb + 9);
      static_for<ODR>([&](auto I) { pf(IC(s_od + I)); });
      wsignal<ODR>(rs, l0, cnt_off(C_O + layer * 128 + ((tt >> 1) & 15) * 4 + (hp >> 2)) + sd);
      if constexpr (layer == 0) asm volatile("" ::"v"(vb));
      STAMP(sb + 10);
      const u32 co = cnt_off(C_O + layer * 128 + (rt2 & 15) * 4 + w) + sd;
      u32 pvo[3], pvz[2];
      BAR();
      if constexpr (OMODE == 1) {
        apeek(rs, l0, co, pvo[0]);
        NAP(PS2);
        apeek(rs, l0, co, pvo[1]);
        NAP(PS2);
        apeek(rs, l0, co, pvo[2]);
        static_for<n_op>([&](auto I) { pf(IC(s_op + I)); });
        pfinish<2 + n_op, 1 + n_op, n_op>(rs, l0, co, 8, pvo);
      } else if constexpr (OMODE == 2) {
        NAP(PS4);
        apeek(rs, l0, co, pvz[0]);
        static_for<n_op>([&](auto I) { pf(IC(s_op + I)); });
        NAP(PS6);
        apeek(rs, l0, co, pvz[1]);
        pfinish2<1 + n_op, 0>(rs, l0, co, 8, pvz);
      } else {
        NAP(PS4);
        pvo[0] = peek(rs, l0, co);
        SB();
        static_for<n_op>([&](auto I) { pf(IC(s_op + I)); });
        wfinish(rs, l0, co, 8, pvo[0]);
      }
      STAMP(sb + 11);
      const u32 ab = OB + layer * MB2 + w * 16384 + (sd ^ (layer * 8192));
      v4u af[DXV];
      splitk_phase<FrO, DXV, 0>(rs, wo, af, ab + (tte & 31) * 65536, ab + ((tte & 31) ^ 1) * 65536, L, w, l16, 1.f, res, [&](int) {}, [&]() {}, [&](auto I) {
        if constexpr (I == 1 && OMODE == 1) pretire(pvo);
        if constexpr (I == 1 && OMODE == 2) pretire(pvz);
        if constexpr (I >= 4 && s_p2 + I - 4 < s_xd) pf(IC(s_p2 + I - 4));
      });
      STAMP(sb + 12);
      resid_store(rs, res, l16, o_st, xtile(2 * layer, tte) + blk(n0 * 64), stile(2 * layer, tte) + (n0 >> 5) * 256);
      STAMP(sb + 13);
      static_for<XDR>([&](auto I) { pf(IC(s_xd + I)); });
      wsignal<XDR>(rs, l0, (cnt_off(C_X1 + layer * 32 + (tte & 31)) + sd));
      STAMP(sb + 14);
    }
    const u32 lh = opqv(opqv(lane) < nlh ? lane * CSTR : 0x80000000u);
    v4u af[WINH];
    const u32 ch0 = cnt_off(C_H + layer * 512 + (rt2 & 15) * 4 + w) + sd;
    const u32 sh0 = cnt_off(C_H + layer * 512 + ((tt >> 1) & 15) * 4 + (2 * fb) / 7) + sd;
    auto htile = [&](u32 t) { return HB + layer * 4 * MB2 + (t & 31) * 262144 + (sd ^ ((t & 8) * 1024)); };
    auto xtile = [&](int i, u32 t) { return XT + i * MB2 + (t & 31) * 65536 + (sd ^ ((i & 1) ? 0 : 8192)); };
    auto stile = [&](int i, u32 t) { return ST + i * ST_SZ + blk((t & 31) * 4096) + sd; };
    const u32 hown = htile(tte) + w * 16384, hoth = hown ^ 262144;
    {
      const u32 ln = opqv(lane), l16 = ln * 16, r = ln & 31, hf = ln >> 5;
      auto w2ld = [&](auto I) {
        constexpr int i = I, k = (i >> 1) % 7;
        w2f[i >> 1][i & 1] = ld128<SC1>(rs, l16 + (k & 1) * 2048, wb + (i / 14) * 57344 + (k >> 1) * 4096 + ((i & 1) ? 1024 - wsw : wsw));
      };
      auto hld = [&](int i) { if (i < HPRE) { af[i] = ld_fr<FrH>(rs, l16, hown, hoth, i); SB(); } };
      if (p3on) {
        static_for<3>([&](auto J) {
          if constexpr (XMODE == 1) apeek(rs, l0, (cnt_off(C_X1 + layer * 32 + (tt & 31)) + sd), pvx[J]);
          if constexpr (XMODE == 0 && J == (layer ? 0 : 1)) { if constexpr (layer) NAP(PS3 + 1); pvx[0] = peek(rs, l0, (cnt_off(C_X1 + layer * 32 + (tt & 31)) + sd)); }
          if constexpr (XMODE == 3 && J > 0) { if constexpr (layer) NAP(J == 1 ? PS3 + 1 : PS5); apeek(rs, l0, (cnt_off(C_X1 + layer * 32 + (tt & 31)) + sd), pvy[J - 1]); }
          SB();
          if constexpr (layer == 0) static_for<W2D / 3>([&](auto I) { w2ld(IC(J * (W2D / 3) + I)); SB(); });
          else if constexpr (J < 2 && XMODE == 1) NAP(PS2);
        });
        constexpr int DX = DXV;
        v4u xf[32];
        const u32 xb = xtile(2 * layer, tt);
        f32x4 sp[4];
        const u32 stb = stile(2 * layer, tt);
        LAS v4u* WL = (LAS v4u*)(L + l16);
        LAS v4u* WH = (LAS v4u*)(L + opqv(l16 + 65536));
        u32 hb[2];
#pragma unroll
        for (int s = 0; s < 2; ++s) hb[s] = opq(htile(tt) + ((2 * fb + s) / 7) * 16384 + ((2 * fb + s) % 7) * 1024);
        SB();
        asm volatile("s_waitcnt vmcnt(%0)" ::"n"((layer == 0 ? W2D : 0) + (XMODE == 1 ? 4 : XMODE == 3 ? 3 : XMODE == 0 ? 2 : 1)) : "memory");
        BAR();
        v4u wa[3], wc[3], wq[4];
#pragma unroll
        for (int i = 0; i < 2; ++i) { wa[i] = WL[i * 64]; wc[i] = WL[(32 + i) * 64]; }
        SB();
        STAMP(sb + 15);
        if constexpr (XMODE == 0) wfinish(rs, l0, (cnt_off(C_X1 + layer * 32 + (tt & 31)) + sd), 16, pvx[0]);
        else if constexpr (XMODE == 3) pfinish2<1 + (layer ? 0 : 2 * W2D / 3), (layer ? 0 : W2D / 3)>(rs, l0, (cnt_off(C_X1 + layer * 32 + (tt & 31)) + sd), 16, pvy);
        else if constexpr (XMODE == 2) { if constexpr (layer) NAP(PS3); wwait(rs, l0, (cnt_off(C_X1 + layer * 32 + (tt & 31)) + sd), 16); }
        else pfinish<2 + (layer ? 0 : W2D), 1 + (layer ? 0 : 2 * W2D / 3), (layer ? 0 : W2D / 3)>(rs, l0, (cnt_off(C_X1 + layer * 32 + (tt & 31)) + sd), 16, pvx);
        STAMP(sb + 16);
#pragma unroll
        for (int ks = 0; ks < DX; ++ks) { xf[ks] = ldr(rs, l16, xb, ks); SB(); }
        f32x16 acc[4];
        acc[0] = zero16;
        acc[1] = zero16;
        float nc, cp[4][4];
        Stats zs_;
        SB();
        static_for<32>([&](auto KSI) {
          constexpr int ks = KSI;
          acc[0] = mfma32(wa[ks % 3], xf[ks], acc[0]);
          SB();
          if constexpr (ks + 2 < 32) wa[(ks + 2) % 3] = WL[(ks + 2) * 64];
          else wq[2 * (ks - 30)] = WH[(2 * (ks - 30)) * 64];
          SB();
          if constexpr (ks + DX < 32) xf[ks + DX] = ldr(rs, l16, xb, ks + DX);
          if constexpr (layer == 0 && ks >= 20) w2ld(IC(W2D + ks - 20));
          if constexpr (layer == 0 && ks >= 20 && ks < 28) {
            constexpr int t = (ks - 20) / 2;
            if constexpr (ks & 1) { u32 cw = cs_word(cp[t][0], hf); PIN(cw); cs1[t] = cw; }
            else { cp[t][0] = (cp[t][0] + cp[t][1]) + (cp[t][2] + cp[t][3]); PIN(cp[t][0]); }
          }
          SB();
          acc[1] = mfma32(wc[ks % 3], xf[ks], acc[1]);
          SB();
          if constexpr (ks + 2 < 32) wc[(ks + 2) % 3] = WL[(32 + ks + 2) * 64];
          else wq[2 * (ks - 30) + 1] = WH[(2 * (ks - 30) + 1) * 64];
          SB();
          if constexpr (ks < 4) sp[ks] = __builtin_bit_cast(f32x4, ld128<SC1>(rs, l16 + ks * 1024, stb));
          if constexpr (layer == 0 && ks >= 4 && ks < 8) {
#pragma unroll
            for (int i = 0; i < 4; ++i) cp[ks - 4][i] = __builtin_bit_cast(float, ld32<SC1>(rs, ln * 4 + i * 256 + (ks - 4) * 1024, CS_W1 + fb * 4096));
          }
          if constexpr (ks == 1) pretire(pvy);
          if constexpr (ks >= 16 && ks < 24) stats_step<ks - 16>(zs_, sp);
          if constexpr (ks == 24) { nc = zs_.rstd * -LOG2E; PIN(nc); }
          SB();
          if constexpr (ks == 27) acc[0] = mean_fix<0>(cs1, zs_.mw, acc[0]);
          if constexpr (ks == 28) acc[1] = mean_fix<1>(cs1, zs_.mw, acc[1]);
          if constexpr (ks == 29) acc[2] = mean_fix<2>(cs1, zs_.mw, zero16);
          if constexpr (ks == 30) acc[3] = mean_fix<3>(cs1, zs_.mw, zero16);
          SB();
        });
        STAMP(sb + 17);
        float tv[64], ev[64];
        v4u ho[8];
        u32 pv0;
        auto stage = [&](auto V, auto ST_) {
          constexpr int v = V, st = ST_;
          if constexpr (st == 0) { tv[v] = acc[v >> 4][v & 15] * nc; PIN(tv[v]); }
          else if constexpr (st == 1) { ev[v] = __builtin_amdgcn_exp2f(tv[v]); PIN(ev[v]); }
          else if constexpr (st == 2) { ev[v] = 1.f + ev[v]; PIN(ev[v]); }
          else if constexpr (st == 3) { ev[v] = __builtin_amdgcn_rcpf(ev[v]); PIN(ev[v]); }
          else if constexpr (st == 4) { tv[v] = tv[v] * ev[v]; PIN(tv[v]); }
          else if constexpr (v & 1) {
            u32 hh = pk(tv[v - 1], tv[v]);
            PIN(hh);
            ho[v >> 3][(v >> 1) & 3] = hh;
            if constexpr ((v & 7) == 7) st128<SC1>(ho[v >> 3], rs, l16, hb[(v >> 3) & 1] + (v >> 4) * 65536);
          }
        };
        static_for<64>([&](auto GI) {
          constexpr int gi = GI, t = 2 + (gi >> 5), ks = gi & 31;
          acc[t] = mfma32(wq[gi & 3], xf[ks], acc[t]);
          SB();
          if constexpr (gi + 4 < 64) wq[gi & 3] = WH[(gi + 4) * 64];
          if constexpr (layer == 0 && p3_w2gap(gi)) w2ld(IC(W2D + 12 + (gi * W2B) / 8));
          SB();
          static_for<48>([&](auto V) {
            constexpr int d = gi - p3_g0(V);
            if constexpr (d >= 0 && d < 6) stage(V, IC(d));
          });
          if constexpr (gi == HS0) wsignal<p3_younger(layer, 0, HS0)>(rs, lh, sh0);
          if constexpr (gi == HS1) wsignal<p3_younger(layer, 1, HS1)>(rs, lh, sh0 + 128 * CSTR);
          if constexpr (gi == PK0) pv0 = peek(rs, l0, ch0);
          SB();
        });
        STAMP(sb + 18);
        constexpr int W2E = 56 - W2D - 12 - 8 * W2B;
        static_assert(W2E >= 0, "w2 schedule");
        {
          f32x2 t2[8], e2[8];
#pragma unroll
          for (int m = 0; m < 8; ++m) { t2[m] = f32x2{acc[3][2 * m], acc[3][2 * m + 1]} * nc; PIN(t2[m]); }
          SB();
#pragma unroll
          for (int m = 0; m < 8; ++m) { e2[m] = f32x2{__builtin_amdgcn_exp2f(t2[m].x), __builtin_amdgcn_exp2f(t2[m].y)}; PIN(e2[m]); }
          SB();
          if constexpr (HPRE > 0) wfinish(rs, l0, ch0, 8, pv0);
          STAMP(sb + 21);
          hld(0);
#pragma unroll
          for (int m = 0; m < 8; ++m) { e2[m] = e2[m] + 1.f; PIN(e2[m]); }
          SB();
#pragma unroll
          for (int m = 0; m < 8; ++m) { e2[m] = f32x2{__builtin_amdgcn_rcpf(e2[m].x), __builtin_amdgcn_rcpf(e2[m].y)}; PIN(e2[m]); if (m == 1 || m == 5) { SB(); hld(1 + m / 4); } }
          SB();
#pragma unroll
          for (int m = 0; m < 8; ++m) { t2[m] = t2[m] * e2[m]; PIN(t2[m]); }
          SB();
#pragma unroll
          for (int i = 3; i < HPRE; ++i) hld(i);
#pragma unroll
          for (int s = 0; s < 2; ++s)
            st128<SC1>(v4u{pk(t2[4 * s].x, t2[4 * s].y), pk(t2[4 * s + 1].x, t2[4 * s + 1].y), pk(t2[4 * s + 2].x, t2[4 * s + 2].y), pk(t2[4 * s + 3].x, t2[4 * s + 3].y)},
                       rs, l16, hb[s] + 3 * 65536);
        }
        SB();
        wsignal<p3_younger(layer, 2, 64) + HPRE + 2>(rs, lh, sh0 + 256 * CSTR);
        if constexpr (layer == 0) static_for<W2E>([&](auto E) { w2ld(IC(W2D + 12 + 8 * W2B + E)); });
        SB();
        STAMP(sb + 19);
        if constexpr (HPRE == 0) wfinish(rs, l0, ch0, 8, pv0);
      } else {
        if constexpr (layer == 0) static_for<56>([&](auto E) { w2ld(E); });
        DRAIN();
        BAR();
        wwait(rs, l0, ch0, 8);
#pragma unroll
        for (int i = 0; i < HPRE; ++i) hld(i);
      }
    }
    {
      const u32 ln = opqv(lane), l16 = ln * 16, r = ln & 31, hf = ln >> 5;
      const u32 wqb = opq(WQKV_T + hp * 98304 + w * 16384);
      u32 pv1[2], pv2[2], pv3[2];
      splitk_phase<FrH, WINH, HPRE>(rs, w2f, af, hown, hoth, L, w, l16, -LN2, res,
        [&](int i) { if (i == T1A || i == T1B) apeek(rs, l0, ch0 + 128 * CSTR, pv1[i == T1B]); },
        [&]() {
          wsignal<3 + 14 - HPRE>(rs, lh, sh0 + 384 * CSTR);
          STAMP(sb + 22);
          pfinish2<14 - T1A + 2, 14 - T1B + 1>(rs, l0, ch0 + 128 * CSTR, 8, pv1);
          STAMP(sb + 20);
        },
        [&](auto I) {
          constexpr int i = I, g = i - 42;
          if constexpr (i == 15) pretire(pv1);
          constexpr auto ops = [](int a, int b) {
            int n = 0;
            for (int q = 14; q < 56; ++q) n += FrH::lstep(q, WINH) >= a && FrH::lstep(q, WINH) < b;
            for (int q : {PK2, PK2B, PK3, PK3B}) n += q > a && q < b;
            return n;
          };
          static_assert(PK2 < PK2B && PK2B < T2CHK && PK2B != PK3 && PK2 != PK3 && PK3 < PK3B && PK3B < T3CHK && T2CHK == 14 && T3CHK >= 21 && T3CHK <= 28, "peek order");
          if constexpr (i == T2CHK) pfinish2<ops(PK2, i), ops(PK2B, i)>(rs, l0, ch0 + 256 * CSTR, 8, pv2);
          if constexpr (i == T3CHK) { STAMP(sb + 27); pfinish2<ops(PK3, i), ops(PK3B, i)>(rs, l0, ch0 + 384 * CSTR, 8, pv3); STAMP(sb + 28); }
          if constexpr (i == PK2 || i == PK2B) apeek(rs, l0, ch0 + 256 * CSTR, pv2[i == PK2B]);
          if constexpr (i == PK3 || i == PK3B) apeek(rs, l0, ch0 + 384 * CSTR, pv3[i == PK3B]);
          if constexpr (i == 29) pretire(pv2);
          if constexpr (i == 36) pretire(pv3);
          if constexpr (KWPOS == 0) {
            if constexpr (layer == 0 && g >= 10 && g <= 14) dma1<g - 10>(rs, l16, wqb, L + w * 16384);
            if constexpr (layer == 0 && g >= 19 && g <= 21) dma1<g - 14>(rs, l16, wqb, L + w * 16384);
          } else {
            if constexpr (layer == 0 && g >= 10 && g < 26) dma1<((g - 10) & 3)>(rs, l16, wqb + ((g - 10) >> 2) * 4096, L + ((w * 16384 + ((g - 10) >> 2) * 4096) ^ rx));
          }
        });
      STAMP(sb + 23);
      if constexpr (layer == 0) {
        resid_store(rs, res, l16, hf ? 0x80000000u : (r & 15) * 16 + (r >> 4) * 8, xtile(1, tte) + blk(n0 * 64), stile(1, tte) + (n0 >> 5) * 256);
        STAMP(sb + 24);
        if constexpr (KWPOS == 0) dma_range<8, 16>(rs, l16, wqb, L + w * 16384);
        wsignal<KWPOS == 0 ? 8 : 0>(rs, l0, (cnt_off(C_X + (tte & 31)) + sd));
        STAMP(sb + 25);
      } else {
        const rsrc_t rso = mkrsrc(out, S * D * 2);
#pragma unroll
        for (int s = 0; s < 2; ++s) st128<OAUX>(res[s], rso, r * 1024 + hf * 16 + s * 32, tte * 32768 + n0 * 2);
        if (b == 255 && tid < 16) st64<SC1>(v2u{tag_lo, tag_hi}, rs, tid * 8, EPOCH_OFF);
        STAMP(sb + 24);
      }
    }
  });
  asm volatile(".Lkcode1:");
}

extern "C" void kernel_launch(void* const* d_in, const int* in_sizes, int n_in, void* d_out, int out_size, void* d_ws,
                              size_t ws_size, hipStream_t stream) {
  (void)hipFuncSetAttribute((const void*)k_main, hipFuncAttributeMaxDynamicSharedMemorySize, LDS_BYTES);
  k_main<<<256, 256, LDS_BYTES, stream>>>((const uint16_t*)d_in[0], (const uint16_t*)d_in[1], (const uint16_t*)d_in[2],
                                          (const uint16_t*)d_in[3], (const uint16_t*)d_in[5], (const uint16_t*)d_in[6],
                                          (unsigned char*)d_ws, (unsigned char*)d_out);
}
```

```cpp
#include <hip/hip_runtime.h>
#include <hip/hip_bf16.h>
#include <stdint.h>
#include <utility>

typedef unsigned int u32;
typedef u32 v4u __attribute__((ext_vector_type(4)));
typedef u32 v2u __attribute__((ext_vector_type(2)));
typedef short s16x4 __attribute__((ext_vector_type(4)));
typedef __bf16 bf16x8 __attribute__((ext_vector_type(8)));
typedef __bf16 bf16x2 __attribute__((ext_vector_type(2)));
typedef float f32x2 __attribute__((ext_vector_type(2)));
typedef float f32x4 __attribute__((ext_vector_type(4)));
typedef float f32x16 __attribute__((ext_vector_type(16)));
#define LAS __attribute__((address_space(3)))
#define DI __device__ __forceinline__
typedef __amdgpu_buffer_rsrc_t rsrc_t;

constexpr int XA = 6, XC = 9, XB = 14, ULP = 34, FPK = 3, FCK = 5, UB = 10;
constexpr int DXV = 10, RA = 2, RB = 6;
constexpr int WOK = 2, NQ1 = 10, NQ2 = 11, ODR = 7, OPL0 = 4, OPL1 = 4, XDR = 5, W2D = 12, W2S = 56;
constexpr int QS = 10, KVPK1 = 4, KVPK2 = 14, KVCHK = 0, KVLD = 2, LK0 = 6, JPK = 0, VPK = 18;
constexpr int HS0 = 36, HS1 = 56, PK0 = 59, T1A = 0, T1B = 7, PK2 = 0, PK2B = 6, PK3 = 7, PK3B = 13, T2CHK = 14, T3CHK = 21;
constexpr int PS3 = 2, PS4 = 2, PS5 = 5, PS6 = 3, TLBW = 262144;
constexpr int SE2 = 6, SE1H = 1, SE1O = 0, SE0 = 2, SEK = 10, SEN = 8, SEA = 5;
constexpr int S = 2048, D = 512;
constexpr u32 CTRL = 0;
constexpr u32 WQKV_T = 262144;
constexpr u32 WO_T = WQKV_T + 16 * 98304 + 32768;
constexpr u32 W1_T = WO_T + 524288;
constexpr u32 W2_T = W1_T + 14 * 131072;
constexpr u32 CS_QKV = W2_T + 8 * 4 * 28 * 2048;
constexpr u32 CS_W1 = CS_QKV + 65536;
constexpr u32 MB2 = 2097152;
constexpr u32 XT = 3 * MB2;
constexpr u32 OB = 6 * MB2;
constexpr u32 HB = 8 * MB2;
constexpr u32 ST = 16 * MB2;
constexpr u32 ST_SZ = 262144;
constexpr u32 ZP = ST + 4 * ST_SZ;
constexpr u32 KVP = 17 * MB2;
constexpr u32 WS_END = 18 * MB2;
static_assert(CS_W1 + 65536 <= XT, "workspace map");
constexpr u32 blk(u32 off) { return (off >> 13) * 16384 + (off & 8191); }
constexpr u32 CSTR = 128;
enum { C_H = 0, C_O = 1024, C_X1 = 1280, C_X = 1408, C_J = 1440, C_V = 1504, C_TMO = 1520, C_KV = 1536, NCNT = 1792 };
constexpr u32 INIT_OFF = CTRL + NCNT * CSTR, EPOCH_OFF = INIT_OFF + 16 * CSTR, FLAG_OFF = EPOCH_OFF + CSTR;
static_assert(FLAG_OFF + 192 * CSTR <= WQKV_T, "control area");

constexpr float LOG2E = 1.4426950408889634f;
constexpr float LN2 = 0.6931471805599453f;

DI u32 pk(float a, float b) {
  f32x2 v = {a, b};
  bf16x2 r = __builtin_convertvector(v, bf16x2);
  return __builtin_bit_cast(u32, r);
}
DI float bflo(u32 u) { return __builtin_bit_cast(float, u << 16); }
DI float bfhi(u32 u) { return __builtin_bit_cast(float, u & 0xffff0000u); }
DI f32x16 mfma32(v4u a, v4u b, f32x16 c) {
  f32x16 d = __builtin_amdgcn_mfma_f32_32x32x16_bf16(__builtin_bit_cast(bf16x8, a), __builtin_bit_cast(bf16x8, b), c, 0, 0, 0);
  asm volatile("" : "+a"(d));
  return d;
}
template <bool FIRST, bool AA, bool PAD = false> DI void vmfma(f32x16& c, const v4u& a, const v4u& b) {
#define VM_(pre, cc, oc, ac) asm volatile(pre "v_mfma_f32_32x32x16_bf16 %0, %1, %2, " cc : oc(c) : ac(a), "v"(b))
  if constexpr (PAD) {
    if constexpr (FIRST) { if constexpr (AA) VM_("s_nop 1\n\t", "0", "=&v", "a"); else VM_("s_nop 1\n\t", "0", "=&v", "v"); }
    else { if constexpr (AA) VM_("s_nop 1\n\t", "%0", "+v", "a"); else VM_("s_nop 1\n\t", "%0", "+v", "v"); }
  } else {
    if constexpr (FIRST) { if constexpr (AA) VM_("", "0", "=&v", "a"); else VM_("", "0", "=&v", "v"); }
    else { if constexpr (AA) VM_("", "%0", "+v", "a"); else VM_("", "%0", "+v", "v"); }
  }
#undef VM_
}
template <int N> DI void settle(f32x16& c) { if constexpr (N > 0) asm volatile("s_nop %1" : "+v"(c) : "n"(N - 1)); }
DI f32x4 mfma16(v4u a, v4u b, f32x4 c) {
  return __builtin_amdgcn_mfma_f32_16x16x32_bf16(__builtin_bit_cast(bf16x8, a), __builtin_bit_cast(bf16x8, b), c, 0, 0, 0);
}
DI float dot2(u32 a, u32 b, float c) {
  return __builtin_amdgcn_fdot2_f32_bf16(__builtin_bit_cast(bf16x2, a), __builtin_bit_cast(bf16x2, b), c, false);
}
template <int AUX> DI v4u ld128(rsrc_t rs, u32 voff, u32 soff) { return __builtin_amdgcn_raw_buffer_load_b128(rs, voff, soff, AUX); }
template <int AUX> DI v2u ld64(rsrc_t rs, u32 voff, u32 soff) { return __builtin_amdgcn_raw_buffer_load_b64(rs, voff, soff, AUX); }
template <int AUX> DI u32 ld32(rsrc_t rs, u32 voff, u32 soff) { return __builtin_amdgcn_raw_buffer_load_b32(rs, voff, soff, AUX); }
template <int AUX> DI void st128(v4u v, rsrc_t rs, u32 voff, u32 soff) {
  __builtin_amdgcn_raw_buffer_store_b128(v, rs, voff, soff, AUX);
  asm volatile("s_nop 1" ::"v"(v) : "memory");
}
template <int AUX> DI void st64(v2u v, rsrc_t rs, u32 voff, u32 soff) { __builtin_amdgcn_raw_buffer_store_b64(v, rs, voff, soff, AUX); }
DI rsrc_t mkrsrc(const void* p, u32 bytes) { return __builtin_amdgcn_make_buffer_rsrc((void*)p, 0, (int)bytes, 0x00020000); }
DI float sum32(float v) {
  u32 u = __builtin_bit_cast(u32, v);
  auto r = __builtin_amdgcn_permlane32_swap(u, u, false, false);
  const u32 r0 = r[0], r1 = r[1];
  return __builtin_bit_cast(float, r0) + __builtin_bit_cast(float, r1);
}
DI float mix16(float a, float b) {
  auto r = __builtin_amdgcn_permlane16_swap(__builtin_bit_cast(u32, a), __builtin_bit_cast(u32, b), false, false);
  const u32 r0 = r[0], r1 = r[1];
  return __builtin_bit_cast(float, r0) + __builtin_bit_cast(float, r1);
}
DI float mix32(float a, float b) {
  auto r = __builtin_amdgcn_permlane32_swap(__builtin_bit_cast(u32, a), __builtin_bit_cast(u32, b), false, false);
  const u32 r0 = r[0], r1 = r[1];
  return __builtin_bit_cast(float, r0) + __builtin_bit_cast(float, r1);
}
DI v2u trread(LAS unsigned char* __restrict__ p) {
  return __builtin_bit_cast(v2u, __builtin_amdgcn_ds_read_tr16_b64_v4i16((LAS s16x4*)p));
}
DI int rho32(int reg, int hf) { return (reg & 3) + 8 * (reg >> 2) + 4 * hf; }
constexpr u32 ONES2 = 0x3f803f80u;

DI v4u pack8(const f32x16& a, int s) {
  v4u r;
#pragma unroll
  for (int m = 0; m < 4; ++m) r[m] = pk(a[8 * s + 2 * m], a[8 * s + 2 * m + 1]);
  return r;
}

#define SB() __builtin_amdgcn_sched_barrier(0)
#define PIN(x) asm volatile("" : "+v"(x))
#define BAR() asm volatile("s_waitcnt lgkmcnt(0)\n\ts_barrier" ::: "memory")
#define DRAIN() asm volatile("s_waitcnt vmcnt(0)" ::: "memory")
constexpr int SC1 = 16;

DI u32 opq(u32 x) { asm volatile("" : "+s"(x)); return x; }
DI u32 opqv(u32 x) { asm volatile("" : "+v"(x)); return x; }
DI v4u ldr(rsrc_t rs, u32 vo, u32 sbase, int ks) { return ld128<16>(rs, vo + (ks & 3) * 1024, sbase + blk((ks >> 2) * 4096)); }
template <int AUX> DI v4u ldf(rsrc_t rs, u32 vo, u32 sbase, u32 off) { return ld128<AUX>(rs, vo + (off & 4095u), sbase + (off & ~4095u)); }
template <int I> DI void dma1(rsrc_t rs, u32 vo, u32 sbase, LAS unsigned char* d) {
  __builtin_amdgcn_raw_ptr_buffer_load_lds(rs, (LAS u32*)(d + (I >> 2) * 4096), 16, vo, sbase + (I >> 2) * 4096, (I & 3) * 1024, SC1);
}
template <class F, int... I> DI void static_for_impl(F&& f, std::integer_sequence<int, I...>) { (f(std::integral_constant<int, I>{}), ...); }
template <int N, class F> DI void static_for(F&& f) { static_for_impl(f, std::make_integer_sequence<int, N>{}); }
DI u32 cnt_off(int idx) { return CTRL + idx * CSTR; }
DI u32 lane0_off() { return (threadIdx.x & 63) == 0 ? 0u : 0x80000000u; }
DI void add1(rsrc_t rs, u32 l0, u32 coff) { (void)__builtin_amdgcn_raw_ptr_buffer_atomic_add_i32(1, rs, l0, coff, 0); }
template <int N> DI void wsignal(rsrc_t rs, u32 l0, u32 coff) {
  SB();
  asm volatile("s_waitcnt vmcnt(%0)" ::"n"(N) : "memory");
  add1(rs, l0, coff);
  SB();
}
DI u32 peek(rsrc_t rs, u32 l0, u32 coff) { return __builtin_amdgcn_raw_ptr_buffer_atomic_add_i32(0, rs, l0, coff, 0); }
DI void wfinish(rsrc_t rs, u32 l0, u32 coff, u32 n, u32 pv) {
  if (__builtin_amdgcn_readfirstlane(pv) < n) {
#pragma unroll 1
    for (u32 spins = 0;; ++spins) {
      if (__builtin_amdgcn_readfirstlane(peek(rs, l0, coff)) >= n) break;
      if (spins > (1u << 16)) { __builtin_amdgcn_raw_buffer_store_b32(1u, rs, 0, cnt_off(C_TMO), SC1); break; }
    }
  }
  asm volatile("" ::: "memory");
}
DI void wwait(rsrc_t rs, u32 l0, u32 coff, u32 n) { wfinish(rs, l0, coff, n, peek(rs, l0, coff)); }
DI void apeek(rsrc_t rs, u32 l0, u32 coff, u32& p) {
  SB();
  asm volatile("v_mov_b32 %0, 0\n\tbuffer_atomic_add %0, %1, %2, %3 offen sc0 ; APEEK" : "=&v"(p) : "v"(l0), "s"(rs), "s"(coff) : "memory");
  SB();
}
#define PCHK2(P, W) "s_waitcnt vmcnt(" W ")\n\tv_readfirstlane_b32 %2, " P "\n\ts_cmp_ge_u32 %2, %7\n\ts_cbranch_scc1 .Lqd%=\n\tv_mov_b32 " P ", 0\n\tbuffer_atomic_add " P ", %4, %5, %6 offen sc0\n\t"
template <int N0, int N1> DI void pfinish2(rsrc_t rs, u32 l0, u32 coff, u32 n, u32 (&p)[2]) {
  u32 t, cnt;
  SB();
  asm volatile(PCHK2("%0", "%8") PCHK2("%1", "%9") "s_movk_i32 %3, 0x7fff\n.Lql%=:\n\t" PCHK2("%0", "1") PCHK2("%1", "1")
               "s_sub_u32 %3, %3, 1\n\ts_cbranch_scc0 .Lql%=\n.Lqd%=:"
               : "+v"(p[0]), "+v"(p[1]), "=&s"(t), "=&s"(cnt)
               : "v"(l0), "s"(rs), "s"(coff), "n"(n), "n"(N0), "n"(N1 + 1)
               : "memory", "scc");
  SB();
}
DI void pretire(u32 (&p)[2]) { asm volatile("; PRETIRE %0 %1" ::"v"(p[0]), "v"(p[1])); }
#define NAP(n) do { SB(); __builtin_amdgcn_s_sleep(n); SB(); } while (0)
DI void poll3(rsrc_t rs, u32 vo, u32 need) {
#pragma unroll 1
  for (u32 spins = 0;; ++spins) {
    const u32 v = peek(rs, vo, 0);
    if ((__builtin_amdgcn_ballot_w64(v >= need) & 7ull) == 7ull) break;
    if (spins > (1u << 16)) { __builtin_amdgcn_raw_buffer_store_b32(1u, rs, 0, cnt_off(C_TMO), SC1); break; }
  }
  asm volatile("" ::: "memory");
}
template <int CTRL_> DI float dpp_add(float v) {
  return v + __builtin_bit_cast(float, __builtin_amdgcn_update_dpp(0, __builtin_bit_cast(int, v), CTRL_, 0xf, 0xf, true));
}

constexpr int SCR = 131072;
constexpr int PK_OFF = SCR, PK_STR = 144, VV_OFF = PK_OFF + 128 * PK_STR, VV_STR = 80, KF_OFF = VV_OFF + 128 * VV_STR;
constexpr int XS_OFF = 98304;
constexpr int RED_OFF = SCR;
constexpr int LDS_BYTES = 163840;
static_assert(KF_OFF + 4 * 544 <= LDS_BYTES, "lds");

DI void lds_put1(LAS unsigned char* p, const f32x16& a, int q) {
  *(LAS f32x4*)(p + q * 1024) = f32x4{a[4 * q], a[4 * q + 1], a[4 * q + 2], a[4 * q + 3]};
}

extern __shared__ __attribute__((aligned(16))) unsigned char smem[];

struct Sch { int put2[4], put3[4], bar1, rd[8], fu[8], put1[4], bar2, rd1, f1[4]; };
constexpr Sch sch_of(int KL) {
  return KL >= 14 ? Sch{{0, 1, 2, 3}, {4, 5, 6, 7}, 9, {9, 9, 9, 10, 10, 10, 11, 11}, {12, 13, 14, 19, 21, 22, 23, 24}, {15, 16, 17, 18}, 20, 20, {25, 26, 27, 27}}
                  : Sch{{0, 0, 1, 1}, {2, 2, 3, 3}, 5, {5, 5, 5, 5, 6, 6, 6, 6}, {7, 7, 8, 8, 11, 12, 12, 13}, {9, 9, 10, 10}, 11, 11, {14, 14, 15, 15}};
}
struct FrO {
  static constexpr int NS = 16, KL = 8;
  static constexpr bool own(int i) { return i >= 8; }
  static constexpr int ks(int i) { return i & 7; }
  static constexpr u32 off(int i) { return (i & 7) * 1024; }
  static constexpr int lstep(int q, int win) { return q - win; }
  static constexpr int first(int win) { return win; }
};
struct FrH {
  static constexpr int NS = 56, KL = 14;
  static constexpr int part(int i) { return i < 28 ? i / 14 : 2 + ((i / 7) & 1); }
  static constexpr bool own(int i) { return i < 28 ? ((i / 7) & 1) : i >= 42; }
  static constexpr int ks(int i) { return part(i) * 7 + i % 7; }
  static constexpr u32 off(int i) { return part(i) * 65536 + (i % 7) * 1024; }
  static constexpr int lstep(int q, int win) {
    const int g = T3CHK - 21;
    return q < 35 ? q - 14 : q < 42 ? (q - 14 < T3CHK ? T3CHK : q - 14) : q < 42 + g ? q - 21 : q - 14 - g;
  }
  static constexpr int first(int win) { return 14; }
};
template <class Fr> DI v4u ld_fr(rsrc_t rs, u32 l16, u32 a_own, u32 a_oth, int i) { return ldf<SC1>(rs, l16, Fr::own(i) ? a_own : a_oth, Fr::off(i)); }
template <class Fr, int WIN, class Win, class Pre, class Mid>
DI void splitk_phase(rsrc_t rs, const v4u (&wf)[Fr::NS / 2][2], u32 a_own, u32 a_oth, LAS unsigned char* L, int w, u32 l16, float scale,
                     v4u (&res)[2], Win&& win, Pre&& pre_loop, Mid&& mid) {
  constexpr int NS = Fr::NS, KP = Fr::KL, E0 = NS - KP, F0 = Fr::first(WIN);
  constexpr Sch sc = sch_of(KP);
  static_assert(WIN >= KP, "window");
  v4u af[WIN];
  auto ld = [&](int i) { return ld_fr<Fr>(rs, l16, a_own, a_oth, i); };
#pragma unroll
  for (int i = 0; i < F0; ++i) { win(i); af[i] = ld(i); SB(); }
  LAS unsigned char* r2 = L + opqv(RED_OFF + l16 + (w ^ 2) * 8192);
  LAS unsigned char* r3 = L + opqv(RED_OFF + l16 + (w ^ 3) * 8192 + 4096);
  LAS unsigned char* r0 = L + opqv(RED_OFF + l16 + w * 8192);
  LAS unsigned char* r1 = L + opqv(RED_OFF + l16 + (w ^ 1) * 8192);
  pre_loop();
  SB();
  f32x16 a0, a1, a2, a3;
  constexpr bool WA = NS > 16;
  constexpr auto fst = [](int i) { for (int q = 0; q < i; ++q) if (Fr::own(q) == Fr::own(i)) return false; return true; };
  float u[16];
  static_for<E0>([&](auto I) {
    constexpr int i = I, ks = Fr::ks(i), e = i - (E0 - 4);
    const v4u bb = af[i % WIN];
    if constexpr (!Fr::own(i)) vmfma<fst(i), WA>(a2, wf[ks][0], bb);
    else vmfma<fst(i), WA>(a1, wf[ks][1], bb);
    SB();
    mid(I);
    static_for<NS - F0>([&](auto Q) { if constexpr (Fr::lstep(Q + F0, WIN) == i) { af[(Q + F0) % WIN] = ld(Q + F0); SB(); } });
    SB();
    if constexpr (!Fr::own(i)) vmfma<fst(i), WA>(a3, wf[ks][1], bb);
    else vmfma<fst(i), WA>(a0, wf[ks][0], bb);
    SB();
    if constexpr (e >= 0) {
      const u32 ra = res[e >> 1][2 * (e & 1)], rb = res[e >> 1][2 * (e & 1) + 1];
      u[4 * e] = bflo(ra); u[4 * e + 1] = bfhi(ra); u[4 * e + 2] = bflo(rb); u[4 * e + 3] = bfhi(rb);
#pragma unroll
      for (int m = 0; m < 4; ++m) PIN(u[4 * e + m]);
    }
    SB();
  });
  f32x4 p[8];
  auto acc4 = [&](int q, const f32x4& x) {
#pragma unroll
    for (int m = 0; m < 4; ++m) { const float xv = x[m]; u[4 * q + m] = __builtin_fmaf(xv, scale, u[4 * q + m]); PIN(u[4 * q + m]); }
  };
  static_for<2 * KP>([&](auto G) {
    constexpr int g = G, k = g % KP, ks = Fr::ks(E0 + k);
    if constexpr (g < KP) vmfma<fst(E0 + k), WA>(a1, wf[ks][1], af[(E0 + k) % WIN]);
    else vmfma<fst(E0 + k), WA>(a0, wf[ks][0], af[(E0 + k) % WIN]);
    SB();
    if constexpr (g == sc.bar1 || g == sc.bar2) BAR();
    static_for<8>([&](auto Q) { if constexpr (sc.rd[Q] == g) p[Q] = *(LAS f32x4*)(r0 + Q * 1024); });
    if constexpr (g == sc.rd1) static_for<4>([&](auto Q) { p[Q] = *(LAS f32x4*)(r1 + Q * 1024); });
    SB();
    if constexpr (g == sc.put2[0]) settle<SE2>(a2);
    if constexpr (g == sc.put1[0]) settle<(WA ? SE1H : SE1O)>(a1);
    static_for<4>([&](auto Q) { if constexpr (sc.put2[Q] == g) lds_put1(r2, a2, Q); });
    static_for<4>([&](auto Q) { if constexpr (sc.put3[Q] == g) lds_put1(r3, a3, Q); });
    static_for<4>([&](auto Q) { if constexpr (sc.put1[Q] == g) lds_put1(r0, a1, Q); });
    SB();
    static_for<8>([&](auto Q) { if constexpr (sc.fu[Q] == g) acc4(Q & 3, p[Q]); });
    static_for<4>([&](auto Q) { if constexpr (sc.f1[Q] == g) acc4(Q, p[Q]); });
    SB();
    mid(std::integral_constant<int, E0 + g>{});
    SB();
  });
  settle<SE0>(a0);
#pragma unroll
  for (int q = 0; q < 4; ++q) {
    acc4(q, f32x4{a0[4 * q], a0[4 * q + 1], a0[4 * q + 2], a0[4 * q + 3]});
    res[q >> 1][2 * (q & 1)] = pk(u[4 * q], u[4 * q + 1]);
    res[q >> 1][2 * (q & 1) + 1] = pk(u[4 * q + 2], u[4 * q + 3]);
  }
}
DI void resid_store(rsrc_t rs, const v4u (&res)[2], u32 l16, u32 sto, u32 xoff, u32 soff) {
#pragma unroll
  for (int s = 0; s < 2; ++s) st128<SC1>(res[s], rs, l16 + s * 1024, xoff);
  SB();
  float s0 = 0.f, q0 = 0.f, s1 = 0.f, q1 = 0.f;
#pragma unroll
  for (int m = 0; m < 4; ++m) {
    s0 = dot2(res[0][m], ONES2, s0); q0 = dot2(res[0][m], res[0][m], q0);
    s1 = dot2(res[1][m], ONES2, s1); q1 = dot2(res[1][m], res[1][m], q1);
  }
  st64<SC1>(__builtin_bit_cast(v2u, f32x2{sum32(s0 + s1), sum32(q0 + q1)}), rs, sto, soff);
  asm volatile("" ::: "memory");
  SB();
}

DI u32 cs_word(float part, u32 hf) {
  const float cs = sum32(part);
  const u32 H = pk(cs, cs);
  const float rem = cs - bflo(H);
  return hf ? pk(rem, rem) : H;
}
DI u32 mean_word(float nmean) { return pk(nmean, nmean - bflo(pk(nmean, nmean))); }
template <int I, bool FIRST = false> DI void mean_fix(f32x16& c, const v4u& cs4, u32 mw) {
  v4u bm = {0u, 0u, 0u, 0u};
  bm[I] = mw;
  vmfma<FIRST, true, true>(c, cs4, bm);
}
struct Stats { float t[4], s, q, nmean, rstd; u32 mw; };
template <int I> DI void stats_step(Stats& z, const f32x4 (&sp)[4]) {
  if constexpr (I < 3) {
#pragma unroll
    for (int m = 0; m < 4; ++m) { const float a = I ? z.t[m] : sp[0][m], c = sp[I + 1][m]; z.t[m] = a + c; PIN(z.t[m]); }
  } else if constexpr (I == 3) { z.s = mix16(z.t[0], z.t[2]); PIN(z.s); z.q = mix16(z.t[1], z.t[3]); PIN(z.q); }
  else if constexpr (I == 4) { z.nmean = sum32(z.s) * (-1.f / 512.f); PIN(z.nmean); }
  else if constexpr (I == 5) { z.q = sum32(z.q) * (1.f / 512.f); PIN(z.q); }
  else if constexpr (I == 6) { z.mw = mean_word(z.nmean); PIN(z.mw); }
  else if constexpr (I == 7) { z.rstd = __builtin_amdgcn_rsqf(fmaxf(z.q - z.nmean * z.nmean, 0.f) + 1e-5f); PIN(z.rstd); }
}

template <class Hook> DI void dual_softmax(const f32x16& p, float c, u32 (&o)[16], Hook&& hook) {
  float mx = p[0], mn = p[0];
#pragma unroll
  for (int i = 1; i < 16; ++i) { mx = fmaxf(mx, p[i]); mn = fminf(mn, p[i]); }
  float ap = -mx * c, an = mn * c;
  PIN(ap);
  PIN(an);
  SB();
  hook(std::integral_constant<int, 0>{});
  SB();
  f32x2 ep[8], en[8];
#pragma unroll
  for (int i = 0; i < 8; ++i) {
    const f32x2 pv = {p[2 * i], p[2 * i + 1]};
    ep[i] = pv * c + ap;
    en[i] = pv * (-c) + an;
    PIN(ep[i]);
    PIN(en[i]);
  }
  SB();
  hook(std::integral_constant<int, 1>{});
  SB();
  f32x2 sa, sc;
  static_for<8>([&](auto I) {
    constexpr int i = I;
    ep[i] = f32x2{__builtin_amdgcn_exp2f(ep[i].x), __builtin_amdgcn_exp2f(ep[i].y)};
    en[i] = f32x2{__builtin_amdgcn_exp2f(en[i].x), __builtin_amdgcn_exp2f(en[i].y)};
    PIN(ep[i]);
    PIN(en[i]);
    if constexpr (i == 2) { sa = ep[0] + ep[1]; sc = en[0] + en[1]; PIN(sa); PIN(sc); }
    if constexpr (i > 2) { sa += ep[i - 1]; sc += en[i - 1]; PIN(sa); PIN(sc); }
    SB();
    hook(std::integral_constant<int, 2 + i>{});
    SB();
  });
  sa += ep[7];
  sc += en[7];
  const float rp = __builtin_amdgcn_rcpf(sa.x + sa.y), rn = __builtin_amdgcn_rcpf(sc.x + sc.y);
#pragma unroll
  for (int i = 0; i < 8; ++i) { ep[i] *= rp; PIN(ep[i]); }
#pragma unroll
  for (int i = 0; i < 8; ++i) { en[i] *= rn; PIN(en[i]); }
  SB();
  hook(std::integral_constant<int, 10>{});
  SB();
#pragma unroll
  for (int i = 0; i < 8; ++i) { o[i] = pk(ep[i].x, ep[i].y); o[8 + i] = pk(en[i].x, en[i].y); }
}
constexpr int p3_g0(int v) { return (v * 58) / 48; }
constexpr int W2B = 56 - W2D - 12;
constexpr bool p3_w2gap(int gi) { return gi < W2S && (gi * W2B) / W2S != ((gi + 1) * W2B) / W2S; }
constexpr int p3_pre(int layer, int g) {
  int n = layer == 0 && p3_w2gap(g);
  for (int q = 0; q < 6; ++q) n += p3_g0(8 * q + 7) + 5 == g;
  return n;
}
constexpr int p3_post(int g) { return (g == HS0) + (g == HS1) + (g == PK0); }
constexpr int p3_younger(int layer, int t, int gw) {
  const int gs = p3_g0(16 * t + 15) + 5;
  int n = p3_post(gs);
  for (int g = gs + 1; g < gw; ++g) n += p3_pre(layer, g) + p3_post(g);
  return n + (gw < 64 ? p3_pre(layer, gw) : 0);
}
static_assert(p3_g0(32) >= 35 && p3_g0(47) + 5 < 64 && p3_g0(15) + 5 < HS0 && p3_g0(31) + 5 < HS1 && HS0 != HS1, "P3 schedule");

__global__ void __launch_bounds__(256, 1)
k_main(const uint16_t* __restrict__ x, const uint16_t* __restrict__ wqkv, const uint16_t* __restrict__ wphi,
       const uint16_t* __restrict__ wo_, const uint16_t* __restrict__ w1, const uint16_t* __restrict__ w2,
       unsigned char* ws, unsigned char* out) {
  u32 cpf;
  {
    unsigned long long pc;
    u32 o = threadIdx.x * 128 + (blockIdx.x & 8) * 4096;
    asm volatile("s_getpc_b64 %1\n.Lkcode0:\n\tv_min_u32 %2, .Lkcode1-.Lkcode0, %2\n\tglobal_load_dword %0, %2, %1 ; APEEK" : "=v"(cpf), "=&s"(pc), "+v"(o)::"memory");
  }
  SB();
  unsigned long long px = (unsigned long long)x, pq = (unsigned long long)wqkv, po = (unsigned long long)wo_, p1 = (unsigned long long)w1, p2 = (unsigned long long)w2;
  asm volatile("" : "+s"(px));
  asm volatile("" : "+s"(ws));
  asm volatile("" : "+s"(out));
  asm volatile("" : "+s"(wphi));
  LAS unsigned char* L = (LAS unsigned char*)smem;
  const int b = blockIdx.x, tid = threadIdx.x, lane = tid & 63, w = __builtin_amdgcn_readfirstlane(tid >> 6);
  const int xcd = b & 7, j = b >> 3;
  const int rt = 4 * (xcd >> 1) + (j >> 3), hp = 8 * (xcd & 1) + (j & 7), tt = 4 * rt + w;
  const bool p3on = (j & 7) < 7;
  const int fb = p3on ? 7 * (xcd & 1) + (j & 7) : 0;
  const int rt2 = 4 * xcd + (j >> 3), cb = j & 7;
  const int tte = 2 * rt2 + (w >> 1), n0 = 64 * cb + 32 * (w & 1);
  const rsrc_t rs = mkrsrc((unsigned char*)(((size_t)ws + MB2 - 1) & ~(size_t)(MB2 - 1)), WS_END);
  const f32x16 zero16 = {0.f, 0.f, 0.f, 0.f, 0.f, 0.f, 0.f, 0.f, 0.f, 0.f, 0.f, 0.f, 0.f, 0.f, 0.f, 0.f};
  v4u res[2];
  v4u csq = {0u, 0u, 0u, 0u}, cs1;
  v4u wph[2];
  u32 tag_lo, tag_hi, tws = 0;
  u32 sd;
  const u32 l0 = lane0_off();
  const u32 nlh = !p3on ? 0u : (fb == 3 || fb == 10) ? 2u : 1u;
  v4u xr[32];
  const rsrc_t rsx = mkrsrc((const void*)px, S * D * 2);
  auto trfrag = [&](LAS unsigned char* p, int str) { const v2u t0 = trread(p), t1 = trread(p + str); return v4u{t0.x, t0.y, t1.x, t1.y}; };
  const u32 sv = ((lane >> 2) & 1) == ((lane >> 4) & 1) ? ONES2 : 0u;
  v4u selm = {sv, sv, sv, sv};
  const u32 u2 = opq(2 * b + (w & 1));
  const bool k0 = w == 0, k1 = b < 112, k3 = b >= 144, kon = k0 ? b < 192 : w >= 2;
  auto pick = [&](auto a0, auto a1, auto a2, auto a3) {
    asm volatile("" : "+s"(a0));
    asm volatile("" : "+s"(a1));
    asm volatile("" : "+s"(a2));
    asm volatile("" : "+s"(a3));
    auto r = k3 ? a3 : a2;
    r = k1 ? a1 : r;
    r = k0 ? a0 : r;
    asm volatile("" : "+s"(r));
    return r;
  };
  const u32 n3 = opq(opq(u2 - 288) * 2341) >> 16;
  const u32 ub = opq(k0 ? b : k1 ? u2 : u2 - 224), q3 = opq(u2 - 288 - 28 * n3);
  const u32 nb = opq(!k0 && k3 ? n3 : ub >> 3), kq = opq(!k0 && k3 ? q3 : ub & 7), ld2 = opq(k0 ? 3072u : k1 ? 3584u : 1024u);
  LAS unsigned char* img = L + SCR + w * 8192;
  const rsrc_t rsu = mkrsrc((const void*)pick(pq, p1, po, p2), opq(kon ? (k0 || k1 || !k3 ? 512u : 1792u) : 0u) * ld2);
  const u32 vu = (lane >> 3) * ld2 + (((lane & 7) ^ ((lane >> 2) & 4)) * 16);
  auto uld = [&](int i) { __builtin_amdgcn_raw_ptr_buffer_load_lds(rsu, (LAS u32*)(img + i * 1024), 16, vu, (64 * kq + 8 * i) * ld2 + nb * 128, 0, 0); };
  if (k0) {
#pragma unroll
    for (int i = 0; i < 8; ++i) uld(i);
  }
  SB();
  if (b >= 248 && w < 2) {
#pragma unroll 1
    for (u32 i = 0; i < NCNT * CSTR / 16; i += 1024) st128<SC1>(v4u{0u, 0u, 0u, 0u}, rs, lane * 16, CTRL + ((b - 248) * 2 + w) * (NCNT * CSTR / 16) + i);
  }
  const v2u ep = ld64<SC1>(rs, 0, EPOCH_OFF);
  SB();
  const u32 vx = (lane >> 3) * 1024 + (lane & 7) * 16;
  const u32 rot = j & 7;
  auto xld = [&](auto I) { constexpr int i = I; xr[i] = ld128<0>(rsx, vx, tt * 32768 + (i & 3) * 8192 + (((i >> 2) ^ rot) * 128)); SB(); };
  static_for<XA>([&](auto I) { xld(I); });
  u32 obase, cstr, kstr, kstr2, csb, ovo, cvo, ua;
  {
    const u32 t32 = 2 * nb, tile = (0x12u >> ((nb >> 3) * 2)) & 3;
    obase = opq(pick(WQKV_T + (t32 & 15) * 98304 + tile * 32768 + kq * 4096, W1_T + t32 * 32768 + kq * 4096, WO_T + (nb * 32 + kq * 4) * 2048,
                     W2_T + (nb * 112 + (kq & 1) * 56 + (kq >> 1) * 2) * 2048));
    cstr = opq(pick(98304u, 32768u, 1024u, 1024u));
    kstr = opq(pick(1024u, 1024u, 2048u, 2048u));
    kstr2 = opq(pick(2048u, 2048u, 4096u, 57344u));
    csb = opq(pick(CS_QKV + (((t32 & 15) * 3 + tile) * 8 + kq) * 128, CS_W1 + (t32 * 8 + kq) * 128, 0u, 0u));
    ovo = opqv(lane * 16 | (kon ? 0u : 0x80000000u));
    cvo = opqv(((lane >> 5) * (k0 ? 3072 : 1024) + (lane & 31) * 4) | (kon && (k0 || k1) ? 0u : 0x80000000u));
    const u32 c16 = lane & 15, Q = ((lane >> 2) & 4) + (lane & 3);
    const u32 Cc = ((k0 ? 0x73625140u : 0x75643120u) >> (4 * Q)) & 7;
    ua = opqv((8 * (lane >> 5) + (c16 >> 2)) * 128 + (c16 >> 3) * 64 + Cc * 8);
  }
  const u32 jadd = opq(cnt_off(C_J + pick(0u, 16 + (nb >> 1), nb, nb)));
  auto unit = [&]() {
    v4u o[8];
#pragma unroll
    for (int i = 0; i < 8; ++i) o[i] = trfrag(img + (ua ^ ((i >> 2) * 64)) + (i & 3) * 2048, 512);
    SB();
    f32x4 cs[2] = {{0.f, 0.f, 0.f, 0.f}, {0.f, 0.f, 0.f, 0.f}};
#pragma unroll
    for (int i = 0; i < 8; ++i) {
      st128<SC1>(o[i], rs, ovo, obase + (i >> 2) * cstr + ((i >> 1) & 1) * kstr2 + (i & 1) * kstr);
      cs[i >> 2] = mfma16(selm, o[i], cs[i >> 2]);
    }
    const float c0 = cs[0][0], c1 = cs[1][0];
    __builtin_amdgcn_raw_buffer_store_b32(__builtin_bit_cast(u32, lane < 32 ? c0 : c1), rs, cvo, csb, SC1);
    SB();
  };
  SB();
  const u32 tvo = opqv((((b >= 248 && w < 2) || (k0 && kon)) && lane < 16) ? lane * 8 : 0x80000000u);
  const u32 tso = opq(b >= 248 ? INIT_OFF + (((b & 7) * 2 + w) & 15) * CSTR : FLAG_OFF + b * CSTR);
  SB();
  v4u w2f[28][2];

  static_for<2>([&](auto LAYER) __attribute__((always_inline)) {
    constexpr int layer = LAYER;
    v4u wo[8][2];
    const u32 wb = opq(W2_T + cb * 229376 + w * 14336);
    const u32 wsw = (w & 1) * 1024;
    u32 pvy[2];
#define IC(n) std::integral_constant<int, n>{}
    {
      const u32 ln = opqv(lane), l16 = ln * 16, r = ln & 31, hf = ln >> 5;
      constexpr int DX = DXV;
      v4u xf[32];
      auto xtile = [&](int i, u32 t) { return XT + i * MB2 + (t & 31) * 65536 + (sd ^ ((i & 1) ? 0 : 8192)); };
      auto stile = [&](int i, u32 t) { return ST + i * ST_SZ + blk((t & 31) * 4096) + sd; };
      const u32 xb = layer ? xtile(1, tt) : 0;
      SB();
      f32x4 sp[4];
      const u32 stb = layer ? stile(1, tt) : 0;
      const u32 wqb = opq(WQKV_T + hp * 98304 + 65536 + w * 8192);
      const u32 g4 = ln >> 4, c16 = ln & 15;
      LAS unsigned char* a_v = L + opqv(VV_OFF + (32 * w + r) * VV_STR + hf * 32);
      LAS unsigned char* a_pk = L + opqv(PK_OFF + (32 * w + r) * PK_STR + hf * 64);
      LAS unsigned char* a_pa = L + opqv(PK_OFF + (8 * g4 + (c16 >> 2)) * PK_STR + w * 32 + (ln & 3) * 8);
      LAS unsigned char* a_pb = L + opqv(VV_OFF + (8 * g4 + (c16 >> 2)) * VV_STR + (w >> 1) * 32 + (ln & 3) * 8);
      const u32 o_kv = opqv((g4 >> 1) * 256 + c16 * 16 + (g4 & 1) * 8), o_z = opqv(hf * 8192 + r * 4);
      u32 jv = 0, lat = 0, vv = 0;
      constexpr int wok = layer ? WOK : 0, NPF = 16 - wok + (layer ? 24 : 32);
      constexpr int s_q1 = 0, s_q2 = s_q1 + NQ1, s_od = s_q2 + NQ2, s_op = s_od + ODR, n_op = layer ? OPL1 : OPL0, s_p2 = s_op + n_op, s_xd = NPF - XDR;
      static_assert(s_p2 <= s_xd && s_xd - s_p2 <= 12 && s_od <= NPF - 8 && s_op >= 16 - wok && (layer || s_op <= 32), "prefetch schedule");
      LAS v4u* WL = (LAS v4u*)(L + l16);
      LAS v4u* WQ = (LAS v4u*)(L + opqv(l16 + 65536));
      const u32 wob = opq(WO_T + (cb * 4 + w) * 16384);
      auto wold = [&](auto I) {
        constexpr int i = I;
        wo[i >> 1][i & 1] = ld128<SC1>(rs, l16 + ((i >> 1) & 1) * 2048, wob + (i >> 2) * 4096 + ((i & 1) ? 1024 - wsw : wsw));
      };
      f32x16 aK, aV, aQ;
      float rstd, c;
      Stats zs_;
      const u32 jvo = opqv(ln == 0 ? cnt_off(C_J + cb) : ln == 1 ? cnt_off(C_J + 16 + fb) : 0x80000000u);
      float cp[3][4];
      v4u wk[RA + 1], wv[RA + 1];
      SB();
      if constexpr (layer == 0) {
        LAS unsigned char* sw[2];
        LAS unsigned char* sr[4];
#pragma unroll
        for (int i = 0; i < 2; ++i) sw[i] = L + opqv(XS_OFF + w * 8192 + (ln >> 3) * 128 + (((ln & 7) ^ (4 * i + (ln >> 4))) * 16));
#pragma unroll
        for (int f = 0; f < 4; ++f) sr[f] = L + opqv(XS_OFF + w * 8192 + r * 128 + (((2 * f + hf) ^ ((r >> 1) & 7)) * 16));
        const u32 fu = ln >> 1, fvo = opqv(ln >= 12 ? 0x80000000u : FLAG_OFF + ((fu < 4 ? (1 + (w >> 1)) * 64 + (w & 1) * 4 + fu : 2 * w + fu - 4) + (hp >> 1) * 8) * CSTR + (ln & 1) * 4);
        PIN(selm);
        SB();
        if (w == 0) {
          asm volatile("s_waitcnt vmcnt(%0)" ::"n"(XA + 1) : "memory");
          unit();
        }
        asm volatile("s_barrier" ::: "memory");
        {
          const unsigned long long e = ((unsigned long long)__builtin_amdgcn_readfirstlane(ep.y) << 32) | __builtin_amdgcn_readfirstlane(ep.x);
          unsigned long long t = e * 0x9E3779B97F4A7C15ull + 0x632BE59BD9B4E019ull;
          if (t == e) t = ~e;
          tag_lo = (u32)t;
          tag_hi = (u32)(t >> 32);
        }
        const u32 fneed = (ln & 1) ? tag_hi : tag_lo;
        f32x4 sa = {0.f, 0.f, 0.f, 0.f};
        f32x16 gr = zero16;
        u32 fv;
        v2u tg;
        SB();
        auto vm0 = [&](auto N) {
          constexpr int n = N, xi = XA + n, di = n - XC - 8 * FCK;
          if constexpr (xi < 32) xld(IC(xi));
          if constexpr (xi == 32) __builtin_amdgcn_raw_ptr_buffer_load_lds(mkrsrc(wphi, 16384), (LAS u32*)(L + XS_OFF + w * 8192 + 4096), 16, l16, hp * 1024, 0, 0);
          if constexpr (xi == 33) tws = ld32<0>(rs, (ln + 64 * w) * TLBW, 0);
          if constexpr (xi == ULP) {
            if (w >= 2) {
#pragma unroll
              for (int i = 0; i < 8; ++i) uld(i);
            }
          }
          if constexpr (xi == XB) {
            if (w == 0) asm volatile("s_waitcnt vmcnt(%0)" ::"n"(XB - XA + 1) : "memory");
            st64<SC1>(v2u{tag_lo, tag_hi}, rs, tvo, tso);
          }
          if constexpr (di >= 0 && di < 16)
            dma1<(di & 3)>(rs, l16, WQKV_T + hp * 98304 + w * 16384 + (di >> 2) * 4096, L + (w >> 1) * 32768 + ((((w & 1) * 4 + (di >> 2)) ^ rot) * 4096));
          if constexpr (di == 16) tg = ld64<SC1>(rs, c16 * CSTR, INIT_OFF);
          SB();
        };
        auto vm = [&](auto N) { vm0(IC(N + XC)); };
        static_assert(XA + XC >= 4 && XA + XC + 8 * FCK > 33 && XB < 32 && FCK <= 7 && UB >= FCK + 3 && ULP < XA + 8 * FCK && ULP >= XA, "start schedule");
        static_for<XC>([&](auto I) { vm0(I); });
        static_for<11>([&](auto B) {
          constexpr int blk = B;
          if constexpr (blk == UB) {
            if (w >= 2) {
              asm volatile("s_waitcnt vmcnt(17)" ::: "memory");
              unit();
            }
          }
          if constexpr (blk == FPK) { fv = peek(rs, fvo, 0); SB(); }
          if constexpr (blk == FCK) {
            if ((__builtin_amdgcn_ballot_w64(fv == fneed) & 0xfffull) != 0xfffull) {
#pragma unroll 1
              for (u32 spins = 0; spins < (1u << 16); ++spins)
                if ((__builtin_amdgcn_ballot_w64(peek(rs, fvo, 0) == fneed) & 0xfffull) == 0xfffull) break;
            }
            asm volatile("" ::: "memory");
          }
          if constexpr (blk < 10) static_for<4>([&](auto I) {
            constexpr int i = I;
            if constexpr (blk > 1) gr = mfma32(xf[4 * blk - 8 + i], xf[4 * blk - 8 + i], gr);
            SB();
            vm(IC(8 * blk + 2 * i));
            if constexpr (blk < 8) *(LAS v4u*)(sw[i & 1] + i * 1024) = xr[4 * blk + i];
            SB();
            if constexpr (blk > 1) { sa = mfma16(selm, xf[4 * blk - 8 + i], sa); asm volatile("" : "+a"(sa)); }
            SB();
            vm(IC(8 * blk + 2 * i + 1));
          });
          if constexpr (blk < 8) static_for<4>([&](auto F) { xf[4 * blk + F] = *(LAS v4u*)(sr[F]); SB(); });
        });
        asm volatile("" ::"v"(tws));
        asm volatile("; PRETIRE %0" ::"v"(cpf));
        {
          const u32 Qc = 4 * (g4 & 1) + (c16 & 3);
          LAS unsigned char* a_ph = L + opqv(XS_OFF + w * 8192 + 4096 + hf * 512 + (c16 >> 2) * 32 + (Qc >> 1) * 8);
#pragma unroll
          for (int s = 0; s < 2; ++s) wph[s] = trfrag(a_ph + s * 256, 128);
        }
        const float sa0 = sa[0];
        const float nmean = sa0 * (-1.f / 512.f);
        zs_.mw = mean_word(nmean);
        auto sel = [](bool cnd, float a0, float a1) { PIN(a0); PIN(a1); return cnd ? a1 : a0; };
        auto g4f = [&](int i) { return sel(r & 2, sel(r & 1, gr[4 * i], gr[4 * i + 1]), sel(r & 1, gr[4 * i + 2], gr[4 * i + 3])); };
        const float gd = sel(r & 16, sel(r & 8, g4f(0), g4f(1)), sel(r & 8, g4f(2), g4f(3)));
        const bool dl = ((r >> 2) & 1) == hf;
        const float dg = dl ? gd : 0.f;
        rstd = __builtin_amdgcn_rsqf(fmaxf(sum32(dg) * (1.f / 512.f) - nmean * nmean, 0.f) + 1e-5f);
        c = rstd * LOG2E;
#pragma unroll
        for (int s = 0; s < 2; ++s)
#pragma unroll
          for (int m = 0; m < 4; ++m) wph[s][m] = dl ? wph[s][m] : 0u;
        PIN(wph[0]);
        PIN(wph[1]);
        PIN(c);
        PIN(rstd);
        PIN(zs_.mw);
        SB();
        if (w >= 2) asm volatile("s_waitcnt vmcnt(9)" ::: "memory");
        else DRAIN();
        if (w == 0 && !__all(tg.x == tag_lo && tg.y == tag_hi)) {
#pragma unroll 1
          for (u32 spins = 0; spins < (1u << 16); ++spins) {
            asm volatile("" ::: "memory");
            const v2u t = ld64<SC1>(rs, c16 * CSTR, INIT_OFF);
            if (__all(t.x == tag_lo && t.y == tag_hi)) break;
          }
        }
        BAR();
#pragma unroll
        for (int i = 0; i < RA; ++i) { wk[i] = WL[i * 64]; wv[i] = WL[(32 + i) * 64]; }
        SB();
      } else {
        const u32 cx = cnt_off(C_X + (tt & 31)) + sd;
        NAP(PS3);
        apeek(rs, l0, cx, pvy[0]);
        NAP(PS5);
        apeek(rs, l0, cx, pvy[1]);
        asm volatile("s_waitcnt vmcnt(3)" ::: "memory");
        BAR();
#pragma unroll
        for (int i = 0; i < RA; ++i) { wk[i] = WL[i * 64]; wv[i] = WL[(32 + i) * 64]; }
        SB();
        pfinish2<1, 0>(rs, l0, cx, 16, pvy);
#pragma unroll
        for (int ks = 0; ks < DX; ++ks) { xf[ks] = ldr(rs, l16, xb, ks); SB(); }
      }
      static_for<32>([&](auto KSI) {
        constexpr int ks = KSI, q0 = layer ? 20 : 8;
        vmfma<ks == 0, false>(aK, wk[ks % (RA + 1)], xf[ks]);
        SB();
        if constexpr (ks + RA < 32) wk[(ks + RA) % (RA + 1)] = WL[(ks + RA) * 64];
        SB();
        if constexpr (layer > 0 && ks + DX < 32) xf[ks + DX] = ldr(rs, l16, xb, ks + DX);
        if constexpr (ks >= q0 && ks < q0 + 8) dma1<((ks - q0) & 3)>(rs, l16, wqb + ((ks - q0) >> 2) * 4096, L + 65536 + (((2 * w + ((ks - q0) >> 2)) ^ (layer ? 0 : rot)) * 4096));
        if constexpr (ks >= 28 && ks < 28 + wok) wold(IC(ks - 28));
        if constexpr (layer == 0 && ks < 6) cp[ks >> 1][2 * (ks & 1)] = __builtin_bit_cast(float, ld32<SC1>(rs, ln * 4 + (ks & 1) * 512 + (ks >> 1) * 1024, CS_QKV + hp * 3072));
        SB();
        vmfma<ks == 0, false>(aV, wv[ks % (RA + 1)], xf[ks]);
        SB();
        if constexpr (ks + RA < 32) wv[(ks + RA) % (RA + 1)] = WL[(32 + ks + RA) * 64];
        SB();
        if constexpr (layer == 0) {
          if constexpr (ks < 6) cp[ks >> 1][2 * (ks & 1) + 1] = __builtin_bit_cast(float, ld32<SC1>(rs, ln * 4 + (ks & 1) * 512 + 256 + (ks >> 1) * 1024, CS_QKV + hp * 3072));
          if constexpr (ks == 18 || ks == 22) res[(ks - 18) >> 2] = ld128<0>(rsx, r * 1024 + hf * 16 + ((ks - 18) >> 2) * 32, tte * 32768 + n0 * 2);
          if constexpr (ks == 19 || ks == 21 || ks == 23) { constexpr int t = (ks - 19) / 2; cp[t][0] = (cp[t][0] + cp[t][1]) + (cp[t][2] + cp[t][3]); PIN(cp[t][0]); }
          if constexpr (ks == 20 || ks == 22 || ks == 24) { constexpr int t = (ks - 20) / 2; u32 cw = cs_word(cp[t][0], hf); PIN(cw); csq[t] = cw; }
          if constexpr (ks == 19) wsignal<21>(rs, w >= 2 ? l0 : 0x80000000u, jadd);
        } else {
          if constexpr (ks < 4) sp[ks] = __builtin_bit_cast(f32x4, ld128<SC1>(rs, l16 + ks * 1024, stb));
          if constexpr (ks == 1) pretire(pvy);
          if constexpr (ks >= 16 && ks < 24) stats_step<ks - 16>(zs_, sp);
          if constexpr (ks == 24) { rstd = zs_.rstd; c = rstd * LOG2E; PIN(c); }
        }
        SB();
        if constexpr (ks == 25) mean_fix<0>(aK, csq, zs_.mw);
        if constexpr (ks == 27) mean_fix<1>(aV, csq, zs_.mw);
        SB();
      });
      const u32 w1b = opq(W1_T + fb * 131072);
      auto w1dma = [&](auto I) {
        constexpr int i = I;
        const u32 o = i < 16 ? w * 16384 + (i >> 2) * 4096 : (i < 24 ? 98304 : 65536) + w * 8192 + ((i >> 2) & 1) * 4096;
        dma1<(i & 3)>(rs, l16, w1b + o, L + o);
      };
      auto pf = [&](auto N) {
        constexpr int n = N + wok;
        if constexpr (n < 16) wold(IC(n));
        else w1dma(IC(n - 16 + (layer && n >= 32 ? 8 : 0)));
        SB();
      };
      {
        settle<SEK>(aK);
        const v4u k0 = pack8(aK, 0), k1 = pack8(aK, 1);
        f32x16 p;
        vmfma<true, true, true>(p, wph[0], k0);
        vmfma<false, true, true>(p, wph[1], k1);
        SB();
#pragma unroll
        for (int i = 0; i < 16; ++i) { aV[i] *= rstd; PIN(aV[i]); }
        SB();
        *(LAS v4u*)(a_v) = pack8(aV, 0);
        *(LAS v4u*)(a_v + 16) = pack8(aV, 1);
        SB();
        if constexpr (layer == 0) { vv = peek(rs, l0, cnt_off(C_H + (b >> 5) * 128 + (w & 1) * 64 + (b & 31) * 2 + (w >> 1))); SB(); }
        u32 o[16];
        auto look = [&](auto I) {
          if constexpr (layer == 0) {
            u32 t;
            if constexpr (I >= LK0) asm volatile("s_getreg_b32 %1, hwreg(HW_REG_IB_STS, 0, 4)\n\ts_cmp_gt_u32 %1, %2\n\ts_addc_u32 %0, %0, 0" : "+s"(lat), "=&s"(t) : "n"(I > JPK ? 1 : 0) : "scc");
            if constexpr (I == JPK) jv = peek(rs, w != 0 ? 0x80000000u : jvo, 0);
          }
        };
        dual_softmax(p, c, o, look);
        static_for<4>([&](auto I) {
          constexpr int i = I;
          *(LAS v4u*)(a_pk + i * 16) = v4u{o[4 * i], o[4 * i + 1], o[4 * i + 2], o[4 * i + 3]};
          if constexpr (layer == 0 && (i & 1)) { SB(); look(IC(11)); SB(); }
        });
        if constexpr (layer == 0) *(LAS u32*)(L + opqv(XS_OFF + w * 4)) = lat;
      }
      SB();
      mean_fix<2, true>(aQ, csq, zs_.mw);
      asm volatile("s_waitcnt vmcnt(%0)" ::"n"(wok) : "memory");
      if constexpr (layer == 0) {
        asm volatile("" ::"v"(vv));
        const u32 need = ln == 0 ? 36u : ln == 1 ? 16u : 0u;
        if (w == 0 && (__builtin_amdgcn_ballot_w64(jv >= need) & 7ull) != 7ull) poll3(rs, jvo, need);
      }
      BAR();
      v4u lt;
      if constexpr (layer == 0) { lt = *(LAS v4u*)(L + opqv(XS_OFF)); sd = 0; }
      const u32 pbase = KVP + (layer * 64 + 4 * hp + w) * 16384, zbase = ZP + (layer * 16 + hp) * 16384 + w * 2048;
      v4u wq[4];
      auto qfrag = [&](int ks) { return WQ[ks * 64]; };
      {
        f32x4 kv = {0.f, 0.f, 0.f, 0.f}, zz = {0.f, 0.f, 0.f, 0.f};
        const v4u ones = {ONES2, ONES2, ONES2, ONES2};
        v2u a0[4], a1[4], b0[4], b1[4];
#pragma unroll
        for (int ks = 0; ks < 4; ++ks) {
          a0[ks] = trread(a_pa + (32 * ks) * PK_STR); a1[ks] = trread(a_pa + (32 * ks + 4) * PK_STR);
          b0[ks] = trread(a_pb + (32 * ks) * VV_STR); b1[ks] = trread(a_pb + (32 * ks + 4) * VV_STR);
          SB();
        }
#pragma unroll
        for (int ks = 0; ks < 4; ++ks) {
          const v4u A = {a0[ks].x, a0[ks].y, a1[ks].x, a1[ks].y}, B = {b0[ks].x, b0[ks].y, b1[ks].x, b1[ks].y};
          kv = mfma16(A, B, kv);
          zz = mfma16(ones, A, zz);
          asm volatile("" : "+a"(zz));
          SB();
        }
#pragma unroll
        for (int ks = 0; ks < 4; ++ks) wq[ks] = qfrag(ks);
        SB();
        const v2u kvb = {pk(kv[0], kv[1]), pk(kv[2], kv[3])};
        st64<SC1>(kvb, rs, o_kv, pbase + rt * 512 + sd);
        st64<SC1>(kvb, rs, o_kv, pbase + rt * 512 + (sd ^ 8192));
        const float z0 = zz[0];
        __builtin_amdgcn_raw_buffer_store_b32(__builtin_bit_cast(u32, z0), rs, o_z, zbase + rt * 128, SC1);
      }
      SB();
      auto qstep = [&](auto I) {
        constexpr int i = I;
        vmfma<false, false>(aQ, wq[i & 3], xf[i]);
        SB();
        if constexpr (i + 4 < 32) wq[i & 3] = qfrag(i + 4);
      };
      LAS unsigned char *a_kz, *a_kw, *a_kr;
      u32 o_st, o_zl, l2k;
      v4u sel;
      if constexpr (layer == 0) {
        const int df = __builtin_amdgcn_readfirstlane((int)(lt[0] + lt[2]) - (int)(lt[1] + lt[3]));
        sd = df > 0 ? 8192 : 0;
        const u32 vote = df == 0 ? 1 : (((df > 0) ^ (xcd >> 2)) & 1) * 2;
        (void)__builtin_amdgcn_raw_ptr_buffer_atomic_add_i32(1 + (vote << 16), rs, (w == 0 && j < 4 && ln < 16) ? ln * CSTR : 0x80000000u, cnt_off(C_V), 0);
        dma1<0>(rs, l16, CS_W1 + fb * 4096 + w * 1024, L + XS_OFF + 1024 + w * 1024);
        SB();
      }
      const u32 skv = cnt_off(C_KV + layer * 128 + hp * 4 + w), ckv = skv + sd;
      u32 pvk[2];
      static_for<32>([&](auto I) {
        constexpr int i2 = I - QS, n2 = 32 - QS;
        if constexpr (I == QS) { wsignal<NQ1 + (layer ? 0 : 2)>(rs, l2k, skv); }
        qstep(I);
        if constexpr (I < QS) { if constexpr ((I * NQ1 + QS - 1) / QS != ((I + 1) * NQ1 + QS - 1) / QS) pf(IC(s_q1 + (I * NQ1 + QS - 1) / QS)); }
        else {
          if constexpr (layer == 0 && i2 == VPK) { vv = peek(rs, w == 0 ? l0 : 0x80000000u, cnt_off(C_V + (b & 15))); SB(); }
          if constexpr (i2 == KVPK1 || i2 == KVPK2) apeek(rs, l0, ckv, pvk[i2 == KVPK2]);
          if constexpr ((i2 * NQ2 + n2 - 1) / n2 != ((i2 + 1) * NQ2 + n2 - 1) / n2) pf(IC(s_q2 + (i2 * NQ2 + n2 - 1) / n2));
        }
        const u32 l2 = opqv(l16) >> 4, g2 = l2 >> 4, c2 = l2 & 15;
        if constexpr (I == 2) l2k = opqv(l2 < 2 ? l2 * 8192 : 0x80000000u);
        if constexpr (I == 1) o_zl = opqv((l2 >> 2) * 128 + (l2 & 3) * 16);
        if constexpr (I == 3) a_kz = L + opqv(KF_OFF + w * 544 + ((l2 >> 1) & 1) * 272 + 256 + (l2 & 1) * 8 + g2 * 2);
        if constexpr (I == 5) a_kw = L + opqv(KF_OFF + w * 544 + (g2 >> 1) * 272 + (g2 & 1) * 8 + rho32(c2 & 7, c2 >> 3) * 16);
        if constexpr (I == 7) a_kr = L + opqv(KF_OFF + (l2 >> 5) * 272 + ((l2 & 31) < 16 ? (l2 & 31) : 16) * 16);
        if constexpr (I == 9) o_st = opqv(l2 >= 32 ? 0x80000000u : c2 * 16 + g2 * 8);
        if constexpr (I == 11 || I == 13 || I == 15 || I == 17) {
          constexpr int m = (I - 11) / 2;
          sel[m] = opqv((c2 >> 1) == 4 * (g2 & 1) + m ? 0x3f80u << (16 * (c2 & 1)) : 0u);
        }
        SB();
      });
      settle<12>(aQ);
      const v4u q0 = pack8(aQ, 0), q1 = pack8(aQ, 1);
      f32x16 pq;
      vmfma<true, true, true>(pq, wph[0], q0);
      vmfma<false, true, true>(pq, wph[1], q1);
      settle<12>(pq);
      SB();
      v4u phq[4];
      v4u v[8];
      f32x4 z;
      {
        u32 o[16];
        dual_softmax(pq, c, o, [&](auto I) {
          constexpr int i = I;
          if constexpr (i == KVCHK) {
            pfinish2<1 + NQ2 - (KVPK1 * NQ2 + 31 - QS) / (32 - QS) + (!layer && VPK > KVPK1), NQ2 - (KVPK2 * NQ2 + 31 - QS) / (32 - QS) + (!layer && VPK > KVPK2)>(rs, l0, ckv, 16, pvk);
            z = __builtin_bit_cast(f32x4, ld128<SC1>(rs, o_zl, zbase + sd));
            SB();
          }
          static_for<8>([&](auto K) {
            constexpr int k = K, at = KVCHK + (k + 1) / KVLD;
            if constexpr ((at < 9 ? at : 9) == i) { v[k] = ldf<SC1>(rs, l16, pbase + (sd ^ 8192), k * 1024); SB(); }
          });
        });
#pragma unroll
        for (int m = 0; m < 4; ++m) {
          auto sw = __builtin_amdgcn_permlane32_swap(o[m], o[4 + m], false, false);
          phq[0][m] = sw[0];
          phq[2][m] = sw[1];
          auto sx = __builtin_amdgcn_permlane32_swap(o[8 + m], o[12 + m], false, false);
          phq[1][m] = sx[0];
          phq[3][m] = sx[1];
        }
#pragma unroll
        for (int m = 0; m < 4; ++m) PIN(phq[m]);
      }
      SB();
      {
        const float z0 = z[0], z1 = z[1], z2 = z[2], z3 = z[3];
        const float zs = dpp_add<0x128>(dpp_add<0x124>(mix32(mix16(z0, z1), mix16(z2, z3))));
        *(LAS unsigned short*)(a_kz) = (unsigned short)pk(zs, zs);
        pretire(pvk);
        SB();
        f32x4 sv = {0.f, 0.f, 0.f, 0.f};
#pragma unroll
        for (int i = 0; i < 8; ++i) { sv = mfma16(sel, v[i], sv); SB(); }
        *(LAS v2u*)(a_kw) = v2u{pk(sv[0], sv[1]), pk(sv[2], sv[3])};
      }
      if constexpr (layer == 0) {
        if (w == 0) {
          u32 vs = __builtin_amdgcn_readfirstlane(vv);
#pragma unroll 1
          for (u32 spins = 0; spins < (1u << 16) && (vs & 0xffffu) < 32u; ++spins) vs = __builtin_amdgcn_readfirstlane(peek(rs, l0, cnt_off(C_V + (b & 15))));
          *(LAS u32*)(L + opqv(XS_OFF + 16)) = vs;
        }
      }
      BAR();
      u32 vr = 0;
      if constexpr (layer == 0) vr = *(LAS u32*)(L + opqv(XS_OFF + 16));
      {
        v4u kvf[4];
#pragma unroll
        for (int i = 0; i < 4; ++i) kvf[i] = *(LAS v4u*)(a_kr + i * 544);
        SB();
        f32x16 nd[2];
        vmfma<true, false>(nd[0], kvf[0], phq[0]);
        vmfma<false, false>(nd[0], kvf[1], phq[1]);
        vmfma<true, false>(nd[1], kvf[2], phq[2]);
        vmfma<false, false>(nd[1], kvf[3], phq[3]);
        settle<SEN>(nd[0]);
        SB();
        if constexpr (layer == 0) {
          sd = opq(((((u32)__builtin_amdgcn_readfirstlane(vr) >> 16 > 32u ? 1u : 0u) ^ (xcd >> 2)) & 1u) * 8192);
        }
#pragma unroll
        for (int h = 0; h < 2; ++h) {
          if (h) asm volatile("" : "+v"(nd[1]));
          const float inv = __builtin_amdgcn_rcpf(nd[h][8] + 1e-6f);
          v4u ob;
#pragma unroll
          for (int m = 0; m < 4; ++m) ob[m] = pk(nd[h][2 * m] * inv, nd[h][2 * m + 1] * inv);
          st128<SC1>(ob, rs, l16 + h * 1024, OB + layer * MB2 + (tt & 31) * 65536 + blk(hp * 2048) + (sd ^ (layer * 8192)));
          SB();
        }
      }
      float c1p[4][4];
      if constexpr (layer == 0) {
        LAS float* cl = (LAS float*)(L + opqv(XS_OFF + 1024 + (l16 >> 2)));
#pragma unroll
        for (int i = 0; i < 16; ++i) c1p[i >> 2][i & 3] = cl[i * 64];
        SB();
      }
      static_for<ODR>([&](auto I) { pf(IC(s_od + I)); });
      wsignal<ODR>(rs, l0, cnt_off(C_O + layer * 128 + ((tt >> 1) & 15) * 4 + (hp >> 2)) + sd);
      const u32 co = cnt_off(C_O + layer * 128 + (rt2 & 15) * 4 + w) + sd;
      u32 pvz[2];
      BAR();
      {
        NAP(PS4);
        apeek(rs, l0, co, pvz[0]);
        static_for<n_op>([&](auto I) { pf(IC(s_op + I)); });
        if constexpr (layer == 0) {
#pragma unroll
          for (int t = 0; t < 4; ++t) { u32 cw = cs_word((c1p[t][0] + c1p[t][1]) + (c1p[t][2] + c1p[t][3]), hf); PIN(cw); cs1[t] = cw; }
        } else NAP(PS6);
        apeek(rs, l0, co, pvz[1]);
        pfinish2<1 + n_op, 0>(rs, l0, co, 8, pvz);
      }
      const u32 ab = OB + layer * MB2 + w * 16384 + (sd ^ (layer * 8192));
      splitk_phase<FrO, DXV>(rs, wo, ab + (tte & 31) * 65536, ab + ((tte & 31) ^ 1) * 65536, L, w, l16, 1.f, res, [&](int) {}, [&]() {}, [&](auto I) {
        if constexpr (I == 1) pretire(pvz);
        if constexpr (I >= 4 && s_p2 + I - 4 < s_xd) pf(IC(s_p2 + I - 4));
      });
      resid_store(rs, res, l16, o_st, xtile(2 * layer, tte) + blk(n0 * 64), stile(2 * layer, tte) + (n0 >> 5) * 256);
      static_for<XDR>([&](auto I) { pf(IC(s_xd + I)); });
      wsignal<XDR>(rs, l0, (cnt_off(C_X1 + layer * 32 + (tte & 31)) + sd));
    }
    const u32 lh = opqv(opqv(lane) < nlh ? lane * CSTR : 0x80000000u);
    const u32 ch0 = cnt_off(C_H + layer * 512 + (rt2 & 15) * 4 + w) + sd;
    const u32 sh0 = cnt_off(C_H + layer * 512 + ((tt >> 1) & 15) * 4 + (2 * fb) / 7) + sd;
    auto htile = [&](u32 t) { return HB + layer * 4 * MB2 + (t & 31) * 262144 + (sd ^ ((t & 8) * 1024)); };
    auto xtile = [&](int i, u32 t) { return XT + i * MB2 + (t & 31) * 65536 + (sd ^ ((i & 1) ? 0 : 8192)); };
    auto stile = [&](int i, u32 t) { return ST + i * ST_SZ + blk((t & 31) * 4096) + sd; };
    const u32 hown = htile(tte) + w * 16384, hoth = hown ^ 262144;
    {
      const u32 ln = opqv(lane), l16 = ln * 16, r = ln & 31, hf = ln >> 5;
      auto w2ld = [&](auto I) {
        constexpr int i = I, k = (i >> 1) % 7;
        w2f[i >> 1][i & 1] = ld128<SC1>(rs, l16 + (k & 1) * 2048, wb + (i / 14) * 57344 + (k >> 1) * 4096 + ((i & 1) ? 1024 - wsw : wsw));
      };
      if (__builtin_expect(p3on, 1)) {
        const u32 cx = cnt_off(C_X1 + layer * 32 + (tt & 31)) + sd;
        static_for<3>([&](auto J) {
          if constexpr (J > 0) { if constexpr (layer) NAP(J == 1 ? PS3 + 1 : PS5); apeek(rs, l0, cx, pvy[J - 1]); }
          SB();
          if constexpr (layer == 0) static_for<W2D / 3>([&](auto I) { w2ld(IC(J * (W2D / 3) + I)); SB(); });
        });
        constexpr int DX = DXV;
        v4u xf[32];
        const u32 xb = xtile(2 * layer, tt);
        f32x4 sp[4];
        const u32 stb = stile(2 * layer, tt);
        LAS v4u* WL = (LAS v4u*)(L + l16);
        LAS v4u* WH = (LAS v4u*)(L + opqv(l16 + 65536));
        u32 hb[2];
#pragma unroll
        for (int s = 0; s < 2; ++s) hb[s] = opq(htile(tt) + ((2 * fb + s) / 7) * 16384 + ((2 * fb + s) % 7) * 1024);
        SB();
        asm volatile("s_waitcnt vmcnt(%0)" ::"n"((layer == 0 ? W2D : 0) + 3) : "memory");
        BAR();
        v4u wa[RA + 1], wc[RA + 1], wq[RB];
#pragma unroll
        for (int i = 0; i < RA; ++i) { wa[i] = WL[i * 64]; wc[i] = WL[(32 + i) * 64]; }
        SB();
        pfinish2<1 + (layer ? 0 : 2 * W2D / 3), (layer ? 0 : W2D / 3)>(rs, l0, cx, 16, pvy);
#pragma unroll
        for (int ks = 0; ks < DX; ++ks) { xf[ks] = ldr(rs, l16, xb, ks); SB(); }
        f32x16 acc[4];
        float nc;
        Stats zs_;
        SB();
        static_for<32>([&](auto KSI) {
          constexpr int ks = KSI;
          vmfma<ks == 0, false>(acc[0], wa[ks % (RA + 1)], xf[ks]);
          SB();
          if constexpr (ks + RA < 32) wa[(ks + RA) % (RA + 1)] = WL[(ks + RA) * 64];
          if constexpr (ks >= 32 - RB / 2) wq[2 * (ks - 32 + RB / 2)] = WH[(2 * (ks - 32 + RB / 2)) * 64];
          SB();
          if constexpr (ks + DX < 32) xf[ks + DX] = ldr(rs, l16, xb, ks + DX);
          if constexpr (layer == 0 && ks >= 20) w2ld(IC(W2D + ks - 20));
          SB();
          vmfma<ks == 0, false>(acc[1], wc[ks % (RA + 1)], xf[ks]);
          SB();
          if constexpr (ks + RA < 32) wc[(ks + RA) % (RA + 1)] = WL[(32 + ks + RA) * 64];
          if constexpr (ks >= 32 - RB / 2) wq[2 * (ks - 32 + RB / 2) + 1] = WH[(2 * (ks - 32 + RB / 2) + 1) * 64];
          SB();
          if constexpr (ks < 4) sp[ks] = __builtin_bit_cast(f32x4, ld128<SC1>(rs, l16 + ks * 1024, stb));
          if constexpr (ks == 1) pretire(pvy);
          if constexpr (ks >= 16 && ks < 24) stats_step<ks - 16>(zs_, sp);
          if constexpr (ks == 24) { nc = zs_.rstd * -LOG2E; PIN(nc); }
          SB();
          if constexpr (ks == 27) mean_fix<0>(acc[0], cs1, zs_.mw);
          if constexpr (ks == 28) mean_fix<1>(acc[1], cs1, zs_.mw);
          if constexpr (ks == 29) mean_fix<2, true>(acc[2], cs1, zs_.mw);
          SB();
        });
        float tv[64], ev[64];
        v4u ho[8];
        u32 pv0;
        auto stage = [&](auto V, auto ST_) {
          constexpr int v = V, st = ST_;
          if constexpr (st == 0) { tv[v] = acc[v >> 4][v & 15] * nc; PIN(tv[v]); }
          else if constexpr (st == 1) { ev[v] = __builtin_amdgcn_exp2f(tv[v]); PIN(ev[v]); }
          else if constexpr (st == 2) { ev[v] = 1.f + ev[v]; PIN(ev[v]); }
          else if constexpr (st == 3) { ev[v] = __builtin_amdgcn_rcpf(ev[v]); PIN(ev[v]); }
          else if constexpr (st == 4) { tv[v] = tv[v] * ev[v]; PIN(tv[v]); }
          else if constexpr (v & 1) {
            u32 hh = pk(tv[v - 1], tv[v]);
            PIN(hh);
            ho[v >> 3][(v >> 1) & 3] = hh;
            if constexpr ((v & 7) == 7) st128<SC1>(ho[v >> 3], rs, l16, hb[(v >> 3) & 1] + (v >> 4) * 65536);
          }
        };
        static_for<64>([&](auto GI) {
          constexpr int gi = GI, t = 2 + (gi >> 5), ks = gi & 31;
          if constexpr (gi == 32) mean_fix<3, true>(acc[3], cs1, zs_.mw);
          if constexpr (RB > 4 && !(gi & 1)) asm volatile("" ::"v"(wq[(gi + 1) % RB]));
          vmfma<false, false>(acc[t], wq[gi % RB], xf[ks]);
          SB();
          if constexpr (gi + RB < 64) wq[gi % RB] = WH[(gi + RB) * 64];
          if constexpr (layer == 0 && p3_w2gap(gi)) w2ld(IC(W2D + 12 + (gi * W2B) / W2S));
          SB();
          if constexpr (gi == 0) settle<SEA>(acc[0]);
          static_for<48>([&](auto V) {
            constexpr int d = gi - p3_g0(V);
            if constexpr (d >= 0 && d < 6) stage(V, IC(d));
          });
          if constexpr (gi == HS0) wsignal<p3_younger(layer, 0, HS0)>(rs, lh, sh0);
          if constexpr (gi == HS1) wsignal<p3_younger(layer, 1, HS1)>(rs, lh, sh0 + 128 * CSTR);
          if constexpr (gi == PK0) pv0 = peek(rs, l0, ch0);
          SB();
        });
        {
          f32x2 t2[8], e2[8];
          settle<12>(acc[3]);
#pragma unroll
          for (int m = 0; m < 8; ++m) { t2[m] = f32x2{acc[3][2 * m], acc[3][2 * m + 1]} * nc; PIN(t2[m]); }
          SB();
#pragma unroll
          for (int m = 0; m < 8; ++m) { e2[m] = f32x2{__builtin_amdgcn_exp2f(t2[m].x), __builtin_amdgcn_exp2f(t2[m].y)}; PIN(e2[m]); }
          SB();
#pragma unroll
          for (int m = 0; m < 8; ++m) { e2[m] = e2[m] + 1.f; PIN(e2[m]); }
          SB();
#pragma unroll
          for (int m = 0; m < 8; ++m) { e2[m] = f32x2{__builtin_amdgcn_rcpf(e2[m].x), __builtin_amdgcn_rcpf(e2[m].y)}; PIN(e2[m]); }
          SB();
#pragma unroll
          for (int m = 0; m < 8; ++m) { t2[m] = t2[m] * e2[m]; PIN(t2[m]); }
          SB();
#pragma unroll
          for (int s = 0; s < 2; ++s)
            st128<SC1>(v4u{pk(t2[4 * s].x, t2[4 * s].y), pk(t2[4 * s + 1].x, t2[4 * s + 1].y), pk(t2[4 * s + 2].x, t2[4 * s + 2].y), pk(t2[4 * s + 3].x, t2[4 * s + 3].y)},
                       rs, l16, hb[s] + 3 * 65536);
        }
        SB();
        wsignal<p3_younger(layer, 2, 64) + 2>(rs, lh, sh0 + 256 * CSTR);
        SB();
        wfinish(rs, l0, ch0, 8, pv0);
      } else {
        if constexpr (layer == 0) static_for<56>([&](auto E) { w2ld(E); });
        DRAIN();
        BAR();
        wwait(rs, l0, ch0, 8);
      }
    }
    {
      const u32 ln = opqv(lane), l16 = ln * 16, r = ln & 31, hf = ln >> 5;
      const u32 wqb = opq(WQKV_T + hp * 98304 + w * 16384);
      u32 pv1[2], pv2[2], pv3[2];
      splitk_phase<FrH, 56>(rs, w2f, hown, hoth, L, w, l16, -LN2, res,
        [&](int i) { if (i == T1A || i == T1B) apeek(rs, l0, ch0 + 128 * CSTR, pv1[i == T1B]); },
        [&]() {
          wsignal<3 + 14>(rs, lh, sh0 + 384 * CSTR);
          pfinish2<14 - T1A + 2, 14 - T1B + 1>(rs, l0, ch0 + 128 * CSTR, 8, pv1);
        },
        [&](auto I) {
          constexpr int i = I, g = i - 42;
          if constexpr (i == 15) pretire(pv1);
          constexpr auto ops = [](int a, int b) {
            int n = 0;
            for (int q = 14; q < 56; ++q) n += FrH::lstep(q, 56) >= a && FrH::lstep(q, 56) < b;
            for (int q : {PK2, PK2B, PK3, PK3B}) n += q > a && q < b;
            return n;
          };
          static_assert(PK2 < PK2B && PK2B < T2CHK && PK2B != PK3 && PK2 != PK3 && PK3 < PK3B && PK3B < T3CHK && T2CHK == 14 && T3CHK >= 21 && T3CHK <= 28, "peek order");
          if constexpr (i == T2CHK) pfinish2<ops(PK2, i), ops(PK2B, i)>(rs, l0, ch0 + 256 * CSTR, 8, pv2);
          if constexpr (i == T3CHK) { pfinish2<ops(PK3, i), ops(PK3B, i)>(rs, l0, ch0 + 384 * CSTR, 8, pv3); }
          if constexpr (i == PK2 || i == PK2B) apeek(rs, l0, ch0 + 256 * CSTR, pv2[i == PK2B]);
          if constexpr (i == PK3 || i == PK3B) apeek(rs, l0, ch0 + 384 * CSTR, pv3[i == PK3B]);
          if constexpr (i == 29) pretire(pv2);
          if constexpr (i == 36) pretire(pv3);
          if constexpr (layer == 0 && g >= 10 && g < 26) dma1<g - 10>(rs, l16, wqb, L + w * 16384);
        });
      if constexpr (layer == 0) {
        resid_store(rs, res, l16, hf ? 0x80000000u : (r & 15) * 16 + (r >> 4) * 8, xtile(1, tte) + blk(n0 * 64), stile(1, tte) + (n0 >> 5) * 256);
        wsignal<0>(rs, l0, (cnt_off(C_X + (tte & 31)) + sd));
      } else {
        const rsrc_t rso = mkrsrc(out, S * D * 2);
#pragma unroll
        for (int s = 0; s < 2; ++s) st128<0>(res[s], rso, r * 1024 + hf * 16 + s * 32, tte * 32768 + n0 * 2);
        if (b == 255 && tid < 16) st64<SC1>(v2u{tag_lo, tag_hi}, rs, tid * 8, EPOCH_OFF);
      }
    }
  });
  asm volatile(".Lkcode1:");
}

extern "C" void kernel_launch(void* const* d_in, const int* in_sizes, int n_in, void* d_out, int out_size, void* d_ws,
                              size_t ws_size, hipStream_t stream) {
  (void)hipFuncSetAttribute((const void*)k_main, hipFuncAttributeMaxDynamicSharedMemorySize, LDS_BYTES);
  k_main<<<256, 256, LDS_BYTES, stream>>>((const uint16_t*)d_in[0], (const uint16_t*)d_in[1], (const uint16_t*)d_in[2],
                                          (const uint16_t*)d_in[3], (const uint16_t*)d_in[5], (const uint16_t*)d_in[6],
                                          (unsigned char*)d_ws, (unsigned char*)d_out);
}
```
